```python
import torch

def _softmax(x, axis=-1):
    return torch.softmax(x, dim=axis)
def _gelu(x):
    return torch.nn.functional.gelu(x, approximate="tanh")
def _silu(x):
    return torch.nn.functional.silu(x)
def _sigmoid(x):
    return torch.sigmoid(x)
def _layernorm(x, axis=-1, eps=1e-5):
    m = x.mean(dim=axis, keepdim=True)
    v = x.var(dim=axis, keepdim=True, unbiased=False)
    return (x - m) / torch.sqrt(v + eps)
def _scatter_add(tgt, idx, src, axis=0):
    return tgt.clone().index_add_(axis, idx, src)
def _segment_sum(src, seg, n):
    out = torch.zeros((n,) + src.shape[1:], dtype=src.dtype)
    return out.index_add_(0, seg, src)
def _pad(x, pw):
    flat = tuple(v for pair in reversed(pw) for v in pair)
    return torch.nn.functional.pad(x, flat)

kernel_name = 'parallel_sum__linear_attn_block__glu_gated_glu__prompt'

def setup_inputs():

    _w = (lambda a: (lambda t: t.to(torch.bfloat16) if t.is_floating_point() else t)(torch.as_tensor(a)))
    def _A_setup():
        B, S, D = 1, 2048, 640
        return {
            "x": _w((lambda *s: torch.randn(*s, dtype=torch.bfloat16) * 0.05)(B, S, D)),
            "wqkv": _w((lambda *s: torch.randn(*s, dtype=torch.bfloat16) * 0.2)(D, 3 * D)),
            "wo": _w((lambda *s: torch.randn(*s, dtype=torch.bfloat16) * 0.2)(D, D)),
        }
    _A_in = _A_setup()

    def _B_setup():
        B, S, D, F = 1, 2048, 640, 1728
        return {
            "x": _w((lambda *s: torch.randn(*s, dtype=torch.bfloat16) * 0.05)(B, S, D)),
            "w_gate": _w((lambda *s: torch.randn(*s, dtype=torch.bfloat16) * 0.05)(D, F)),
            "w_up": _w((lambda *s: torch.randn(*s, dtype=torch.bfloat16) * 0.05)(D, F)),
            "w_down": _w((lambda *s: torch.randn(*s, dtype=torch.bfloat16) * 0.05)(F, D)),
        }
    _B_in = _B_setup()

    return {
        "A_x": _A_in["x"],
        "A_wqkv": _A_in["wqkv"],
        "A_wo": _A_in["wo"],
        "B_x": _B_in["x"],
        "B_w_gate": _B_in["w_gate"],
        "B_w_up": _B_in["w_up"],
        "B_w_down": _B_in["w_down"],
    }

def reference(A_x, A_wqkv, A_wo, B_x, B_w_gate, B_w_up, B_w_down):
    def _A_ref(x, wqkv, wo):
        B, S, D = x.shape
        H = 32; Dh = D // H
        qkv = (lambda x, *d: x.permute(*d))((x @ wqkv).reshape(B, S, 3, H, Dh), 2, 0, 3, 1, 4)
        q, k, v = qkv[0], qkv[1], qkv[2]
        phi = lambda t: torch.where(t > 0, t + 1, torch.exp(t)) + 1e-6
        pq, pk = phi(q), phi(k)
        kv = torch.einsum("bhsd,bhse->bhde", pk, v)
        z = torch.einsum("bhsd->bhd", pk)
        num = torch.einsum("bhqd,bhde->bhqe", pq, kv)
        den = torch.einsum("bhqd,bhd->bhq", pq, z)[..., None] + 1e-6
        o = num / den
        return (lambda x, *d: x.permute(*d))(o, 0, 2, 1, 3).reshape(B, S, D) @ wo

    def _B_ref(x, w_gate, w_up, w_down):
        g = torch.einsum("bsd,df->bsf", x, w_gate)
        u = torch.einsum("bsd,df->bsf", x, w_up)
        h = _sigmoid(g) * u
        return torch.einsum("bsf,fd->bsd", h, w_down)

    x = A_x
    xn = _layernorm(x, axis=-1)
    return x + _A_ref(xn, A_wqkv, A_wo) + _B_ref(xn, B_w_gate, B_w_up, B_w_down)
```

```cpp
#include <hip/hip_runtime.h>
#include <hip/hip_bf16.h>
#include <stdint.h>
namespace nv {
typedef unsigned short u16;
__device__ __forceinline__ float b2f(u16 v) { return __uint_as_float(((unsigned)v) << 16); }
__device__ __forceinline__ u16 f2b(float f) { unsigned u = __float_as_uint(f); u += 0x7fffu + ((u >> 16) & 1u); return (u16)(u >> 16); }
__device__ __forceinline__ float rb(float f) { return b2f(f2b(f)); }
constexpr int S = 2048, D = 640, F = 1728, H = 32, DH = 20;

__global__ void k_ln(const u16* x, u16* xn) {
  int r = blockIdx.x; int t = threadIdx.x;
  float s = 0.f;
  for (int i = t; i < D; i += 64) s += b2f(x[r * D + i]);
  for (int o = 32; o; o >>= 1) s += __shfl_xor(s, o);
  float m = s / D;
  float v = 0.f;
  for (int i = t; i < D; i += 64) { float d = b2f(x[r * D + i]) - m; v += d * d; }
  for (int o = 32; o; o >>= 1) v += __shfl_xor(v, o);
  v = rb(v / D); m = rb(m);
  float sd = rb(sqrtf(rb(v + 1e-5f)));
  for (int i = t; i < D; i += 64) xn[r * D + i] = f2b(rb(b2f(x[r * D + i]) - m) / sd);
}
__global__ void k_gemm(const u16* A, const u16* B, u16* C, int M, int N, int K) {
  int n = blockIdx.x * 64 + threadIdx.x; int m0 = blockIdx.y * 8;
  if (n >= N) return;
  float acc[8] = {0,0,0,0,0,0,0,0};
  for (int k = 0; k < K; ++k) { float b = b2f(B[(size_t)k * N + n]);
    for (int i = 0; i < 8; ++i) acc[i] += b2f(A[(size_t)(m0 + i) * K + k]) * b; }
  for (int i = 0; i < 8; ++i) C[(size_t)(m0 + i) * N + n] = f2b(acc[i]);
}
__device__ __forceinline__ float phi(float t) { float r = t > 0.f ? rb(t + 1.f) : rb(expf(t)); return rb(r + 1e-6f); }
__global__ void k_kv(const u16* qkv, u16* kv, u16* z) {
  int h = blockIdx.x, t = threadIdx.x;
  if (t < 400) { int d = t / 20, e = t % 20; float a = 0.f;
    for (int s = 0; s < S; ++s) a += phi(b2f(qkv[s * 1920 + 640 + h * 20 + d])) * b2f(qkv[s * 1920 + 1280 + h * 20 + e]);
    kv[h * 400 + t] = f2b(a);
  } else if (t < 420) { int d = t - 400; float a = 0.f;
    for (int s = 0; s < S; ++s) a += phi(b2f(qkv[s * 1920 + 640 + h * 20 + d]));
    z[h * 20 + d] = f2b(a);
  }
}
__global__ void k_o(const u16* qkv, const u16* kv, const u16* z, u16* o) {
  int s = blockIdx.x, c = threadIdx.x, h = c / 20, e = c % 20;
  float num = 0.f, den = 0.f;
  for (int d = 0; d < 20; ++d) { float p = phi(b2f(qkv[s * 1920 + h * 20 + d])); num += p * b2f(kv[h * 400 + d * 20 + e]); den += p * b2f(z[h * 20 + d]); }
  num = rb(num); den = rb(rb(den) + 1e-6f);
  o[s * D + c] = f2b(num / den);
}
__global__ void k_h(const u16* g, const u16* u, u16* h, int n) { int i = blockIdx.x * 256 + threadIdx.x; if (i < n) {
  float gg = b2f(g[i]); float sg = rb(1.f / (1.f + expf(-gg))); h[i] = f2b(sg * b2f(u[i])); } }
__global__ void k_fin(const u16* x, const u16* a, const u16* b, u16* out, int n) { int i = blockIdx.x * 256 + threadIdx.x; if (i < n)
  out[i] = f2b(rb(b2f(x[i]) + b2f(a[i])) + b2f(b[i])); }

static void launch(void* const* d_in, void* d_out, void* d_ws, hipStream_t st) {
  const u16* x = (const u16*)d_in[0]; const u16* wqkv = (const u16*)d_in[1]; const u16* wo = (const u16*)d_in[2];
  const u16* wg = (const u16*)d_in[4]; const u16* wu = (const u16*)d_in[5]; const u16* wd = (const u16*)d_in[6];
  u16* ws = (u16*)d_ws;
  u16* xn = ws; u16* qkv = xn + S * D; u16* g = qkv + S * 1920; u16* u = g + S * F; u16* h = u + S * F;
  u16* o = h + S * F; u16* a = o + S * D; u16* b = a + S * D; u16* kv = b + S * D; u16* z = kv + H * 400;
  k_ln<<<S, 64, 0, st>>>(x, xn);
  k_gemm<<<dim3(1920 / 64, S / 8), 64, 0, st>>>(xn, wqkv, qkv, S, 1920, D);
  k_gemm<<<dim3(F / 64, S / 8), 64, 0, st>>>(xn, wg, g, S, F, D);
  k_gemm<<<dim3(F / 64, S / 8), 64, 0, st>>>(xn, wu, u, S, F, D);
  k_kv<<<H, 448, 0, st>>>(qkv, kv, z);
  k_o<<<S, 640, 0, st>>>(qkv, kv, z, o);
  k_h<<<(S * F + 255) / 256, 256, 0, st>>>(g, u, h, S * F);
  k_gemm<<<dim3(D / 64, S / 8), 64, 0, st>>>(o, wo, a, S, D, D);
  k_gemm<<<dim3(D / 64, S / 8), 64, 0, st>>>(h, wd, b, S, D, F);
  k_fin<<<(S * D + 255) / 256, 256, 0, st>>>(x, a, b, (u16*)d_out, S * D);
}
}
extern "C" void kernel_launch(void* const* d_in, const int* in_sizes, int n_in, void* d_out, int out_size, void* d_ws, size_t ws_size, hipStream_t stream) {
  nv::launch(d_in, d_out, d_ws, stream);
}
```

```cpp
#include <hip/hip_runtime.h>
#include <stdint.h>

#define ABL_NOREAD 0
#define ABL_NODMA 0
#define ABL_NODOT 0
#define ABL_NOBAR 0
typedef unsigned short u16;
typedef unsigned int u32;
typedef __bf16 bf16x8 __attribute__((ext_vector_type(8)));
typedef __bf16 bf16x2 __attribute__((ext_vector_type(2)));
typedef short s16x4 __attribute__((ext_vector_type(4)));
typedef float f32x2 __attribute__((ext_vector_type(2)));
typedef float f32x4 __attribute__((ext_vector_type(4)));
typedef float f32x16 __attribute__((ext_vector_type(16)));
typedef u32 u32x2 __attribute__((ext_vector_type(2)));
typedef u32 u32x4 __attribute__((ext_vector_type(4)));
#define LAS __attribute__((address_space(3)))
#define GAS __attribute__((address_space(1)))
#define DI __device__ __forceinline__

constexpr int S_ = 2048, D_ = 640, F_ = 1728;
constexpr int NSTEP = 74;
constexpr u32 ACT_OFF = 0;
constexpr u32 WT_OFF  = ACT_OFF + 128u * NSTEP * 1024u;
constexpr u32 WOT_OFF = WT_OFF + 40u * NSTEP * 1024u;
constexpr u32 KVP_OFF = WOT_OFF + 32u * 40u * 1024u;
constexpr u32 ZB_OFF  = KVP_OFF + 32u * 8u * 20u * 96u;
constexpr u32 CNT_OFF = ZB_OFF + 8u * 1280u;
constexpr u32 WS_END  = CNT_OFF + 32u * 256u;
constexpr u32 OOB = 0x7ffffff0u;
#define STAMP(ws, kern, i) do {} while (0)

constexpr int K1_WRING = 0, K1_XRING = 61440, K1_LDS = 98304 + 61440;
constexpr int K2_LDS = 81920 + 4 * 15360;

DI float bf_lo(u32 v) { return __uint_as_float(v << 16); }
DI float bf_hi(u32 v) { return __uint_as_float(v & 0xffff0000u); }
DI u32 pk_bf16(float a, float b) { f32x2 v = {a, b}; return __builtin_bit_cast(u32, __builtin_convertvector(v, bf16x2)); }
DI float round_bf(float a) { return bf_lo(pk_bf16(a, a) & 0xffffu); }
DI float dot2(u32 a, u32 b, float c) { return __builtin_amdgcn_fdot2_f32_bf16(__builtin_bit_cast(bf16x2, a), __builtin_bit_cast(bf16x2, b), c, false); }
DI float swap_add(float v) { auto r = __builtin_amdgcn_permlane32_swap(__float_as_uint(v), __float_as_uint(v), false, false); return __uint_as_float(r[0]) + __uint_as_float(r[1]); }
DI f32x16 mfma32(u32x4 a, u32x4 b, f32x16 c) { return __builtin_amdgcn_mfma_f32_32x32x16_bf16(__builtin_bit_cast(bf16x8, a), __builtin_bit_cast(bf16x8, b), c, 0, 0, 0); }
DI f32x4 mfma16(u32x4 a, u32x4 b, f32x4 c) { return __builtin_amdgcn_mfma_f32_16x16x32_bf16(__builtin_bit_cast(bf16x8, a), __builtin_bit_cast(bf16x8, b), c, 0, 0, 0); }
DI u32x2 tr_read(LAS char* p) { return __builtin_bit_cast(u32x2, __builtin_amdgcn_ds_read_tr16_b64_v4i16((LAS s16x4*)p)); }
DI void dma16(const char* sbase, u32 voff, u32 ldst) {
  asm volatile("s_mov_b32 m0, %2\n\ts_nop 0\n\tglobal_load_lds_dwordx4 %0, %1" ::"v"(voff), "s"(sbase), "s"(ldst) : "memory");
}
template <int N> DI void wait_vm_lgkm0() { __builtin_amdgcn_s_waitcnt(0xC07F); asm volatile("s_waitcnt vmcnt(%0)" ::"n"(N) : "memory"); }
#define SB0 __builtin_amdgcn_sched_barrier(0)
DI void bar() { __builtin_amdgcn_s_barrier(); }
constexpr float LOG2E = 1.4426950408889634f;

struct Sched { int n; int op[80]; int first[22]; };
constexpr Sched make_sched() {
  Sched r{}; int n = 0;
  r.first[0] = 0;
  for (int k = 0; k < 4; ++k) r.op[n++] = 100 + k;
  r.op[n++] = 0; r.op[n++] = 1; r.op[n++] = 2;
  for (int k = 0; k < 4; ++k) r.op[n++] = 104 + k;
  r.op[n++] = 3; r.op[n++] = 4; r.op[n++] = 5;
  for (int k = 0; k < 4; ++k) r.op[n++] = 108 + k;
  r.op[n++] = 6;
  for (int s = 0; s < 20; ++s) {
    r.first[s + 1] = n;
    if ((s & 1) == 0) { int g = 7 + 3 * (s / 2); if (g < 30) r.op[n++] = g; if (g + 1 < 30) r.op[n++] = g + 1;
      if (s >= 2) { int i = (s - 2) / 2 + 3; if (i < 10) { r.op[n++] = 100 + 4 * i + 2; r.op[n++] = 100 + 4 * i + 3; } }
    } else { int g = 9 + 3 * ((s - 1) / 2); if (g < 30) r.op[n++] = g;
      int i = (s - 1) / 2 + 3; if (i < 10) { r.op[n++] = 100 + 4 * i; r.op[n++] = 100 + 4 * i + 1; } }
  }
  r.first[21] = n; r.n = n;
  return r;
}
constexpr Sched SCH = make_sched();
constexpr int vm_need(int t) {
  int issued = SCH.first[t < 1 ? 1 : t];
  int last = -1;
  for (int k = 0; k < issued; ++k) {
    int op = SCH.op[k];
    bool need = op < 100 ? (8 * op <= 12 * t + 11) : ((op - 100) / 4 <= t / 2);
    if (need) last = k;
  }
  return issued - 1 - last;
}

struct K1Ctx {
  const char* xg; const char* wb[3]; u32 wvo[3]; u32 xvo; u32 wperiod; int w; u32 lb;
};

template <int OP> DI void k1_issue(const K1Ctx& c, LAS char* lds) {
  if constexpr (OP >= 100) {
    constexpr int i = (OP - 100) / 4, xi = (OP - 100) % 4;
    dma16(c.xg + (xi * 64 * 1280 + i * 128), c.xvo, c.lb + K1_XRING + (i % 3) * 32768 + xi * 8192 + c.w * 1024);
  } else {
    constexpr int m = OP / 3, i = OP % 3, pos = (8 * OP) % 60;
    int p = pos + c.w; if (pos + 7 >= 60) p = p >= 60 ? p - 60 : p;
    dma16(c.wb[i] + (size_t)(m * c.wperiod), c.wvo[i], c.lb + K1_WRING + p * 1024);
  }
}
template <int A, int B> DI void k1_issue_range(const K1Ctx& c, LAS char* lds) {
  if constexpr (A < B) { k1_issue<SCH.op[A]>(c, lds); k1_issue_range<A + 1, B>(c, lds); }
}

template <bool KV> struct K1Frag { u32x4 x[2][2]; u32x4 w[3][2]; };

template <bool KV, int T, int U> DI void k1_read1(K1Frag<KV>& f, LAS char* lds, const u32 (&xa)[2][4], const u32 (&wa)[3][2]) {
  constexpr int xs = K1_XRING + ((T / 2) % 3) * 32768, ws = K1_WRING + (T % 5) * 12288;
  if constexpr (U < 6) {
    constexpr int b = U % 3, k = U / 3;
    LAS char* p = lds + ws + (KV ? wa[b][k] : wa[b][0] + k * 3072);
    u32x2 lo = tr_read(p), hi = tr_read(p + 768);
    f.w[b][k] = u32x4{lo[0], lo[1], hi[0], hi[1]};
  } else {
    constexpr int t = U & 1, k = (U - 6) / 2;
    f.x[t][k] = *(LAS u32x4*)(lds + xs + xa[t][2 * (T & 1) + k]);
  }
}
template <bool KV, int T, int U> DI void k1_read_all(K1Frag<KV>& f, LAS char* lds, const u32 (&xa)[2][4], const u32 (&wa)[3][2]) {
  if constexpr (U < 10) { k1_read1<KV, T, U>(f, lds, xa, wa); k1_read_all<KV, T, U + 1>(f, lds, xa, wa); }
}
constexpr int RD_ORDER[10] = {0, 6, 7, 1, 2, 3, 8, 9, 4, 5};

template <bool KV, int T, int I> DI void k1_slot(f32x16 (&acc)[3][2], K1Frag<KV>& cur, K1Frag<KV>& nxt, float& s1, float& s2, float (&cs)[3],
                                                 const K1Ctx& c, LAS char* lds, const u32 (&xa)[2][4], const u32 (&wa)[3][2]) {
  if constexpr (I < 12) {
    constexpr int k = I / 6, b = (I % 6) / 2, t = I & 1;
    acc[b][t] = KV ? mfma32(cur.x[t][k], cur.w[b][k], acc[b][t]) : mfma32(cur.w[b][k], cur.x[t][k], acc[b][t]);
    if constexpr (!ABL_NOREAD && T + 1 < 20 && I < 5) { k1_read1<KV, T + 1, RD_ORDER[2 * I]>(nxt, lds, xa, wa); k1_read1<KV, T + 1, RD_ORDER[2 * I + 1]>(nxt, lds, xa, wa); }
    if constexpr (!ABL_NODMA && I >= 5 && I < 9 && SCH.first[T + 1] + (I - 5) < SCH.first[T + 2]) k1_issue<SCH.op[SCH.first[T + 1] + (I - 5)]>(c, lds);
    if constexpr (!ABL_NODOT && I >= 1 && I < 9) {
      constexpr int kk = (I - 1) / 4, jj = (I - 1) % 4;
      s1 = dot2(cur.x[0][kk][jj], 0x3f803f80u, s1); s2 = dot2(cur.x[0][kk][jj], cur.x[0][kk][jj], s2);
      asm volatile("" : "+v"(s1), "+v"(s2));
    }
    if constexpr (ABL_NODOT) {} else if constexpr (KV) { cs[I / 4] = dot2(cur.w[I / 4][0][I % 4], 0x3f803f80u, cs[I / 4]); asm volatile("" : "+v"(cs[I / 4])); }
    else if constexpr (I >= 4) { cs[0] = dot2(cur.w[0][(I - 4) / 4][I % 4], 0x3f803f80u, cs[0]); asm volatile("" : "+v"(cs[0])); }
    SB0;
    k1_slot<KV, T, I + 1>(acc, cur, nxt, s1, s2, cs, c, lds, xa, wa);
  }
}

template <bool KV, int T> DI void k1_steps(f32x16 (&acc)[3][2], K1Frag<KV>& cur, K1Frag<KV>& nxt, float& s1, float& s2, float (&cs)[3],
                                           const K1Ctx& c, LAS char* lds, const u32 (&xa)[2][4], const u32 (&wa)[3][2]) {
  if constexpr (T < 20) {
    wait_vm_lgkm0<ABL_NODMA ? 0 : vm_need(T + 1 < 20 ? T + 1 : 19)>();
    if constexpr (!ABL_NOBAR) bar();
    SB0;
    k1_slot<KV, T, 0>(acc, cur, nxt, s1, s2, cs, c, lds, xa, wa);
    if constexpr (ABL_NOREAD) k1_steps<KV, T + 1>(acc, cur, nxt, s1, s2, cs, c, lds, xa, wa); else
    k1_steps<KV, T + 1>(acc, nxt, cur, s1, s2, cs, c, lds, xa, wa);
  }
}

DI float phi_f(float t) { return fmaxf(t, 0.f) + 1e-6f + __builtin_amdgcn_exp2f(fminf(t, 0.f) * LOG2E); }

template <bool KV> DI void k1_tile(int type, int rt, int j, const u16* x, const u16* wqkv, const u16* wg, const u16* wu, char* ws, LAS char* lds) {
  const int tid = threadIdx.x, l = tid & 63, w = __builtin_amdgcn_readfirstlane(tid >> 6);
  const int tg = w & 3, cgp = w >> 2, h = l >> 5;
  STAMP(ws, 0, 0);
  K1Ctx c; c.w = w; c.lb = (u32)(size_t)lds;
  {
    const char *b0, *b1; u32 stride; int lim0, lim1;
    if (type == 0) { b0 = (const char*)(wg + 96 * j); b1 = (const char*)(wu + 96 * j); stride = 3456; lim0 = 12; lim1 = 12; }
    else if (type == 1) { b0 = (const char*)(wqkv + 160 * j); b1 = b0 + 192; stride = 3840; lim0 = 12; lim1 = 8; }
    else { b0 = (const char*)(wqkv + 640 + 80 * j); b1 = b0 + 1280; stride = 3840; lim0 = 10; lim1 = 10; }
    c.wperiod = 64u * stride;
#pragma unroll
    for (int i = 0; i < 3; ++i) {
      const int pp = 8 * i + w, st = pp >= 12, pi = pp - 12 * st, part = pi >= 6;
      const int n = (pi - 6 * part) * 64 + l, row = n / 12, cp = n - 12 * row;
      int ch = part ? (cp ^ 2) : cp; if (ch >= (part ? lim1 : lim0)) ch -= 4;
      c.wb[i] = part ? b1 : b0;
      c.wvo[i] = (u32)(st * 32 + row) * stride + ch * 16;
    }
    c.xg = (const char*)x + (size_t)rt * 256 * 1280;
    c.xvo = (u32)(8 * w + (l >> 3)) * 1280 + (((l & 7) ^ ((4 * (w & 1) + (l >> 4)) & 7)) * 16);
  }
  k1_issue_range<SCH.first[0], SCH.first[1]>(c, lds);

  u32 xa[2][4], wa[3][2];
  {
    const int ksw = KV ? (tg & 1) : 0;
#pragma unroll
    for (int t = 0; t < 2; ++t) {
      const int row = 64 * tg + 32 * (t ^ cgp) + (l & 31);
#pragma unroll
      for (int k = 0; k < 4; ++k) xa[t][k] = row * 128 + 16 * ((2 * (k ^ ksw) + h) ^ ((row >> 1) & 7));
    }
    const int blk = (l >> 4) & 1, q = (l & 15) >> 2, p = l & 3, rowb = (8 * h + q) * 192;
    if constexpr (KV) {
      const int a = cgp ? 3 : 0;
      u32 b0 = rowb + (a + blk) * 32 + 8 * p;
      u32 b1 = 6144 + rowb + ((a + blk) ^ 1) * 32 + 8 * p;
      u32 b2 = (p < 2 ? rowb + (4 + cgp) * 16 : 6144 + rowb + ((4 + cgp) ^ 2) * 16) + 8 * (p & 1);
#pragma unroll
      for (int k = 0; k < 2; ++k) { const u32 o = (k ^ ksw) * 3072; wa[0][k] = b0 + o; wa[1][k] = b1 + o; wa[2][k] = b2 + o; }
    } else {
#pragma unroll
      for (int b = 0; b < 3; ++b) {
        int bp = b + tg; bp = bp >= 3 ? bp - 3 : bp; bp = bp >= 3 ? bp - 3 : bp;
        wa[b][0] = blk * 6144 + rowb + ((3 * cgp + bp) ^ blk) * 32 + 16 * (p & 1) + 8 * (p >> 1); wa[b][1] = 0;
      }
    }
  }
  f32x16 acc[3][2];
#pragma unroll
  for (int b = 0; b < 3; ++b)
#pragma unroll
    for (int t = 0; t < 2; ++t)
#pragma unroll
      for (int i = 0; i < 16; ++i) acc[b][t][i] = 0.f;
  float s1 = 0.f, s2 = 0.f, cs[3] = {0.f, 0.f, 0.f};
  K1Frag<KV> fa, fb;
  wait_vm_lgkm0<vm_need(0)>();
  bar();
  k1_read_all<KV, 0, 0>(fa, lds, xa, wa);
  SB0;
  k1_steps<KV, 0>(acc, fa, fb, s1, s2, cs, c, lds, xa, wa);

  STAMP(ws, 0, 2);
  LAS f32x2* stat = (LAS f32x2*)lds;
  LAS float* csl = (LAS float*)(lds + 2048);
  {
    s1 = swap_add(s1); s2 = swap_add(s2);
    const float mean = s1 * (1.f / 640.f);
    const float var = fmaxf(s2 * (1.f / 640.f) - mean * mean, 0.f);
    const float sd = round_bf(sqrtf(round_bf(round_bf(var) + 1e-5f)));
    const float rstd = 1.f / sd;
    if (l < 32) stat[64 * tg + 32 * cgp + l] = f32x2{rstd, round_bf(mean) * rstd};
    if constexpr (KV) {
#pragma unroll
      for (int b = 0; b < 3; ++b) { const float v = swap_add(cs[b]); if (l < 32 && tg < 2) csl[((tg * 2 + cgp) * 3 + b) * 32 + l] = v; }
    } else {
      const float v = swap_add(cs[0]);
      const int p = (l & 15) >> 2;
      if (l < 32 && tg < 3) csl[(cgp * 3 + tg) * 32 + (l & 16) + 8 * (p & 1) + 4 * (p >> 1) + (l & 3)] = v;
    }
  }
  __syncthreads();
  STAMP(ws, 0, 3);
  if constexpr (!KV) {
    f32x2 st[2];
#pragma unroll
    for (int t = 0; t < 2; ++t) st[t] = stat[64 * tg + 32 * (t ^ cgp) + (l & 31)];
#pragma unroll
    for (int b = 0; b < 3; ++b) {
      int bp = b + tg; bp = bp >= 3 ? bp - 3 : bp; bp = bp >= 3 ? bp - 3 : bp;
      float cv[16];
#pragma unroll
      for (int u = 0; u < 2; ++u)
#pragma unroll
        for (int v = 0; v < 2; ++v) {
          const f32x4 t4 = *(LAS f32x4*)(csl + (cgp * 3 + bp) * 32 + u * 16 + 8 * h + 4 * v);
#pragma unroll
          for (int e = 0; e < 4; ++e) cv[u * 8 + v * 4 + e] = t4[e];
        }
#pragma unroll
      for (int t = 0; t < 2; ++t) {
        const int tok = 256 * rt + 64 * tg + 32 * (t ^ cgp) + (l & 31);
        char* dst = ws + ACT_OFF + (size_t)(tok >> 4) * (NSTEP * 1024) + (tok & 15) * 16;
        float val[16];
#pragma unroll
        for (int i = 0; i < 16; ++i) val[i] = st[t][0] * acc[b][t][i] - st[t][1] * cv[i];
        if (type == 0) {
          u32x4 o;
#pragma unroll
          for (int i = 0; i < 4; ++i) {
            const float h0 = val[8 + 2 * i] * __builtin_amdgcn_rcpf(1.f + __builtin_amdgcn_exp2f(-LOG2E * val[2 * i]));
            const float h1 = val[9 + 2 * i] * __builtin_amdgcn_rcpf(1.f + __builtin_amdgcn_exp2f(-LOG2E * val[2 * i + 1]));
            o[i] = pk_bf16(h0, h1);
          }
          const int id = 12 * j + 6 * cgp + 2 * bp + h;
          *(u32x4*)(dst + (id >> 2) * 1024 + (id & 3) * 256) = o;
        } else {
#pragma unroll
          for (int u = 0; u < 2; ++u) {
            const int qu = 3 * cgp + bp + 6 * u;
            u32x4 o;
#pragma unroll
            for (int i = 0; i < 4; ++i) o[i] = pk_bf16(phi_f(val[8 * u + 2 * i]), phi_f(val[8 * u + 2 * i + 1]));
            const int ch = 2 * qu + h, g4 = ch / 5, s = ch - 5 * g4;
            if (qu < 10) *(u32x4*)(dst + (54 + 5 * j + s) * 1024 + g4 * 256) = o;
          }
        }
      }
    }
  } else {
    float cv[3];
#pragma unroll
    for (int b = 0; b < 3; ++b) cv[b] = csl[(cgp * 3 + b) * 32 + (l & 31)] + csl[((2 + cgp) * 3 + b) * 32 + (l & 31)];
    u32x4 xp[3][2][2];
    const int n = l & 31;
#pragma unroll
    for (int t = 0; t < 2; ++t) {
      f32x2 st[16];
#pragma unroll
      for (int a = 0; a < 4; ++a) {
        const LAS f32x4* sp = (const LAS f32x4*)(stat + 64 * tg + 32 * (t ^ cgp) + 8 * a + 4 * h);
        const f32x4 u0 = sp[0], u1 = sp[1];
        st[4 * a] = f32x2{u0[0], u0[1]}; st[4 * a + 1] = f32x2{u0[2], u0[3]}; st[4 * a + 2] = f32x2{u1[0], u1[1]}; st[4 * a + 3] = f32x2{u1[2], u1[3]};
      }
#pragma unroll
      for (int b = 0; b < 3; ++b) {
        float val[16];
#pragma unroll
        for (int i = 0; i < 16; ++i) {
          float v = st[i][0] * acc[b][t][i] - st[i][1] * cv[b];
          if (b == 0) v = phi_f(v);
          if (b == 2) { const float pv = phi_f(v); v = n < 8 ? pv : (n < 16 ? v : 1.f); }
          val[i] = v;
        }
#pragma unroll
        for (int k = 0; k < 2; ++k)
#pragma unroll
          for (int i = 0; i < 4; ++i) xp[b][t][k][i] = pk_bf16(val[8 * k + 2 * i], val[8 * k + 2 * i + 1]);
      }
    }
    f32x16 p01, p20, p21, p22;
#pragma unroll
    for (int i = 0; i < 16; ++i) { p01[i] = 0.f; p20[i] = 0.f; p21[i] = 0.f; p22[i] = 0.f; }
#pragma unroll
    for (int t = 0; t < 2; ++t)
#pragma unroll
      for (int k = 0; k < 2; ++k) {
        p01 = mfma32(xp[0][t][k], xp[1][t][k], p01);
        p20 = mfma32(xp[2][t][k], xp[0][t][k], p20);
        p21 = mfma32(xp[2][t][k], xp[1][t][k], p21);
        p22 = mfma32(xp[2][t][k], xp[2][t][k], p22);
      }
    LAS f32x4* red = (LAS f32x4*)(lds + 4096);
#pragma unroll
    for (int qd = 0; qd < 4; ++qd) red[(w * 8 + qd) * 64 + l] = f32x4{p01[4 * qd], p01[4 * qd + 1], p01[4 * qd + 2], p01[4 * qd + 3]};
    red[(w * 8 + 4) * 64 + l] = f32x4{p20[4], p20[5], p20[6], p20[7]};
    red[(w * 8 + 5) * 64 + l] = f32x4{p20[8], p20[9], p20[10], p20[11]};
    red[(w * 8 + 6) * 64 + l] = f32x4{p21[0], p21[1], p21[2], p21[3]};
    red[(w * 8 + 7) * 64 + l] = f32x4{p22[0], p22[1], p22[2], p22[3]};
    __syncthreads();
    float* kvp = (float*)(ws + KVP_OFF);
#pragma unroll
    for (int z = 0; z < 2; ++z) {
      const int qd = 2 * tg + z;
      f32x4 sum = red[((4 * cgp + 0) * 8 + qd) * 64 + l];
#pragma unroll
      for (int g = 1; g < 4; ++g) sum += red[((4 * cgp + g) * 8 + qd) * 64 + l];
      const int lo8 = 8 * cgp, mix = 32 * (1 - cgp);
#pragma unroll
      for (int e = 0; e < 4; ++e) {
        int kc, vc; bool ok = true;
        if (qd < 4) { kc = lo8 + 8 * qd + 4 * h + e; vc = lo8 + n; }
        else if (qd == 4) { kc = lo8 + n; vc = mix + 4 * h + e; }
        else if (qd == 5) { kc = lo8 + n; vc = 64; ok = (h == 0 && e == 0); }
        else if (qd == 6) { kc = mix + 4 * h + e; vc = lo8 + n; }
        else { kc = mix + 4 * h + e; vc = n == 16 ? 64 : mix + n - 8; ok = (n >= 8 && n <= 16); }
        const int hk = kc >= 20, d = kc - 20 * hk;
        int ee; if (vc == 64) ee = 20; else { const int hv = vc >= 20; ee = vc - 20 * hv; ok = ok && (hv == hk); }
        const int head = 4 * j + 2 * cgp + hk;
        if (ok) kvp[((head * 8 + rt) * 20 + d) * 24 + ee] = sum[e];
      }
    }
  }
  STAMP(ws, 0, 4);
  STAMP(ws, 0, 5);
}

DI void k1_spare(int sg, const u16* wo, const u16* wd, char* ws, LAS char* lds) {
  const int tid = threadIdx.x, l = tid & 63, w = __builtin_amdgcn_readfirstlane(tid >> 6);
  STAMP(ws, 0, 0);
  if (sg == 0 && tid < 32) *(u32*)(ws + CNT_OFF + tid * 256) = 0u;
  const int g = l >> 4, q = (l & 15) >> 2, p = l & 3;
#pragma unroll 1
  for (int round = 0; round < 2; ++round) {
    const int nst = round == 0 ? 3 : (sg + 48 < 54 ? 1 : 0);
    for (int a = 0; a < nst; ++a) {
      const int step = sg + 16 * (3 * round + a);
      for (int i = 0; i < 5; ++i) { const int pc = w * 5 + i; dma16((const char*)wd + (size_t)step * 40960 + pc * 1024, l * 16, (u32)(size_t)lds + a * 40960 + pc * 1024); }
    }
    if (round == 1) for (int pc = w; pc < 50; pc += 8) dma16((const char*)wo + (size_t)sg * 51200 + pc * 1024, l * 16, (u32)(size_t)lds + 40960 + pc * 1024);
    asm volatile("s_waitcnt vmcnt(0)" ::: "memory");
    __syncthreads();
    for (int a = 0; a < nst; ++a) {
      const int step = sg + 16 * (3 * round + a);
      for (int i = 0; i < 5; ++i) {
        const int cb = w * 5 + i;
        LAS char* sp = lds + a * 40960 + (8 * g + q) * 1280 + cb * 32 + 8 * p;
        const u32x2 lo = tr_read(sp), hi = tr_read(sp + 4 * 1280);
        *(u32x4*)(ws + WT_OFF + (size_t)(cb * NSTEP + step) * 1024 + l * 16) = u32x4{lo[0], lo[1], hi[0], hi[1]};
      }
    }
    if (round == 1) {
      for (int hl = 0; hl < 2; ++hl)
        for (int i = 0; i < 5; ++i) {
          const int cb = w * 5 + i;
          const int r0 = min(8 * g + q, 19), r1 = min(8 * g + 4 + q, 19);
          LAS char* sp = lds + 40960 + hl * 20 * 1280 + cb * 32 + 8 * p;
          u32x2 lo = tr_read(sp + r0 * 1280), hi = tr_read(sp + r1 * 1280);
          if (g == 3) lo = u32x2{0u, 0u};
          if (g >= 2) hi = u32x2{0u, 0u};
          *(u32x4*)(ws + WOT_OFF + (size_t)((2 * sg + hl) * 40 + cb) * 1024 + l * 16) = u32x4{lo[0], lo[1], hi[0], hi[1]};
        }
    }
    __syncthreads();
    STAMP(ws, 0, 4 + round);
  }
}

__global__ __launch_bounds__(512, 2) void k1_kernel(const u16* __restrict__ x, const u16* __restrict__ wqkv, const u16* __restrict__ wo,
                                                    const u16* __restrict__ wg, const u16* __restrict__ wu, const u16* __restrict__ wd, char* ws) {
  extern __shared__ __attribute__((aligned(16))) char smem[];
  LAS char* lds = (LAS char*)smem;
  const int bid = blockIdx.x, xcd = bid & 7, idx = bid >> 3;
  const int rt = 4 * (xcd & 1) + (idx & 3), cgrp = xcd >> 1, ci = idx >> 2;
  if (cgrp >= 2 && ci == 7) { k1_spare(((cgrp - 2) * 2 + (xcd & 1)) * 4 + (idx & 3), wo, wd, ws, lds); return; }
  const int ct = cgrp < 2 ? cgrp * 8 + ci : 16 + (cgrp - 2) * 7 + ci;
  if (ct >= 18 && ct < 26) k1_tile<true>(2, rt, ct - 18, x, wqkv, wg, wu, ws, lds);
  else k1_tile<false>(ct < 18 ? 0 : 1, rt, ct < 18 ? ct : ct - 26, x, wqkv, wg, wu, ws, lds);
}

typedef __amdgpu_buffer_rsrc_t rsrc_t;
DI u32x4 bload(rsrc_t r, u32 voff, u32 soff) { return __builtin_amdgcn_raw_buffer_load_b128(r, voff, soff, 0); }
DI u32x4 bload_sc1(rsrc_t r, u32 voff, u32 soff) { return __builtin_amdgcn_raw_buffer_load_b128(r, voff, soff, 16); }

template <int T> DI void k2_hsteps(f32x4 (&acc)[5][4], u32x4 (&A)[4][4], u32x4 (&B)[4][5], rsrc_t rs, const u32 (&va)[4], const u32 (&vb)[5], bool extra) {
  if constexpr (T < 14) {
#pragma unroll
    for (int cb = 0; cb < 5; ++cb)
#pragma unroll
      for (int t = 0; t < 4; ++t) acc[cb][t] = mfma16(B[T & 3][cb], A[T & 3][t], acc[cb][t]);
    __builtin_amdgcn_sched_barrier(0);
    if constexpr (T + 4 < 14) {
      constexpr int N = T + 4;
#pragma unroll
      for (int t = 0; t < 4; ++t) A[T & 3][t] = bload(rs, N == 13 ? (extra ? va[t] : OOB) : va[t], N * 1024);
#pragma unroll
      for (int cb = 0; cb < 5; ++cb) B[T & 3][cb] = bload(rs, N == 13 ? (extra ? vb[cb] : OOB) : vb[cb], N * 1024);
      __builtin_amdgcn_sched_barrier(0);
    }
    k2_hsteps<T + 1>(acc, A, B, rs, va, vb, extra);
  }
}

__global__ __launch_bounds__(512, 2) void k2_kernel(const u16* __restrict__ x, u16* __restrict__ out, char* ws) {
  extern __shared__ __attribute__((aligned(16))) char smem[];
  LAS char* lds = (LAS char*)smem;
  const int tid = threadIdx.x, l = tid & 63, w = __builtin_amdgcn_readfirstlane(tid >> 6);
  const int bid = blockIdx.x, xcd = bid & 7, idx = bid >> 3;
  const int r = (xcd >> 1) * 8 + (idx >> 2), c = (xcd & 1) * 4 + (idx & 3);
  const rsrc_t rs = __builtin_amdgcn_make_buffer_rsrc(ws, 0, WS_END, 0x00020000);
  const int g = l >> 4, n = l & 15;
  STAMP(ws, 1, 0);
  if (w < 4) {
    const int start = w * 13 + min(w, 2);
    u32 va[4], vb[5];
#pragma unroll
    for (int t = 0; t < 4; ++t) va[t] = ACT_OFF + (u32)((4 * r + ((t + w) & 3)) * NSTEP + start) * 1024u + l * 16;
#pragma unroll
    for (int cb = 0; cb < 5; ++cb) vb[cb] = WT_OFF + (u32)((5 * c + cb) * NSTEP + start) * 1024u + l * 16;
    u32x4 A[4][4], B[4][5];
#pragma unroll
    for (int s = 0; s < 4; ++s) {
#pragma unroll
      for (int t = 0; t < 4; ++t) A[s][t] = bload(rs, va[t], s * 1024);
#pragma unroll
      for (int cb = 0; cb < 5; ++cb) B[s][cb] = bload(rs, vb[cb], s * 1024);
    }
    __builtin_amdgcn_sched_barrier(0);
    f32x4 acc[5][4];
#pragma unroll
    for (int cb = 0; cb < 5; ++cb)
#pragma unroll
      for (int t = 0; t < 4; ++t) acc[cb][t] = f32x4{0.f, 0.f, 0.f, 0.f};
    k2_hsteps<0>(acc, A, B, rs, va, vb, w < 2);
    STAMP(ws, 1, 2);
    const size_t orow = (size_t)(64 * r + 16 * w + n) * D_ + 80 * c + 4 * g;
    u32x2 xr[5];
#pragma unroll
    for (int cb = 0; cb < 5; ++cb) xr[cb] = *(const u32x2*)(x + orow + 16 * cb);
    LAS f32x4* hp = (LAS f32x4*)(lds + 81920);
#pragma unroll
    for (int cb = 0; cb < 5; ++cb)
#pragma unroll
      for (int t = 1; t < 4; ++t) hp[(w * 15 + cb * 3 + (t - 1)) * 64 + l] = acc[cb][t];
    STAMP(ws, 1, 3);
    __syncthreads();
    STAMP(ws, 1, 4);
    const LAS f32x4* ap = (const LAS f32x4*)lds;
#pragma unroll
    for (int cb = 0; cb < 5; ++cb) {
      f32x4 s = acc[cb][0];
#pragma unroll
      for (int o = 1; o < 4; ++o) { const int wi = (w + o) & 3; s += hp[(wi * 15 + cb * 3 + (3 - o)) * 64 + l]; }
#pragma unroll
      for (int a = 0; a < 4; ++a) s += ap[(a * 20 + cb * 4 + w) * 64 + l];
      u32x2 o2;
      o2[0] = pk_bf16(s[0] + bf_lo(xr[cb][0]), s[1] + bf_hi(xr[cb][0]));
      o2[1] = pk_bf16(s[2] + bf_lo(xr[cb][1]), s[3] + bf_hi(xr[cb][1]));
      *(u32x2*)(out + orow + 16 * cb) = o2;
    }
    STAMP(ws, 1, 5);
  } else {
    const int u = w - 4;
    GAS u32* cnt = (GAS u32*)(ws + CNT_OFF);
    if (u == 0) {
      const u32 k0 = g < 3 ? KVP_OFF + (u32)(r * 8 * 20 + n) * 96u + g * 32 : OOB;
      const u32 k1 = (g < 3 && n < 4) ? k0 + 16 * 96 : OOB;
      u32x4 pa[8][4], wf[5];
#pragma unroll
      for (int t = 0; t < 8; ++t) { pa[t][0] = bload(rs, k0, t * 1920); pa[t][1] = bload(rs, k0 + 16, t * 1920); pa[t][2] = bload(rs, k1, t * 1920); pa[t][3] = bload(rs, k1 + 16, t * 1920); }
#pragma unroll
      for (int cb = 0; cb < 5; ++cb) wf[cb] = bload(rs, WOT_OFF + (u32)(r * 40 + 5 * c + cb) * 1024u + l * 16, 0);
      f32x4 sm[4];
#pragma unroll
      for (int i = 0; i < 4; ++i) {
        sm[i] = __builtin_bit_cast(f32x4, pa[0][i]);
#pragma unroll
        for (int t = 1; t < 8; ++t) sm[i] += __builtin_bit_cast(f32x4, pa[t][i]);
      }
      const float z0 = sm[1][0], z1 = sm[3][0];
      if (g == 2) { sm[1] = f32x4{0.f, 0.f, 0.f, 0.f}; sm[3] = f32x4{0.f, 0.f, 0.f, 0.f}; }
      const u32x4 a0 = {pk_bf16(sm[0][0], sm[0][1]), pk_bf16(sm[0][2], sm[0][3]), pk_bf16(sm[1][0], sm[1][1]), pk_bf16(sm[1][2], sm[1][3])};
      const u32x4 a1 = {pk_bf16(sm[2][0], sm[2][1]), pk_bf16(sm[2][2], sm[2][3]), pk_bf16(sm[3][0], sm[3][1]), pk_bf16(sm[3][2], sm[3][3])};
      const int uu = r >> 3, G = (r & 7) >> 1, p0 = 20 * (r & 1) + 4 * g, p1 = 20 * (r & 1) + 16;
      const u32 mb = WT_OFF + (u32)(5 * c * NSTEP + 54 + 5 * uu) * 1024u + (n + 16 * G) * 16;
      const u32 o0 = mb + (p0 >> 3) * 1024 + (p0 & 7) * 2;
      const u32 o1 = g == 0 ? mb + (p1 >> 3) * 1024 + (p1 & 7) * 2 : OOB;
#pragma unroll
      for (int cb = 0; cb < 5; ++cb) {
        const f32x4 d0 = mfma16(a0, wf[cb], f32x4{0.f, 0.f, 0.f, 0.f});
        const f32x4 d1 = mfma16(a1, wf[cb], f32x4{0.f, 0.f, 0.f, 0.f});
        __builtin_amdgcn_raw_buffer_store_b64(u32x2{pk_bf16(d0[0], d0[1]), pk_bf16(d0[2], d0[3])}, rs, o0, cb * NSTEP * 1024, 16);
        __builtin_amdgcn_raw_buffer_store_b64(u32x2{pk_bf16(d1[0], d1[1]), pk_bf16(d1[2], d1[3])}, rs, o1, cb * NSTEP * 1024, 16);
      }
      const float z0n = __shfl_xor(z0, 1), z1n = __shfl_xor(z1, 1);
      const u32 zb = ZB_OFF + c * 1280 + (20 * r + n) * 2;
      const bool zl = g == 2 && (n & 1) == 0;
      __builtin_amdgcn_raw_buffer_store_b32(pk_bf16(z0, z0n), rs, zl ? zb : OOB, 0, 16);
      __builtin_amdgcn_raw_buffer_store_b32(pk_bf16(z1, z1n), rs, (zl && n < 4) ? zb + 32 : OOB, 0, 16);
      asm volatile("s_waitcnt vmcnt(0)" ::: "memory");
      if (l == 0) __hip_atomic_fetch_add(cnt + (c * 4 + uu) * 64, 1u, __ATOMIC_RELAXED, __HIP_MEMORY_SCOPE_AGENT);
    }
    STAMP(ws, 1, 1);
    u32x4 P[4][5];
#pragma unroll
    for (int t = 0; t < 4; ++t)
#pragma unroll
      for (int s = 0; s < 5; ++s) P[t][s] = bload(rs, ACT_OFF + (u32)((4 * r + t) * NSTEP + 54 + 5 * u) * 1024u + l * 16, s * 1024);
    __builtin_amdgcn_sched_barrier(0);
    for (int spin = 0; spin < (1 << 20); ++spin) {
      const u32 v = __hip_atomic_load(cnt + (c * 4 + u) * 64, __ATOMIC_RELAXED, __HIP_MEMORY_SCOPE_AGENT);
      if (__builtin_amdgcn_readfirstlane(v) >= 8u) break;
      __builtin_amdgcn_s_sleep(2);
    }
    STAMP(ws, 1, 2);
    __builtin_amdgcn_fence(__ATOMIC_ACQUIRE, "wavefront");
    asm volatile("" ::: "memory");
    u32x4 Z[5], M[5][5];
#pragma unroll
    for (int s = 0; s < 5; ++s) Z[s] = bload_sc1(rs, ZB_OFF + c * 1280 + 320 * u + 80 * g, s * 16);
#pragma unroll
    for (int cb = 0; cb < 5; ++cb)
#pragma unroll
      for (int s = 0; s < 5; ++s) M[cb][s] = bload_sc1(rs, WT_OFF + (u32)((5 * c + cb) * NSTEP + 54 + 5 * u) * 1024u + l * 16, s * 1024);
    __builtin_amdgcn_sched_barrier(0);
    const bool m0 = n == 4 * g, m1 = n == 4 * g + 1;
#pragma unroll
    for (int s = 0; s < 5; ++s)
#pragma unroll
      for (int q = 0; q < 4; ++q) Z[s][q] = ((8 * s + 2 * q >= 20) ? m1 : m0) ? Z[s][q] : 0u;
#pragma unroll
    for (int t = 0; t < 4; ++t) {
      f32x4 den = {0.f, 0.f, 0.f, 0.f};
#pragma unroll
      for (int s = 0; s < 5; ++s) den = mfma16(Z[s], P[t][s], den);
      const float r0 = __builtin_amdgcn_rcpf(den[0] + 1e-6f), r1 = __builtin_amdgcn_rcpf(den[1] + 1e-6f);
#pragma unroll
      for (int s = 0; s < 5; ++s)
#pragma unroll
        for (int q = 0; q < 4; ++q) { const float rr = (8 * s + 2 * q >= 20) ? r1 : r0; P[t][s][q] = pk_bf16(bf_lo(P[t][s][q]) * rr, bf_hi(P[t][s][q]) * rr); }
    }
    STAMP(ws, 1, 3);
    LAS f32x4* ap = (LAS f32x4*)lds;
#pragma unroll
    for (int cb = 0; cb < 5; ++cb) {
      f32x4 a[4];
#pragma unroll
      for (int t = 0; t < 4; ++t) a[t] = f32x4{0.f, 0.f, 0.f, 0.f};
#pragma unroll
      for (int s = 0; s < 5; ++s)
#pragma unroll
        for (int t = 0; t < 4; ++t) a[t] = mfma16(M[cb][s], P[t][s], a[t]);
#pragma unroll
      for (int t = 0; t < 4; ++t) ap[(u * 20 + cb * 4 + t) * 64 + l] = a[t];
    }
    STAMP(ws, 1, 4);
    __syncthreads();
    STAMP(ws, 1, 5);
  }
}

extern "C" void kernel_launch(void* const* d_in, const int* in_sizes, int n_in, void* d_out, int out_size, void* d_ws, size_t ws_size, hipStream_t stream) {
  const u16* x = (const u16*)d_in[0]; const u16* wqkv = (const u16*)d_in[1]; const u16* wo = (const u16*)d_in[2];
  const u16* wg = (const u16*)d_in[4]; const u16* wu = (const u16*)d_in[5]; const u16* wd = (const u16*)d_in[6];
  (void)hipFuncSetAttribute((const void*)k1_kernel, hipFuncAttributeMaxDynamicSharedMemorySize, K1_LDS);
  (void)hipFuncSetAttribute((const void*)k2_kernel, hipFuncAttributeMaxDynamicSharedMemorySize, K2_LDS);
  k1_kernel<<<256, 512, K1_LDS, stream>>>(x, wqkv, wo, wg, wu, wd, (char*)d_ws);
  k2_kernel<<<256, 512, K2_LDS, stream>>>(x, (u16*)d_out, (char*)d_ws);
}
```

```cpp
#include <hip/hip_runtime.h>
#include <stdint.h>
#include <type_traits>

#define ABL_NOREAD 0
#define ABL_NODMA 0
#define ABL_NOROT 0
typedef unsigned short u16;
typedef unsigned int u32;
typedef unsigned long long u64;
typedef __bf16 bf16x8 __attribute__((ext_vector_type(8)));
typedef __bf16 bf16x2 __attribute__((ext_vector_type(2)));
typedef short s16x4 __attribute__((ext_vector_type(4)));
typedef float f32x2 __attribute__((ext_vector_type(2)));
typedef float f32x4 __attribute__((ext_vector_type(4)));
typedef float f32x16 __attribute__((ext_vector_type(16)));
typedef u32 u32x2 __attribute__((ext_vector_type(2)));
typedef u32 u32x4 __attribute__((ext_vector_type(4)));
typedef __amdgpu_buffer_rsrc_t rsrc_t;
#define LAS __attribute__((address_space(3)))
#define GAS __attribute__((address_space(1)))
#define DI __device__ __forceinline__
#define RLX_AGENT __ATOMIC_RELAXED, __HIP_MEMORY_SCOPE_AGENT
#define SB0 __builtin_amdgcn_sched_barrier(0)

constexpr int NSTEP = 74;
constexpr u32 ACT_OFF = 0;
constexpr u32 MT_OFF  = ACT_OFF + 128u * NSTEP * 1024u;
constexpr u32 WT_OFF  = MT_OFF + 40u * 20u * 1024u;
constexpr u32 KVR_OFF = WT_OFF + 40u * 54u * 1024u;
constexpr u32 ZB_OFF  = KVR_OFF + 16u * 8u * 8192u;
constexpr u32 STG_OFF = ZB_OFF + 4u * 1280u;
constexpr u32 CSG_OFF = STG_OFF + 2048u * 8u;
constexpr u32 FH_OFF  = CSG_OFF + 30u * 8u * 192u * 8u;
constexpr u32 FK_OFF  = FH_OFF + 8u * 256u;
constexpr u32 FM_OFF  = FK_OFF + 8u * 256u;
constexpr u32 FW_OFF  = FM_OFF + 16u * 256u;
constexpr u32 EP_OFF  = FW_OFF + 256u;
constexpr u32 WS_END  = EP_OFF + 256u;
constexpr u32 OOB = 0x7ffffff0u;
constexpr int SPIN_CAP = 1 << 15;

constexpr int L_W = 98304, L_SP = 159744, L_ATT = 102400, LDS_BYTES = 163840;
constexpr int XB0 = 65536, XB1 = 0, XB2 = 32768;

#define STAMP(i) do {} while (0)
#define SSTAMP(i) do {} while (0)

DI float bf_lo(u32 v) { return __uint_as_float(v << 16); }
DI float bf_hi(u32 v) { return __uint_as_float(v & 0xffff0000u); }
DI u32 pk_bf16(float a, float b) { f32x2 v = {a, b}; return __builtin_bit_cast(u32, __builtin_convertvector(v, bf16x2)); }
DI float round_bf(float a) { return bf_lo(pk_bf16(a, a)); }
DI float dot2(u32 a, u32 b, float c) { return __builtin_amdgcn_fdot2_f32_bf16(__builtin_bit_cast(bf16x2, a), __builtin_bit_cast(bf16x2, b), c, false); }
DI float xsum32(float v) { auto r = __builtin_amdgcn_permlane32_swap(__float_as_uint(v), __float_as_uint(v), false, false); return __uint_as_float(r[0]) + __uint_as_float(r[1]); }
DI float xsum16(float v) { auto r = __builtin_amdgcn_permlane16_swap(__float_as_uint(v), __float_as_uint(v), false, false); return __uint_as_float(r[0]) + __uint_as_float(r[1]); }
DI f32x16 mfma32(u32x4 a, u32x4 b, f32x16 c) { return __builtin_amdgcn_mfma_f32_32x32x16_bf16(__builtin_bit_cast(bf16x8, a), __builtin_bit_cast(bf16x8, b), c, 0, 0, 0); }
DI f32x4 mfma16(u32x4 a, u32x4 b, f32x4 c) { return __builtin_amdgcn_mfma_f32_16x16x32_bf16(__builtin_bit_cast(bf16x8, a), __builtin_bit_cast(bf16x8, b), c, 0, 0, 0); }
DI u32x2 tr_read(u32 a) { return __builtin_bit_cast(u32x2, __builtin_amdgcn_ds_read_tr16_b64_v4i16((LAS s16x4*)(size_t)a)); }
DI u32x4 tr_frag(u32 a, int d) { const u32x2 lo = tr_read(a), hi = tr_read(a + d); return u32x4{lo[0], lo[1], hi[0], hi[1]}; }
template <class T> DI T lds_ld(u32 a) { return *(const LAS T*)(size_t)a; }
template <class T> DI void lds_st(u32 a, T v) { *(LAS T*)(size_t)a = v; }
DI void dma16(const char* sbase, u32 voff, u32 ldst) {
  asm volatile("s_mov_b32 m0, %2\n\ts_nop 0\n\tglobal_load_lds_dwordx4 %0, %1" ::"v"(voff), "s"(sbase), "s"(ldst) : "memory");
}
template <int N> DI void wait_vm() { asm volatile("s_waitcnt vmcnt(%0)" ::"n"(N) : "memory"); }
DI void wait_lgkm0() { __builtin_amdgcn_s_waitcnt(0xC07F); }
DI void bar() { __builtin_amdgcn_s_barrier(); }
DI u32x4 bload(rsrc_t r, u32 voff, u32 soff) { return __builtin_amdgcn_raw_buffer_load_b128(r, voff, soff, 16); }
DI void bstore16(u32x4 v, rsrc_t r, u32 voff) { __builtin_amdgcn_raw_buffer_store_b128(v, r, voff, 0, 16); }
DI void bstore8(u32x2 v, rsrc_t r, u32 voff) { __builtin_amdgcn_raw_buffer_store_b64(v, r, voff, 0, 16); }
constexpr float LOG2E = 1.4426950408889634f;
DI void wait_flags(const char* ws, u32 off, bool on, u32 tag) {
  GAS u32* p = (GAS u32*)(ws + off);
#pragma unroll 1
  for (int spin = 0; spin < SPIN_CAP; ++spin) {
    const u32 v = on ? __hip_atomic_load(p, RLX_AGENT) : tag;
    if (__all(v == tag)) break;
    __builtin_amdgcn_s_sleep(1);
  }
  __builtin_amdgcn_fence(__ATOMIC_ACQUIRE, "wavefront");
  asm volatile("" ::: "memory");
}
DI void publish(char* ws, u32 off, u32 tag) {
  wait_vm<0>();
  __syncthreads();
  if (threadIdx.x == 0) __hip_atomic_store((GAS u32*)(ws + off), tag, RLX_AGENT);
}

struct Sched { int op[96]; int first[18]; };
constexpr Sched make_sched() {
  Sched r{}; int n = 0;
  for (int k = 0; k < 4; ++k) r.op[n++] = 100 + k;
  r.op[n++] = 0; r.op[n++] = 1; r.op[n++] = 2;
  for (int k = 0; k < 4; ++k) r.op[n++] = 104 + k;
  r.op[n++] = 3; r.op[n++] = 4;
  for (int s = 0; s < 16; ++s) {
    r.first[s + 1] = n;
    const int it = s / 2;
    if (s == 0) { r.op[n++] = 5; r.op[n++] = 108; r.op[n++] = 109; r.op[n++] = 6; }
    if (s <= 14) {
      if ((s & 1) == 0) { r.op[n++] = 7 + 3 * it; r.op[n++] = 8 + 3 * it; r.op[n++] = 100 + 4 * (it + 2) + 2; r.op[n++] = 100 + 4 * (it + 2) + 3; }
      else { r.op[n++] = 9 + 3 * it; r.op[n++] = 100 + 4 * (it + 3); r.op[n++] = 100 + 4 * (it + 3) + 1; }
      if (s == 12) for (int k = 0; k < 2; ++k) r.op[n++] = 1000;
    } else for (int k = 0; k < 5; ++k) r.op[n++] = 1000;
  }
  r.first[17] = n;
  return r;
}
constexpr Sched SCH = make_sched();
constexpr int vm_need(int t, int b) {
  const int issued = b < 0 ? 7 : SCH.first[b + 1];
  int last = -1;
  for (int k = 0; k < issued; ++k) {
    const int op = SCH.op[k];
    if (op < 100 ? (8 * op <= 12 * t + 11) : (op < 1000 && (op - 100) / 4 <= t / 2)) last = k;
  }
  return issued - 1 - last;
}

struct Frag { u32x4 x[2][2]; u32x4 w[3][2]; };

DI int p2_start(int i) { return i * 13 + min(i, 2); }
DI void p2_piece(const char* wdc, int piece, int l) {
  const int blk = piece / 5, i = blk / 5, n = (piece - 5 * blk) * 64 + l, r = n / 10, c = n - 10 * r, k = (r & 19) | ((r & 4) << 1) | ((r & 8) >> 1);
  dma16(wdc + (size_t)(p2_start(i) + blk - 5 * i) * 40960, (u32)k * 1280u + c * 16, piece * 1024);
}

template <int TYPE> DI f32x16 p1_mfma(u32x4 w, u32x4 x, f32x16 c) { return TYPE == 2 ? mfma32(x, w, c) : mfma32(w, x, c); }

template <int TYPE, class F> DI void p1_step(f32x16 (&acc)[3][2], const Frag& cur, bool third, F&& fill) {
#pragma unroll
  for (int i = 0; i < 8; ++i) {
    const int b = (i >> 1) & 1, k = i >> 2, t = i & 1;
    acc[b][t] = p1_mfma<TYPE>(cur.w[b][k], cur.x[t][k], acc[b][t]);
    fill(i);
    SB0;
  }
  if (TYPE != 1 || third) {
#pragma unroll
    for (int i = 8; i < 12; ++i) {
      const int k = (i >> 1) & 1, t = i & 1;
      if (TYPE == 2 && t == 1) continue;
      acc[2][t] = p1_mfma<TYPE>(cur.w[2][k], cur.x[t][k], acc[2][t]);
      SB0;
    }
  }
}
template <int I, int N, class F> DI void static_for(F&& f) { if constexpr (I < N) { f(std::integral_constant<int, I>{}); static_for<I + 1, N>(f); } }
constexpr int xslot(int i) { return i % 3 == 0 ? XB0 : (i % 3 == 1 ? XB1 : XB2); }

DI float exp2_le1(float t) { return __builtin_amdgcn_fmed3f(__builtin_amdgcn_exp2f(t), 0.f, 1.f); }
DI float phi_f(float t) { return fmaxf(t, 1e-6f) + exp2_le1(t * LOG2E); }

template <int TYPE>
DI void phase1(int rt, int ct, int j, int c2, const u16* x, const u16* wqkv, const u16* wo, const u16* wg, const u16* wu, const u16* wd, char* ws, u32& tag_out) {
  const int tid = threadIdx.x, l = tid & 63, w = __builtin_amdgcn_readfirstlane(tid >> 6);
  const int tg = w & 3, cgp = w >> 2, h = l >> 5, n = l & 31;
  const int k0 = ABL_NOROT ? 0 : (rt * 10) >> 3;
  const char* xg = (const char*)x + (size_t)rt * 256 * 1280;
  const u32 xvo = (u32)(8 * w + (l >> 3)) * 1280 + (((l & 7) ^ ((4 * (w & 1) + (l >> 4)) & 7)) * 16);
  auto dma_x = [&](int kk, int xi, u32 slot) { dma16(xg + (xi * 64 * 1280 + kk * 128), xvo, slot + xi * 8192 + w * 1024); };
#pragma unroll
  for (int xi = 0; xi < 4; ++xi) dma_x(k0, xi, XB0);
  const char* wb[3]; u32 wvo[3];
  constexpr u32 STRIDE = TYPE == 0 ? 3456 : 3840, WPER = 64 * STRIDE;
  {
    const char* b0 = TYPE == 0 ? (const char*)(wg + 96 * j) : TYPE == 1 ? (const char*)(wqkv + 160 * j) : (const char*)(wqkv + 640 + 80 * j);
    const char* b1 = TYPE == 0 ? (const char*)(wu + 96 * j) : TYPE == 1 ? b0 + 192 : b0 + 1280;
#pragma unroll
    for (int i = 0; i < 3; ++i) {
      const int pp = 8 * i + w, st = pp >= 12, pi = pp - 12 * st, part = pi >= 6;
      const int m = (pi - 6 * part) * 64 + l, row = m / 12, cp = m - 12 * row;
      int ch = part ? cp ^ 2 : cp;
      if (TYPE == 1 && part && ch >= 8) ch -= 4;
      if (TYPE == 2 && ch >= 10) ch -= 4;
      wb[i] = part ? b1 : b0;
      wvo[i] = (u32)(st * 32 + row) * STRIDE + ch * 16;
    }
  }
  const u32 wl0 = L_W + w * 1024, wl1 = w >= 4 ? wl0 - 61440 : wl0;
  auto dma_w = [&](int kk, int grp) { const int pos = (8 * grp) % 60; dma16(wb[grp % 3] + (size_t)((u32)kk * WPER), wvo[grp % 3], (pos == 56 ? wl1 : wl0) + pos * 1024); };
  auto rot = [&](int kk) { return kk + 1 >= 10 ? kk - 9 : kk + 1; };
  const int k1 = rot(k0), k2 = rot(k1);
  dma_w(k0, 0); dma_w(k0, 1); dma_w(k0, 2);
  u32 ep;
  asm volatile("s_load_dword %0, %1, 0x0" : "=s"(ep) : "s"(ws + EP_OFF) : "memory");

  auto bphys = [&](int b) { int v = b + tg; v = v >= 3 ? v - 3 : v; return v >= 3 ? v - 3 : v; };
  u32 xa[2][4], wa[3][2];
  {
    const int ksw = TYPE == 2 ? (tg & 1) : 0;
#pragma unroll
    for (int t = 0; t < 2; ++t) {
      const int row = 64 * tg + 32 * (TYPE == 2 ? t ^ cgp : t) + n;
#pragma unroll
      for (int k = 0; k < 4; ++k) xa[t][k] = row * 128 + 16 * ((2 * (k ^ ksw) + h) ^ ((row >> 1) & 7));
    }
    const int blk = (l >> 4) & 1, q = (l & 15) >> 2, p = l & 3, rowb = L_W + (8 * h + q) * 192;
    if (TYPE == 2) {
      const int a = cgp ? 3 : 0;
      const u32 b0 = rowb + (a + blk) * 32 + 8 * p, b1 = 6144 + rowb + ((a + blk) ^ 1) * 32 + 8 * p;
      const u32 b2 = rowb + (p >> 1) * 6144 + ((4 + (cgp ^ blk)) ^ (p & 2)) * 16 + 8 * (p & 1);
#pragma unroll
      for (int k = 0; k < 2; ++k) { const u32 o = (k ^ ksw) * 3072; wa[0][k] = b0 + o; wa[1][k] = b1 + o; wa[2][k] = b2 + o; }
    } else {
#pragma unroll
      for (int b = 0; b < 3; ++b) {
        const u32 in32 = 16 * (p & 1) + 8 * (p >> 1);
        if (TYPE == 0) wa[b][0] = rowb + blk * 6144 + ((3 * cgp + bphys(b)) ^ blk) * 32 + in32;
        else wa[b][0] = rowb + cgp * 6144 + ((2 * (cgp ? (b == 2 ? 2 : b ^ (tg & 1)) : bphys(b)) + blk) ^ cgp) * 32 + in32;
        wa[b][1] = 0;
      }
    }
  }
  auto rd_w = [&](int b, int k, int off) { return tr_frag((TYPE == 2 ? wa[b][k] : wa[b][0] + k * 3072) + off, 768); };
  auto rd_x = [&](int t, int k4, int off) {
    return TYPE == 2 ? lds_ld<u32x4>(xa[t][k4] + off) : lds_ld<u32x4>(xa[0][k4] + off + t * 4096);
  };
  auto fill_rd = [&](Frag& f, int i, int st) {
    const int wo_ = (st % 5) * 12288, xo = xslot(st / 2), k4 = 2 * (st & 1);
    if (i == 0) { f.w[0][0] = rd_w(0, 0, wo_); f.x[0][0] = rd_x(0, k4, xo); }
    if (i == 1) { f.x[1][0] = rd_x(1, k4, xo); f.w[1][0] = rd_w(1, 0, wo_); }
    if (i == 2) { f.w[0][1] = rd_w(0, 1, wo_); f.x[0][1] = rd_x(0, k4 + 1, xo); }
    if (i == 3) { f.x[1][1] = rd_x(1, k4 + 1, xo); f.w[1][1] = rd_w(1, 1, wo_); }
    if (i == 4) { f.w[2][0] = rd_w(2, 0, wo_); f.w[2][1] = rd_w(2, 1, wo_); }
  };
  f32x16 acc[3][2];
#pragma unroll
  for (int b = 0; b < 3; ++b)
#pragma unroll
    for (int t = 0; t < 2; ++t)
#pragma unroll
      for (int i = 0; i < 16; ++i) acc[b][t][i] = 0.f;
  Frag fa, fb;
  STAMP(1);
  asm volatile("s_waitcnt vmcnt(%1) lgkmcnt(0)" : "+s"(ep) : "n"(vm_need(0, -1)) : "memory");
  bar(); SB0;
  const u32 tag = ep + 1u;
  tag_out = tag;
#pragma unroll
  for (int i = 0; i < 5; ++i) fill_rd(fa, i, 0);
  SB0;
#pragma unroll
  for (int xi = 0; xi < 4; ++xi) dma_x(k1, xi, XB1);
  dma_w(k1, 3); dma_w(k1, 4);
  SB0;
  STAMP(2);
  const bool third = cgp == 0;
  float cs[3] = {0.f, 0.f, 0.f};
  auto colsum = [&](const Frag& f, int i) {
    constexpr u32 ones = 0x3f803f80u;
    if (TYPE == 2) {
      if (i < 6) { const int b = i >> 1; cs[b] = dot2(f.w[b][0][2 * (i & 1)], ones, cs[b]); cs[b] = dot2(f.w[b][0][2 * (i & 1) + 1], ones, cs[b]); asm volatile("" : "+v"(cs[b])); }
    } else { cs[0] = dot2(f.w[0][i >> 2][i & 3], ones, cs[0]); asm volatile("" : "+v"(cs[0])); }
  };
  auto colid = [&](int b) {
    if (TYPE == 2) return b == 2 ? ((n >> 3) & 1) * 96 + 32 + 8 * (cgp ^ (n >> 4)) + (n & 7) : b * 96 + (cgp ? 48 : 0) + n;
    const int p = (n & 15) >> 2, c16 = 8 * (p & 1) + 4 * (p >> 1) + (n & 3);
    if (TYPE == 0) return (n >> 4) * 96 + (3 * cgp + bphys(b)) * 16 + c16;
    return cgp * 96 + (cgp ? (b == 2 ? 2 : b ^ (tg & 1)) : bphys(b)) * 32 + (n & 16) + c16;
  };
  GAS u64* csg = (GAS u64*)(ws + CSG_OFF) + ct * 1536 + rt;
  int ka = k2, kb = rot(k2);
  const char* wdc = (const char*)(wd + 80 * c2);
  u32x4 gr[2];
  u32 goff[2], pvo[5];
  float gs[2];
  auto gload = [&]() {
#pragma unroll
    for (int k = 0; k < 2; ++k) asm volatile("global_load_dwordx4 %0, %1, %2 sc1" : "=v"(gr[k]) : "v"(goff[k]), "s"(ws) : "memory");
  };
  auto gcheck = [&]() {
    asm volatile("" : "+v"(gr[0]), "+v"(gr[1]));
    return __all(gr[0][1] == tag && gr[0][3] == tag && gr[1][1] == tag && gr[1][3] == tag);
  };
  auto quad_sum = [&](float v) {
    v += __uint_as_float(__builtin_amdgcn_mov_dpp(__float_as_uint(v), 0xB1, 0xf, 0xf, true));
    return v + __uint_as_float(__builtin_amdgcn_mov_dpp(__float_as_uint(v), 0x4E, 0xf, 0xf, true));
  };
  auto pf2 = [&](int k) {
    const int piece = k < 12 ? 8 * k + w : 96 + (w & 3), blk = piece / 5, i = blk / 5;
    const u32 vo = k < 12 ? pvo[(3 * k) % 5] : (w < 4 ? pvo[1] : pvo[2]);
    dma16(wdc + (size_t)(p2_start(i) + blk - 5 * i) * 40960, vo, piece * 1024);
  };
  static_for<0, 15>([&](auto SC) {
    constexpr int S = decltype(SC)::value, IT = S / 2;
    Frag& cur = (S & 1) ? fb : fa; Frag& nxt = (S & 1) ? fa : fb;
    wait_lgkm0(); wait_vm<vm_need(S + 1, S)>(); bar(); SSTAMP(16 + S); SB0;
    p1_step<TYPE>(acc, cur, third, [&](int i) {
      if (!ABL_NOREAD) fill_rd(nxt, i, S + 1);
      if (!ABL_NODMA) {
        if (S == 0) { if (i == 0) dma_w(k1, 5); if (i == 1 || i == 2) dma_x(k2, i - 1, XB2); if (i == 3) dma_w(k2, 6); }
        if ((S & 1) == 0) { if (i == 4 || i == 5) dma_w(ka, 3 + 3 * IT + i); if (i >= 6) dma_x(ka, i - 4, xslot(IT + 2)); }
        else { if (i == 5) dma_w(kb, 9 + 3 * IT); if (i >= 6) dma_x(kb, i - 6, xslot(IT)); if (i == 7) { ka = kb; kb = rot(kb); } }
      }
      if (S < 2) colsum(cur, i);
      if ((S == 2 || S == 3) && i == 5 && (rt & 3) == 3) {
#pragma unroll
        for (int e = 0; e < 8; ++e) colsum(cur, e);
      }
      if (S == 4 && i == 2) {
        if (TYPE == 2) {
#pragma unroll
          for (int b = 0; b < 3; ++b) { const float v = xsum32(cs[b]); if (l < 32 && tg < 2) lds_st<float>(L_SP + (tg * 192 + colid(b)) * 4, v); }
        } else {
          const float v = xsum32(cs[0]);
          if (l < 32) __hip_atomic_store(csg + colid(0) * 8, ((u64)tag << 32) | __float_as_uint(v), RLX_AGENT);
        }
      }
      if (TYPE == 2 && S == 5 && i == 2 && tid < 192)
        __hip_atomic_store(csg + tid * 8, ((u64)tag << 32) | __float_as_uint(lds_ld<float>(L_SP + tid * 4) + lds_ld<float>(L_SP + 768 + tid * 4)), RLX_AGENT);
      if (S == 11 && i == 1) {
        const int hi = (TYPE == 1 && tid >= 128) ? tid - 128 : tid;
        goff[0] = CSG_OFF + ct * 12288 + tid * 16;
        goff[1] = w < 4 ? CSG_OFF + ct * 12288 + 8192 + hi * 16 : STG_OFF + 2048 * rt + (tid & 127) * 16;
      }
      if (S == 12 && i == 7) gload();
      if (S >= 13 && i < (S == 13 ? 3 : 2)) {
        int p5 = w + (S - 13) * 3 + i; p5 = p5 >= 10 ? p5 - 10 : p5 >= 5 ? p5 - 5 : p5; p5 = p5 >= 5 ? p5 - 5 : p5;
        const int m = p5 * 64 + l, r = m / 10, c = m - 10 * r;
        pvo[(S - 13) * 3 + i] = (u32)((r & 19) | ((r & 4) << 1) | ((r & 8) >> 1)) * 1280u + c * 16;
      }
      if (S == 14) {
        if (i == 1) {
          wait_vm<3>();
          if (!gcheck()) {
#pragma unroll 1
            for (int spin = 0; spin < SPIN_CAP; ++spin) { __builtin_amdgcn_s_sleep(2); gload(); wait_vm<0>(); if (gcheck()) break; }
          }
        }
        if (i == 2 || i == 3) gs[i - 2] = quad_sum(__uint_as_float(gr[i - 2][0]) + __uint_as_float(gr[i - 2][2]));
        if (i == 4) lds_st<float>(L_SP + 2048 + (tid >> 2) * 4, gs[0]);
        if (i == 5) {
          if (w < 4) lds_st<float>(L_SP + 2560 + (tid >> 2) * 4, gs[1]);
          else lds_st<f32x4>(L_SP + (tid & 127) * 16, f32x4{bf_lo(gr[1][0]), __builtin_amdgcn_rcpf(bf_hi(gr[1][0])), bf_lo(gr[1][2]), __builtin_amdgcn_rcpf(bf_hi(gr[1][2]))});
        }
      }
    });
  });
  constexpr int B0 = TYPE == 2 ? 2 : 0;
  u32x4 tx[2][8], tw[2][8];
  wait_lgkm0(); wait_vm<vm_need(17, 15)>(); bar(); SSTAMP(31); SB0;
  p1_step<TYPE>(acc, fb, third, [&](int i) {
    if (i < 4) { tw[0][i] = rd_w(B0, i & 1, 12288 * (1 + (i >> 1))); tx[0][i] = rd_x(0, i, XB2); }
    else tx[1][i - 4] = rd_x(1, i - 4, XB2);
    if (i >= 3) pf2(i < 7 ? i - 3 : 12); });
  wait_lgkm0(); wait_vm<5>(); bar(); SSTAMP(32); SB0;

  const rsrc_t rs = __builtin_amdgcn_make_buffer_rsrc(ws, 0, WS_END, 0x00020000);
  auto group = [&](int b, int t, const u32x4 (&wf)[8], auto&& fill) {
#pragma unroll
    for (int i = 0; i < 8; ++i) { acc[b][t] = p1_mfma<TYPE>(wf[i], tx[t][i], acc[b][t]); fill(i); SB0; }
  };
  auto rd_tw = [&](u32x4 (&wf)[8], int b, int i) { wf[i] = rd_w(b, i & 1, 12288 * (1 + (i >> 1))); };
  auto first_fill = [&](int i) {
    if (i < 4) { rd_tw(tw[0], B0, 4 + i); tx[0][4 + i] = rd_x(0, i, XB0); } else tx[1][i] = rd_x(1, i - 4, XB0);
  };

  if constexpr (TYPE != 2) {
    f32x2 st[2]; float ce[2], sd[2];
    f32x4 cv[4];
    u32 ob[8];
    const u32 dst = ACT_OFF + (u32)(16 * rt + 4 * tg + (n >> 4)) * (NSTEP * 1024) + (n & 15) * 16;
    auto ld_cv = [&](int b) {
      const int bp = TYPE == 0 ? bphys(b) : (cgp ? (b == 2 ? 2 : b ^ (tg & 1)) : bphys(b));
      const u32 a = L_SP + 2048 + (TYPE == 0 ? (3 * cgp + bp) * 16 : cgp * 96 + bp * 32) * 4 + 32 * h;
#pragma unroll
      for (int u = 0; u < 4; ++u) cv[u] = lds_ld<f32x4>(a + (u >> 1) * (TYPE == 0 ? 384 : 64) + (u & 1) * 16);
    };
    auto epi = [&](int b, int t, int i) {
      const int bp = TYPE == 0 ? bphys(b) : (cgp ? (b == 2 ? 2 : b ^ (tg & 1)) : bphys(b));
      if (TYPE == 0) {
        const float ex = __builtin_amdgcn_exp2f((acc[b][t][i] - st[t][0] * cv[i >> 2][i & 3]) * ce[t]);
        const float hv = (acc[b][t][8 + i] - st[t][0] * cv[2 + (i >> 2)][i & 3]) * __builtin_amdgcn_rcpf(ex * sd[t] + sd[t]);
        if (i & 1) ob[i >> 1] = pk_bf16(__uint_as_float(ob[i >> 1]), hv); else ob[i >> 1] = __float_as_uint(hv);
        if (i == 7) { const int id = 12 * j + 6 * cgp + 2 * bp + h; bstore16(u32x4{ob[0], ob[1], ob[2], ob[3]}, rs, dst + t * 2 * NSTEP * 1024 + (id >> 2) * 1024 + (id & 3) * 256); }
      } else {
        const float u0 = acc[b][t][2 * i] - st[t][0] * cv[i >> 1][(2 * i) & 3], u1 = acc[b][t][2 * i + 1] - st[t][0] * cv[i >> 1][(2 * i + 1) & 3];
        ob[i] = pk_bf16(fmaxf(u0, sd[t]) * st[t][1] + exp2_le1(u0 * ce[t]), fmaxf(u1, sd[t]) * st[t][1] + exp2_le1(u1 * ce[t]));
        if ((i & 3) == 3) {
          const int ch = 4 * (3 * cgp + bp) + 2 * (i >> 2) + h, g4 = ch / 5;
          bstore16(u32x4{ob[i - 3], ob[i - 2], ob[i - 1], ob[i]}, rs, dst + t * 2 * NSTEP * 1024 + (54 + 5 * j + ch - 5 * g4) * 1024 + g4 * 256);
        }
      }
    };
    group(0, 0, tw[0], [&](int i) {
      first_fill(i);
      if (i == 5) { st[0] = lds_ld<f32x2>(L_SP + (64 * tg + n) * 8); st[1] = lds_ld<f32x2>(L_SP + (64 * tg + 32 + n) * 8); }
      if (i == 6) ld_cv(0);
      if (i == 7) {
#pragma unroll
        for (int t = 0; t < 2; ++t) { ce[t] = (TYPE == 0 ? -LOG2E : LOG2E) * st[t][1]; sd[t] = (TYPE == 0 ? 1.f : 1e-6f) * __builtin_amdgcn_rcpf(st[t][1]); }
      } });
    group(0, 1, tw[0], [&](int i) { rd_tw(tw[1], 1, i); epi(0, 0, i); });
    wait_lgkm0(); bar(); SSTAMP(35); SB0;
    group(1, 0, tw[1], [&](int i) { epi(0, 1, i); if (i & 1) pf2(4 + (i >> 1)); if (i == 7) ld_cv(1); });
    group(1, 1, tw[1], [&](int i) { rd_tw(tw[0], 2, i); epi(1, 0, i); if (i & 1) pf2(8 + (i >> 1)); });
    if (TYPE == 0 || third) {
      group(2, 0, tw[0], [&](int i) { epi(1, 1, i); if (i == 7) ld_cv(2); });
      group(2, 1, tw[0], [&](int i) { epi(2, 0, i); });
      STAMP(3);
#pragma unroll
      for (int i = 0; i < 8; ++i) epi(2, 1, i);
    } else {
      STAMP(3);
#pragma unroll
      for (int i = 0; i < 8; ++i) epi(1, 1, i);
    }
    STAMP(4);
    publish(ws, FH_OFF + rt * 256 + (TYPE == 0 ? j : 18 + j) * 4, tag);
    STAMP(5);
  } else {
    float cv[3];
    u32x4 xp[3][2][2];
    f32x4 sa, sb;
    float zs[2] = {0.f, 0.f};
    auto epi = [&](int b, int t, int i) {
      if ((i & 1) == 0) { const u32 a = L_SP + (64 * tg + 32 * (t ^ cgp) + 8 * (i >> 1) + 4 * h) * 8; sa = lds_ld<f32x4>(a); sb = lds_ld<f32x4>(a + 16); }
      const f32x4 s = (i & 1) ? sb : sa;
      float v0 = (acc[b][t][2 * i] - s[0] * cv[b]) * s[1], v1 = (acc[b][t][2 * i + 1] - s[2] * cv[b]) * s[3];
      if (b == 0) { v0 = phi_f(v0); v1 = phi_f(v1); }
      if (b == 2) { const float p0 = phi_f(v0), p1 = phi_f(v1); v0 = (n & 8) ? v0 : p0; v1 = (n & 8) ? v1 : p1; }
      if (b != 1) zs[b >> 1] += v0 + v1;
      xp[b][t][i >> 2][i & 3] = pk_bf16(v0, v1);
    };
    group(2, 0, tw[0], [&](int i) {
      first_fill(i);
      if (i >= 4) rd_tw(tw[1], 0, i - 4);
      if (i == 5) {
#pragma unroll
        for (int b = 0; b < 3; ++b) cv[b] = lds_ld<float>(L_SP + 2048 + colid(b) * 4);
      } });
    group(0, 0, tw[1], [&](int i) { if (i < 4) rd_tw(tw[1], 0, 4 + i); epi(2, 0, i); });
    if (n >= 16) {
#pragma unroll
      for (int k = 0; k < 2; ++k) lds_st<u32x4>(L_ATT + ((w * 2 + k) * 32 + (n - 16) + 16 * h) * 16, xp[2][0][k]);
    }
    wait_lgkm0(); bar(); SB0;
#pragma unroll
    for (int k = 0; k < 2; ++k) xp[2][1][k] = lds_ld<u32x4>(L_ATT + (((w ^ 4) * 2 + k) * 32 + (n & 15) + 16 * h) * 16);
    group(0, 1, tw[1], [&](int i) { rd_tw(tw[0], 1, i); epi(0, 0, i); if (i & 1) pf2(4 + (i >> 1)); });
    wait_lgkm0(); bar(); SB0;
    group(1, 0, tw[0], [&](int i) { epi(0, 1, i); if (i & 1) pf2(8 + (i >> 1)); });
    f32x16 z16;
#pragma unroll
    for (int i = 0; i < 16; ++i) z16[i] = 0.f;
    f32x16 pmk = z16, pmm = z16, p01 = z16, pmv = z16;
    group(1, 1, tw[0], [&](int i) { epi(1, 0, i); });
    STAMP(3);
#pragma unroll
    for (int i = 0; i < 4; ++i) {
      const int t = i >> 1, k = i & 1;
      pmk = mfma32(xp[2][t][k], xp[0][t][k], pmk); epi(1, 1, 2 * i); SB0;
      pmm = mfma32(xp[2][t][k], xp[2][t][k], pmm); epi(1, 1, 2 * i + 1); SB0;
    }
#pragma unroll
    for (int i = 0; i < 4; ++i) { p01 = mfma32(xp[0][i >> 1][i & 1], xp[1][i >> 1][i & 1], p01); pmv = mfma32(xp[2][i >> 1][i & 1], xp[1][i >> 1][i & 1], pmv); }
    const u32 red = L_ATT + w * 7168 + l * 16;
    { const float z0 = xsum32(zs[0]), z1 = xsum32(zs[1]); if (l < 32) lds_st<f32x2>(L_SP + 2048 + (w * 32 + l) * 8, f32x2{z0, z1}); }
    lds_st<f32x4>(red + 4096, f32x4{pmk[4], pmk[5], pmk[6], pmk[7]});
    lds_st<f32x4>(red + 6144, f32x4{pmm[0], pmm[1], pmm[2], pmm[3]});
#pragma unroll
    for (int qd = 0; qd < 4; ++qd) lds_st<f32x4>(red + qd * 1024, f32x4{p01[4 * qd], p01[4 * qd + 1], p01[4 * qd + 2], p01[4 * qd + 3]});
    lds_st<f32x4>(red + 5120, f32x4{pmv[0], pmv[1], pmv[2], pmv[3]});
    __syncthreads();
    {
      f32x4 s0 = lds_ld<f32x4>(L_ATT + (4 * cgp) * 7168 + 2 * tg * 1024 + l * 16), s1;
#pragma unroll
      for (int k = 1; k < 4; ++k) s0 += lds_ld<f32x4>(L_ATT + (4 * cgp + k) * 7168 + 2 * tg * 1024 + l * 16);
      const u32 o = KVR_OFF + (u32)((2 * j + cgp) * 8 + rt) * 8192 + 2 * tg * 1024 + l * 16;
      bstore16(__builtin_bit_cast(u32x4, s0), rs, o);
      if (tg < 3) {
        s1 = lds_ld<f32x4>(L_ATT + (4 * cgp) * 7168 + (2 * tg + 1) * 1024 + l * 16);
#pragma unroll
        for (int k = 1; k < 4; ++k) s1 += lds_ld<f32x4>(L_ATT + (4 * cgp + k) * 7168 + (2 * tg + 1) * 1024 + l * 16);
      } else {
        s1 = f32x4{0.f, 0.f, 0.f, 0.f};
#pragma unroll
        for (int k = 0; k < 4; ++k) {
          const f32x2 a = lds_ld<f32x2>(L_SP + 2048 + ((4 * cgp + k) * 32 + n) * 8);
          s1[0] += a[0]; s1[1] += a[1] + lds_ld<float>(L_SP + 2048 + ((4 * (1 - cgp) + k) * 32 + (n ^ 16)) * 8 + 4);
        }
      }
      bstore16(__builtin_bit_cast(u32x4, s1), rs, o + 1024);
    }
    STAMP(4);
    publish(ws, FK_OFF + j * 256 + rt * 4, tag);
    STAMP(5);
  }
}

DI void mjob_prefetch(int pair, const u16* wo) {
  const int l = threadIdx.x & 63, w = __builtin_amdgcn_readfirstlane(threadIdx.x >> 6);
#pragma unroll 1
  for (int pc = w; pc < 50; pc += 8) dma16((const char*)(wo + (size_t)pair * 40 * 640) + pc * 1024, l * 16, L_ATT + pc * 1024);
}
DI void mjob(int pair, u32 tag, char* ws, rsrc_t rs) {
  const int tid = threadIdx.x, l = tid & 63, w = __builtin_amdgcn_readfirstlane(tid >> 6), h = l >> 5, n = l & 31;
  constexpr u32 KVL = L_ATT + 51200;
  if (tid < 120) lds_st<u32x2>(KVL + (tid / 3) * 64 + 40 + (tid % 3) * 8, u32x2{0u, 0u});
  const int lo8 = 8 * (pair & 1), mix = 32 - 32 * (pair & 1);
  u32 da[4];
#pragma unroll
  for (int e = 0; e < 4; ++e) {
    const int r4 = 4 * h + e;
    const int kc = w < 4 ? lo8 + 8 * w + r4 : w == 4 ? lo8 + n : w < 7 ? mix + r4 : (e == 0 ? lo8 : mix) + n;
    const int vc = w == 4 ? mix + r4 : w == 6 ? mix + n - 8 : w == 7 ? kc : lo8 + n;
    const bool ok = (w == 6 ? (n >= 8 && n < 16) : w == 7 ? (h == 0 && (e == 0 || (e == 1 && n < 8))) : true) && (vc >= 20) == (kc >= 20);
    da[e] = !ok ? KVL + 2688 + tid * 2 : w == 7 ? KVL + 2560 + kc * 2 : KVL + kc * 64 + (vc >= 20 ? vc - 20 : vc) * 2;
  }
  wait_flags(ws, FK_OFF + (pair >> 1) * 256 + (l & 7) * 4, l < 8, tag);
  STAMP(5);
  u32x4 pa[8];
#pragma unroll
  for (int k = 0; k < 8; ++k) pa[k] = bload(rs, KVR_OFF + (u32)pair * 65536 + tid * 16, k * 8192);
  f32x4 s = __builtin_bit_cast(f32x4, pa[0]);
#pragma unroll
  for (int k = 1; k < 8; ++k) s += __builtin_bit_cast(f32x4, pa[k]);
#pragma unroll
  for (int e = 0; e < 4; ++e) lds_st<u16>(da[e], (u16)pk_bf16(s[e], s[e]));
  __syncthreads();
  STAMP(6);
  const int g = l >> 4, c = l & 15;
  const u32x4 zero4 = {0u, 0u, 0u, 0u};
  const int kx = 8 * (c >> 2) + (c & 3), ky = kx + 4, kz = 32 + (c & 7);
  const u32x4 rx = lds_ld<u32x4>(KVL + kx * 64 + g * 16), ry = lds_ld<u32x4>(KVL + ky * 64 + g * 16), az = lds_ld<u32x4>(KVL + kz * 64 + g * 16);
  const u32x4 ax0 = kx < 20 ? rx : zero4, ax1 = kx < 20 ? zero4 : rx, ay0 = ky < 20 ? ry : zero4, ay1 = ky < 20 ? zero4 : ry;
  const int q = c >> 2, p = l & 3;
  const u32 ra = L_ATT + min(8 * g + q, 19) * 1280 + 8 * p, rb = L_ATT + min(8 * g + 4 + q, 19) * 1280 + 8 * p;
  const u32 mo = MT_OFF + (u32)(5 * (pair >> 2)) * 1024 + (c + 16 * (pair & 3)) * 16;
#pragma unroll
  for (int ci = 0; ci < 5; ++ci) {
    const int cb = w + 8 * ci;
    const u32x2 e0 = tr_read(ra + cb * 32), e1 = tr_read(rb + cb * 32), o0 = tr_read(ra + cb * 32 + 25600), o1 = tr_read(rb + cb * 32 + 25600);
    const u32x4 be = {e0[0], e0[1], e1[0], e1[1]}, bo = {o0[0], o0[1], o1[0], o1[1]};
    const f32x4 z4 = {0.f, 0.f, 0.f, 0.f};
    const f32x4 dx = mfma16(ax1, bo, mfma16(ax0, be, z4)), dy = mfma16(ay1, bo, mfma16(ay0, be, z4)), dz = mfma16(az, bo, z4);
    bstore16(u32x4{pk_bf16(dx[0], dx[1]), pk_bf16(dx[2], dx[3]), pk_bf16(dy[0], dy[1]), pk_bf16(dy[2], dy[3])}, rs, mo + g * 1024 + cb * 20480);
    bstore8(u32x2{pk_bf16(dz[0], dz[1]), pk_bf16(dz[2], dz[3])}, rs, g < 2 ? mo + 4096 + 8 * g + cb * 20480 : OOB);
  }
  if (w == 7) bstore16(lds_ld<u32x4>(KVL + 2560 + min(l, 4) * 16), rs, l < 5 ? ZB_OFF + pair * 80 + l * 16 : OOB);
  publish(ws, FM_OFF + (pair >> 2) * 256 + (pair & 3) * 4, tag);
  STAMP(7);
}

DI int wt_step(int i) { const int q = i < 9 ? 0 : i < 18 ? 1 : i < 26 ? 2 : 3; return i + 5 * (q + 1); }

DI void spare(int sg, int c2, const u16* x, const u16* wd, char* ws, u32& tag_out) {
  const int tid = threadIdx.x, l = tid & 63, w = __builtin_amdgcn_readfirstlane(tid >> 6);
  u32x4 v[4][5];
  const int row0 = 128 * sg + 16 * w + (l >> 4);
#pragma unroll
  for (int a = 0; a < 4; ++a)
#pragma unroll
    for (int k = 0; k < 5; ++k) v[a][k] = *(const u32x4*)((const char*)x + (size_t)(row0 + 4 * a) * 1280 + k * 256 + (l & 15) * 16);
  u32 ep;
  asm volatile("s_load_dword %0, %1, 0x0\n\ts_waitcnt lgkmcnt(0)" : "=s"(ep) : "s"(ws + EP_OFF) : "memory");
  const u32 tag = ep + 1u;
  tag_out = tag;
  const int nst = sg < 2 ? 3 : 2;
#pragma unroll
  for (int a = 0; a < 4; ++a) {
    float s = 0.f, q = 0.f;
#pragma unroll
    for (int k = 0; k < 5; ++k)
#pragma unroll
      for (int e = 0; e < 4; ++e) { s = dot2(v[a][k][e], 0x3f803f80u, s); q = dot2(v[a][k][e], v[a][k][e], q); }
#pragma unroll
    for (int o = 1; o < 16; o <<= 1) { s += __shfl_xor(s, o); q += __shfl_xor(q, o); }
    const float mean = s * (1.f / 640.f);
    const float sd = __builtin_amdgcn_sqrtf(round_bf(round_bf(fmaxf(q * (1.f / 640.f) - mean * mean, 0.f)) + 1e-5f));
    if ((l & 15) == 0) __hip_atomic_store((GAS u64*)(ws + STG_OFF) + row0 + 4 * a, ((u64)tag << 32) | pk_bf16(mean, sd), RLX_AGENT);
  }
  STAMP(1);
#pragma unroll 1
  for (int a = 0; a < nst; ++a)
#pragma unroll
    for (int i = 0; i < 5; ++i) dma16((const char*)wd + (size_t)wt_step(sg + 16 * a) * 40960 + (w * 5 + i) * 1024, l * 16, a * 40960 + (w * 5 + i) * 1024);
  wait_vm<0>();
  __syncthreads();
  STAMP(2);
  const rsrc_t rs = __builtin_amdgcn_make_buffer_rsrc(ws, 0, WS_END, 0x00020000);
#pragma unroll 1
  for (int a = 0; a < nst; ++a)
#pragma unroll
    for (int i = 0; i < 5; ++i) {
      const int cb = w * 5 + i;
      const u32x4 f = tr_frag(a * 40960 + (8 * (l >> 4) + ((l & 15) >> 2)) * 1280 + cb * 32 + 8 * (l & 3), 5120);
      bstore16(f, rs, WT_OFF + (u32)(cb * 54 + wt_step(sg + 16 * a)) * 1024 + l * 16);
    }
  STAMP(3);
  publish(ws, FW_OFF + sg * 4, tag);
  STAMP(4);
#pragma unroll 1
  for (int pc = w; pc < 100; pc += 8) p2_piece((const char*)(wd + 80 * c2), pc, l);
  STAMP(5);
  if (sg == 0 && w < 4) {
    const u32 off = tid < 240 ? CSG_OFF + (tid >> 3) * 12288 + (tid & 7) * 8 + 4 : STG_OFF + (tid - 240) * 1024 + 4;
    wait_flags(ws, off, true, tag);
  }
  wait_vm<0>();
  __syncthreads();
  if (sg == 0 && tid == 0) __hip_atomic_store((GAS u32*)(ws + EP_OFF), tag, RLX_AGENT);
}

constexpr int AWIN = 4;
constexpr int h_step(int t) { return t < 4 ? 5 + t : t < 9 ? t - 4 : t; }

template <int T> DI void p2_hsteps(f32x4 (&acc)[5][4], u32x4 (&A)[AWIN][4], u32x4 (&B)[4][5], u32x4& bl, rsrc_t rs, u32 va, const u32 (&so)[4], u32 vb, u32 wl, bool extra) {
  if constexpr (T < 14) {
#pragma unroll
    for (int cb = 0; cb < 5; ++cb) {
      constexpr bool L = T >= 4 && T < 9;
      const u32x4 bc = L ? bl : B[(T < 4 ? T : T - 9) & 3][cb];
      if (cb < 4 ? L : (T + 1 >= 4 && T + 1 < 9)) bl = tr_frag(wl + (cb < 4 ? (T - 4) * 5120 + (cb + 1) * 32 : (T - 3) * 5120), 1280);
#pragma unroll
      for (int t = 0; t < 4; ++t) acc[cb][t] = mfma16(bc, A[T % AWIN][t], acc[cb][t]);
      SB0;
    }
    if constexpr (T + AWIN < 14) {
#pragma unroll
      for (int t = 0; t < 4; ++t) A[T % AWIN][t] = bload(rs, T + AWIN == 13 ? (extra ? va : OOB) : va, so[t] + h_step(T + AWIN) * 1024);
    }
    if constexpr (T < 4 || T == 9) {
      constexpr int N = T < 4 ? T + 9 : 13;
#pragma unroll
      for (int cb = 0; cb < 5; ++cb) B[T < 4 ? T : 0][cb] = bload(rs, N == 13 ? (extra ? vb : OOB) : vb, cb * 55296 + N * 1024);
    }
    SB0;
    p2_hsteps<T + 1>(acc, A, B, bl, rs, va, so, vb, wl, extra);
  }
}

DI void phase2(int r, int c, int job, u32 tag, const u16* x, const u16* wo, u16* out, char* ws) {
  const int tid = threadIdx.x, l = tid & 63, w = __builtin_amdgcn_readfirstlane(tid >> 6);
  const rsrc_t rs = __builtin_amdgcn_make_buffer_rsrc(ws, 0, WS_END, 0x00020000);
  const int g = l >> 4, n = l & 15;
  auto orow = [&]() { int t = threadIdx.x; asm volatile("" : "+v"(t)); return (u32)((64 * r + 16 * (w & 3) + (t & 15)) * 640 + 80 * c + ((t >> 2) & 12)); };
  STAMP(8);
  if (job >= 0) mjob_prefetch(job, wo);
  if (w < 4) {
    const int start = p2_start(w);
    const u32 vb = WT_OFF + (u32)(5 * c * 54 + start) * 1024u + l * 16;
    u32x4 B[4][5];
    wait_flags(ws, FW_OFF + n * 4, l < 16, tag);
#pragma unroll
    for (int s = 0; s < 4; ++s) {
#pragma unroll
      for (int cb = 0; cb < 5; ++cb) B[s][cb] = bload(rs, vb, cb * 55296 + (5 + s) * 1024);
      SB0;
    }
    if (job >= 0) mjob(job, tag, ws, rs);
    const u32 va = ACT_OFF + (u32)(4 * r * NSTEP + start) * 1024u + l * 16;
    u32 so[4];
#pragma unroll
    for (int t = 0; t < 4; ++t) so[t] = ((t + w) & 3) * (NSTEP * 1024);
    const u32 wl = w * 25600 + (16 * (g >> 1) + 4 * (g & 1) + (n >> 2)) * 160 + 8 * (l & 3);
    u32x4 A[AWIN][4], bl;
    wait_flags(ws, FH_OFF + (r >> 2) * 256 + min(l, 17) * 4, l >= start / 3 && l <= (start + 12 + (w < 2)) / 3, tag);
    STAMP(9);
#pragma unroll
    for (int s = 0; s < AWIN; ++s) {
#pragma unroll
      for (int t = 0; t < 4; ++t) A[s][t] = bload(rs, va, so[t] + h_step(s) * 1024);
      SB0;
    }
    f32x4 acc[5][4];
#pragma unroll
    for (int cb = 0; cb < 5; ++cb)
#pragma unroll
      for (int t = 0; t < 4; ++t) acc[cb][t] = f32x4{0.f, 0.f, 0.f, 0.f};
    p2_hsteps<0>(acc, A, B, bl, rs, va, so, vb, wl, w < 2);
    STAMP(10);
    __syncthreads();
    STAMP(11);
#pragma unroll
    for (int cb = 0; cb < 5; ++cb)
#pragma unroll
      for (int t = 1; t < 4; ++t) lds_st<f32x4>(w * 25600 + (cb * 3 + (t - 1)) * 1024 + l * 16, acc[cb][t]);
    __syncthreads();
    STAMP(12);
    const u32 oo = orow();
#pragma unroll
    for (int cb = 0; cb < 5; ++cb) {
      f32x4 s = acc[cb][0];
#pragma unroll
      for (int o = 1; o < 4; ++o) s += lds_ld<f32x4>(((w + o) & 3) * 25600 + (cb * 3 + (3 - o)) * 1024 + l * 16);
      s += lds_ld<f32x4>(w * 25600 + (15 + cb) * 1024 + l * 16);
      *(u32x2*)(out + oo + 16 * cb) = u32x2{pk_bf16(s[0], s[1]), pk_bf16(s[2], s[3])};
    }
    STAMP(13);
  } else {
    const int u = w - 4;
    u32x4 P[4][5];
    wait_flags(ws, FH_OFF + (r >> 2) * 256 + (18 + u) * 4, l == 0, tag);
#pragma unroll
    for (int t = 0; t < 4; ++t) {
#pragma unroll
      for (int s = 0; s < 5; ++s) P[t][s] = bload(rs, ACT_OFF + (u32)(4 * r * NSTEP + 54 + 5 * u) * 1024u + l * 16, ((t + u) & 3) * (NSTEP * 1024) + s * 1024);
      SB0;
    }
    if (job >= 0) mjob(job, tag, ws, rs);
    STAMP(9);
    wait_flags(ws, FM_OFF + u * 256 + (l & 3) * 4, l < 4, tag);
    STAMP(10);
    u32x4 Z[5], M[5][5];
#pragma unroll
    for (int s = 0; s < 5; ++s) Z[s] = bload(rs, ZB_OFF + 320 * u + 80 * g, s * 16);
    SB0;
    const u32 vm = MT_OFF + (u32)(5 * c * 20 + 5 * u) * 1024u + l * 16;
#pragma unroll
    for (int cb = 0; cb < 4; ++cb) {
#pragma unroll
      for (int s = 0; s < 5; ++s) M[cb][s] = bload(rs, vm, (cb * 20 + s) * 1024);
      SB0;
    }
    const bool m0 = n == 4 * g, m1 = n == 4 * g + 1;
#pragma unroll
    for (int s = 0; s < 5; ++s)
#pragma unroll
      for (int q = 0; q < 4; ++q) Z[s][q] = ((8 * s + 2 * q >= 20) ? m1 : m0) ? Z[s][q] : 0u;
    float rd[4][2];
#pragma unroll
    for (int t = 0; t < 4; ++t) {
      f32x4 den = {0.f, 0.f, 0.f, 0.f};
#pragma unroll
      for (int s = 0; s < 5; ++s) den = mfma16(Z[s], P[t][s], den);
      rd[t][0] = __builtin_amdgcn_rcpf(den[0] + 1e-6f); rd[t][1] = __builtin_amdgcn_rcpf(den[1] + 1e-6f);
    }
    SB0;
#pragma unroll
    for (int s = 0; s < 5; ++s) M[4][s] = bload(rs, vm, (80 + s) * 1024);
    u32x2 xr[5];
    const u32 oo = orow();
#pragma unroll
    for (int cb = 0; cb < 5; ++cb) xr[cb] = *(const u32x2*)(x + oo + 16 * cb);
    SB0;
#pragma unroll
    for (int t = 0; t < 4; ++t)
#pragma unroll
      for (int s = 0; s < 5; ++s) {
#pragma unroll
        for (int q = 0; q < 4; ++q) { const float rr = rd[t][8 * s + 2 * q >= 20]; P[t][s][q] = pk_bf16(bf_lo(P[t][s][q]) * rr, bf_hi(P[t][s][q]) * rr); }
        SB0;
      }
    STAMP(11);
    f32x4 own[5];
#pragma unroll
    for (int cb = 0; cb < 5; ++cb) {
      f32x4 a[4];
#pragma unroll
      for (int t = 0; t < 4; ++t) a[t] = f32x4{0.f, 0.f, 0.f, 0.f};
#pragma unroll
      for (int s = 0; s < 5; ++s)
#pragma unroll
        for (int t = 0; t < 4; ++t) a[t] = mfma16(M[cb][s], P[t][s], a[t]);
      own[cb] = a[0];
#pragma unroll
      for (int t = 1; t < 4; ++t) lds_st<f32x4>(L_ATT + (u * 15 + cb * 3 + (t - 1)) * 1024 + l * 16, a[t]);
    }
    STAMP(12);
    __syncthreads();
#pragma unroll
    for (int cb = 0; cb < 5; ++cb) {
      f32x4 s = own[cb] + f32x4{bf_lo(xr[cb][0]), bf_hi(xr[cb][0]), bf_lo(xr[cb][1]), bf_hi(xr[cb][1])};
#pragma unroll
      for (int o = 1; o < 4; ++o) s += lds_ld<f32x4>(L_ATT + (((u + o) & 3) * 15 + cb * 3 + (3 - o)) * 1024 + l * 16);
      lds_st<f32x4>(u * 25600 + (15 + cb) * 1024 + l * 16, s);
    }
    __syncthreads();
  }
}

__global__ __launch_bounds__(512, 2) void fused_kernel(const u16* __restrict__ x, const u16* __restrict__ wqkv, const u16* __restrict__ wo,
                                                       const u16* __restrict__ wg, const u16* __restrict__ wu, const u16* __restrict__ wd,
                                                       u16* __restrict__ out, char* ws) {
  STAMP(0);
  const int bid = blockIdx.x, xcd = bid & 7, idx = bid >> 3;
  const int r2 = xcd * 4 + (idx >> 3), c2 = idx & 7;
  const int rt = 4 * (xcd & 1) + (idx & 3), cgrp = xcd >> 1, ci = idx >> 2;
  const int ct = cgrp < 2 ? cgrp * 8 + ci : 16 + (cgrp - 2) * 7 + ci;
  u32 tag;
  if (cgrp >= 2 && ci == 7) spare(((cgrp - 2) * 2 + (xcd & 1)) * 4 + (idx & 3), c2, x, wd, ws, tag);
  else if (ct < 18) phase1<0>(rt, ct, ct, c2, x, wqkv, wo, wg, wu, wd, ws, tag);
  else if (ct < 26) phase1<2>(rt, ct, ct - 18, c2, x, wqkv, wo, wg, wu, wd, ws, tag);
  else phase1<1>(rt, ct, ct - 26, c2, x, wqkv, wo, wg, wu, wd, ws, tag);
  const int job = (cgrp >= 2 && ci == 7) ? ((cgrp - 2) * 2 + (xcd & 1)) * 4 + (idx & 3) : -1;
  phase2(r2, c2, job, tag, x, wo, out, ws);
}

extern "C" void kernel_launch(void* const* d_in, const int* in_sizes, int n_in, void* d_out, int out_size, void* d_ws, size_t ws_size, hipStream_t stream) {
  (void)hipFuncSetAttribute((const void*)fused_kernel, hipFuncAttributeMaxDynamicSharedMemorySize, LDS_BYTES);
  fused_kernel<<<256, 512, LDS_BYTES, stream>>>((const u16*)d_in[0], (const u16*)d_in[1], (const u16*)d_in[2], (const u16*)d_in[4], (const u16*)d_in[5],
                                                (const u16*)d_in[6], (u16*)d_out, (char*)d_ws);
}
```

```cpp
#include <hip/hip_runtime.h>
#include <stdint.h>
#include <type_traits>

#define ABL_NOREAD 0
#define ABL_NODMA 0
#define MAPPING 2
#define ABL_NOROT 0
typedef unsigned short u16;
typedef unsigned int u32;
typedef unsigned long long u64;
typedef __bf16 bf16x8 __attribute__((ext_vector_type(8)));
typedef __bf16 bf16x2 __attribute__((ext_vector_type(2)));
typedef short s16x4 __attribute__((ext_vector_type(4)));
typedef float f32x2 __attribute__((ext_vector_type(2)));
typedef float f32x4 __attribute__((ext_vector_type(4)));
typedef float f32x16 __attribute__((ext_vector_type(16)));
typedef u32 u32x2 __attribute__((ext_vector_type(2)));
typedef u32 u32x4 __attribute__((ext_vector_type(4)));
typedef __amdgpu_buffer_rsrc_t rsrc_t;
#define LAS __attribute__((address_space(3)))
#define GAS __attribute__((address_space(1)))
#define DI __device__ __forceinline__
#define RLX_AGENT __ATOMIC_RELAXED, __HIP_MEMORY_SCOPE_AGENT
#define SB0 __builtin_amdgcn_sched_barrier(0)

constexpr int NSTEP = 74;
constexpr u32 ACT_OFF = 0;
constexpr u32 MT_OFF  = ACT_OFF + 128u * NSTEP * 1024u;
constexpr u32 WT_OFF  = MT_OFF + 40u * 20u * 1024u;
constexpr u32 KVR_OFF = WT_OFF + 40u * 54u * 1024u;
constexpr u32 ZB_OFF  = KVR_OFF + 16u * 8u * 3840u;
constexpr u32 STG_OFF = ZB_OFF + 32u * 1280u;
constexpr u32 CSG_OFF = STG_OFF + 2048u * 8u;
constexpr u32 FH_OFF  = CSG_OFF + 30u * 8u * 192u * 8u;
constexpr u32 FK_OFF  = FH_OFF + 64u * 256u;
constexpr u32 FW_OFF  = FK_OFF + 8u * 256u;
constexpr u32 EP_OFF  = FW_OFF + 8u * 256u;
constexpr u32 WS_END  = EP_OFF + 256u;
constexpr u32 OOB = 0x7ffffff0u;
constexpr int SPIN_CAP = 1 << 15;

constexpr int L_W = 98304, L_SP = 159744, L_ATT = 102400, L_MJ = 143360, L_CNT = 163808, LDS_BYTES = 163840;
constexpr int XB0 = 65536, XB1 = 0, XB2 = 32768;

#define STAMP(i) do {} while (0)
#define SSTAMP(i) do {} while (0)

DI float bf_lo(u32 v) { return __uint_as_float(v << 16); }
DI float bf_hi(u32 v) { return __uint_as_float(v & 0xffff0000u); }
DI u32 pk_bf16(float a, float b) { f32x2 v = {a, b}; return __builtin_bit_cast(u32, __builtin_convertvector(v, bf16x2)); }
DI float round_bf(float a) { return bf_lo(pk_bf16(a, a)); }
DI float dot2(u32 a, u32 b, float c) { return __builtin_amdgcn_fdot2_f32_bf16(__builtin_bit_cast(bf16x2, a), __builtin_bit_cast(bf16x2, b), c, false); }
DI float xsum32(float v) { auto r = __builtin_amdgcn_permlane32_swap(__float_as_uint(v), __float_as_uint(v), false, false); return __uint_as_float(r[0]) + __uint_as_float(r[1]); }
DI float xsum16(float v) { auto r = __builtin_amdgcn_permlane16_swap(__float_as_uint(v), __float_as_uint(v), false, false); return __uint_as_float(r[0]) + __uint_as_float(r[1]); }
DI f32x16 mfma32(u32x4 a, u32x4 b, f32x16 c) { return __builtin_amdgcn_mfma_f32_32x32x16_bf16(__builtin_bit_cast(bf16x8, a), __builtin_bit_cast(bf16x8, b), c, 0, 0, 0); }
DI f32x4 mfma16(u32x4 a, u32x4 b, f32x4 c) { return __builtin_amdgcn_mfma_f32_16x16x32_bf16(__builtin_bit_cast(bf16x8, a), __builtin_bit_cast(bf16x8, b), c, 0, 0, 0); }
DI u32x2 tr_read(u32 a) { return __builtin_bit_cast(u32x2, __builtin_amdgcn_ds_read_tr16_b64_v4i16((LAS s16x4*)(size_t)a)); }
DI u32x4 tr_frag(u32 a, int d) { const u32x2 lo = tr_read(a), hi = tr_read(a + d); return u32x4{lo[0], lo[1], hi[0], hi[1]}; }
template <class T> DI T lds_ld(u32 a) { return *(const LAS T*)(size_t)a; }
template <class T> DI void lds_st(u32 a, T v) { *(LAS T*)(size_t)a = v; }
DI void dma16(const char* sbase, u32 voff, u32 ldst) {
  asm volatile("s_mov_b32 m0, %2\n\ts_nop 0\n\tglobal_load_lds_dwordx4 %0, %1" ::"v"(voff), "s"(sbase), "s"(ldst) : "memory");
}
template <int N> DI void wait_vm() { asm volatile("s_waitcnt vmcnt(%0)" ::"n"(N) : "memory"); }
DI void wait_lgkm0() { __builtin_amdgcn_s_waitcnt(0xC07F); }
DI void bar() { __builtin_amdgcn_s_barrier(); }
DI u32x4 bload(rsrc_t r, u32 voff, u32 soff) { return __builtin_amdgcn_raw_buffer_load_b128(r, voff, soff, 16); }
DI void bstore16(u32x4 v, rsrc_t r, u32 voff) { __builtin_amdgcn_raw_buffer_store_b128(v, r, voff, 0, 16); }
DI void bstore8(u32x2 v, rsrc_t r, u32 voff) { __builtin_amdgcn_raw_buffer_store_b64(v, r, voff, 0, 16); }
constexpr u32 SENT = 0xffffffffu;
DI u32 nosent(u32 v) { return min(v, SENT - 1u); }
DI u32x4 nosent4(u32x4 v) { return u32x4{nosent(v[0]), nosent(v[1]), nosent(v[2]), nosent(v[3])}; }
DI u32 max4(u32 m, u32x4 v) { return max(max(m, max(v[0], v[1])), max(v[2], v[3])); }
constexpr float LOG2E = 1.4426950408889634f;
template <int SLEEP = 4> DI void wait_flags(const char* ws, u32 off, bool on, u32 tag) {
  GAS u32* p = (GAS u32*)(ws + off);
#pragma unroll 1
  for (int spin = 0; spin < SPIN_CAP; ++spin) {
    const u32 v = on ? __hip_atomic_load(p, RLX_AGENT) : tag;
    if (__all(v == tag)) break;
    __builtin_amdgcn_s_sleep(SLEEP);
  }
  __builtin_amdgcn_fence(__ATOMIC_ACQUIRE, "wavefront");
  asm volatile("" ::: "memory");
}
template <int SLEEP> DI void wait_data(rsrc_t rs, u32 off) {
#pragma unroll 1
  for (int spin = 0; spin < SPIN_CAP; ++spin) {
    if (!__any(__builtin_amdgcn_raw_buffer_load_b32(rs, off, 0, 16) == SENT)) break;
    __builtin_amdgcn_s_sleep(SLEEP);
    asm volatile("" ::: "memory");
  }
  asm volatile("" ::: "memory");
}
struct Poll { u32 v[4]; };
DI void poll_flags(Poll& p, const char* ws, u32 off, bool on, u32 tag) {
  const u64 skip = __ballot(!on);
  u32 cnt = SPIN_CAP;
#define POLL_STEP(LD, CK) "global_load_dword %" #LD ", %5, %6 sc1\n\ts_waitcnt vmcnt(3)\n\tv_cmp_eq_u32 vcc, %7, %" #CK "\n\ts_nop 0\n\ts_or_b64 vcc, vcc, %8\n\ts_cmp_eq_u64 vcc, -1\n\ts_cbranch_scc1 .Lpd%=\n\ts_sleep 6\n\t"
  asm volatile("global_load_dword %0, %5, %6 sc1\n\ts_sleep 6\n\tglobal_load_dword %1, %5, %6 sc1\n\ts_sleep 6\n\tglobal_load_dword %2, %5, %6 sc1\n\ts_sleep 6\n"
               ".Lpl%=:\n\t" POLL_STEP(3, 0) POLL_STEP(0, 1) POLL_STEP(1, 2) POLL_STEP(2, 3)
               "s_sub_u32 %4, %4, 1\n\ts_cmp_lg_u32 %4, 0\n\ts_cbranch_scc1 .Lpl%=\n"
               ".Lpd%=:"
               : "=&v"(p.v[0]), "=&v"(p.v[1]), "=&v"(p.v[2]), "=&v"(p.v[3]), "+s"(cnt)
               : "v"(off), "s"(ws), "s"(tag), "s"(skip) : "vcc", "scc", "memory");
#undef POLL_STEP
}
DI void poll_done(Poll& p) { asm volatile("" ::"v"(p.v[0]), "v"(p.v[1]), "v"(p.v[2]), "v"(p.v[3])); }
template <int REP> DI void raise_flag(char* ws, u32 off, u32 tag) {
  asm volatile("s_mov_b64 exec, %3\n\tglobal_store_dword %0, %1, %2 sc1\n\ts_mov_b64 exec, -1" ::"v"(off), "v"(tag), "s"(ws), "s"((1ull << REP) - 1) : "memory");
}
template <int REP = 1, int STRIDE = 0, int KEEP = 0> DI void publish(char* ws, u32 off, u32 tag) {
  wait_vm<KEEP>();
  wait_lgkm0(); bar();
  asm volatile("" ::: "memory");
  if (__builtin_amdgcn_readfirstlane(threadIdx.x) == 0) raise_flag<REP>(ws, off + threadIdx.x * STRIDE, tag);
}

struct Sched { int op[96]; int first[20]; };
constexpr Sched make_sched(int n16) {
  Sched r{}; int n = 0;
  for (int k = 0; k < 4; ++k) r.op[n++] = 100 + k;
  r.op[n++] = 0; r.op[n++] = 1; r.op[n++] = 2;
  for (int k = 0; k < 4; ++k) r.op[n++] = 104 + k;
  r.op[n++] = 3; r.op[n++] = 4;
  for (int s = 0; s < 18; ++s) {
    r.first[s + 1] = n;
    const int it = s / 2;
    if (s == 13) { r.op[n++] = 1000; r.op[n++] = 1000; }
    if (s == 0) { r.op[n++] = 5; r.op[n++] = 108; r.op[n++] = 109; r.op[n++] = 6; }
    if (s <= 14) {
      if ((s & 1) == 0) { r.op[n++] = 7 + 3 * it; r.op[n++] = 8 + 3 * it; r.op[n++] = 100 + 4 * (it + 2) + 2; r.op[n++] = 100 + 4 * (it + 2) + 3; }
      else { r.op[n++] = 9 + 3 * it; r.op[n++] = 100 + 4 * (it + 3); r.op[n++] = 100 + 4 * (it + 3) + 1; }
    } else for (int k = 0; k < (s == 15 ? n16 / 100 : s == 16 ? n16 / 10 % 10 : n16 % 10); ++k) r.op[n++] = 1000;
  }
  r.first[19] = n;
  return r;
}
constexpr int vm_need(int n16, int t, int b) {
  const Sched sch = make_sched(n16);
  const int issued = b < 0 ? 7 : sch.first[b + 1];
  int last = -1;
  for (int k = 0; k < issued; ++k) {
    const int op = sch.op[k];
    if (op < 100 ? (8 * op <= 12 * t + 11) : (op < 1000 && (op - 100) / 4 <= t / 2)) last = k;
  }
  return issued - 1 - last;
}

struct Frag { u32x4 x[2][2]; u32x4 w[3][2]; };

DI int p2_start(int i) { return i * 13 + min(i, 2); }
DI int p2_blkstep(int blk) { const int i = blk < 20 ? blk / 5 : (blk - 20) >> 1; return p2_start(i) + (blk < 20 ? blk - 5 * i : 5 + (blk & 1)); }
DI u32 p2_voff(int p5, int l) { const int n = p5 * 64 + l, r = n / 10, c = n - 10 * r; return (u32)((r & 19) | ((r & 4) << 1) | ((r & 8) >> 1)) * 1280u + c * 16; }
DI void p2_piece(const char* wdc, int piece, int l) {
  const int blk = piece / 5;
  dma16(wdc + (size_t)p2_blkstep(blk) * 40960, p2_voff(piece - 5 * blk, l), piece * 1024);
}

template <int TYPE> DI f32x16 p1_mfma(u32x4 w, u32x4 x, f32x16 c) { return TYPE == 2 ? mfma32(x, w, c) : mfma32(w, x, c); }

template <int TYPE, class F> DI void p1_step(f32x16 (&acc)[3][2], const Frag& cur, bool third, F&& fill) {
#pragma unroll
  for (int i = 0; i < 8; ++i) {
    const int b = (i >> 1) & 1, k = i >> 2, t = i & 1;
    acc[b][t] = p1_mfma<TYPE>(cur.w[b][k], cur.x[t][k], acc[b][t]);
    fill(i);
    SB0;
  }
  if (TYPE != 1 || third) {
#pragma unroll
    for (int i = 8; i < 12; ++i) {
      const int k = (i >> 1) & 1, t = i & 1;
      if (TYPE == 2 && t == 1) continue;
      acc[2][t] = p1_mfma<TYPE>(cur.w[2][k], cur.x[t][k], acc[2][t]);
      SB0;
    }
  }
}
template <int I, int N, class F> DI void static_for(F&& f) { if constexpr (I < N) { f(std::integral_constant<int, I>{}); static_for<I + 1, N>(f); } }
constexpr int xslot(int i) { return i % 3 == 0 ? XB0 : (i % 3 == 1 ? XB1 : XB2); }

DI float exp2_le1(float t) { return __builtin_amdgcn_fmed3f(__builtin_amdgcn_exp2f(t), 0.f, 1.f); }
DI float phi_f(float t) { return fmaxf(t, 1e-6f) + exp2_le1(t * LOG2E); }

template <int TYPE>
DI void phase1(int rt, int ct, int j, int c2, const u16* x, const u16* wqkv, const u16* wo, const u16* wg, const u16* wu, const u16* wd, char* ws, u32& tag_out) {
  const int tid = threadIdx.x, l = tid & 63, w = __builtin_amdgcn_readfirstlane(tid >> 6);
  const int tg = w & 3, cgp = w >> 2, h = l >> 5, n = l & 31;
  const int k0 = ABL_NOROT ? 0 : (rt * 10) >> 3;
  const char* xg = (const char*)x + (size_t)rt * 256 * 1280;
  const u32 xvo = (u32)(8 * w + (l >> 3)) * 1280 + (((l & 7) ^ ((4 * (w & 1) + (l >> 4)) & 7)) * 16);
  auto dma_x = [&](int kk, int xi, u32 slot) { dma16(xg + (xi * 64 * 1280 + kk * 128), xvo, slot + xi * 8192 + w * 1024); };
#pragma unroll
  for (int xi = 0; xi < 4; ++xi) dma_x(k0, xi, XB0);
  const char* wb[3]; u32 wvo[3];
  constexpr u32 STRIDE = TYPE == 0 ? 3456 : 3840, WPER = 64 * STRIDE;
  {
    const char* b0 = TYPE == 0 ? (const char*)(wg + 96 * j) : TYPE == 1 ? (const char*)(wqkv + 160 * j) : (const char*)(wqkv + 640 + 80 * j);
    const char* b1 = TYPE == 0 ? (const char*)(wu + 96 * j) : TYPE == 1 ? b0 + 192 : b0 + 1280;
#pragma unroll
    for (int i = 0; i < 3; ++i) {
      const int pp = 8 * i + w, st = pp >= 12, pi = pp - 12 * st, part = pi >= 6;
      const int m = (pi - 6 * part) * 64 + l, row = m / 12, cp = m - 12 * row;
      int ch = part ? cp ^ 2 : cp;
      if (TYPE == 1 && part && ch >= 8) ch -= 4;
      if (TYPE == 2 && ch >= 10) ch -= 4;
      wb[i] = part ? b1 : b0;
      wvo[i] = (u32)(st * 32 + row) * STRIDE + ch * 16;
    }
  }
  const u32 wl0 = L_W + w * 1024, wl1 = w >= 4 ? wl0 - 61440 : wl0;
  auto dma_w = [&](int kk, int grp) { const int pos = (8 * grp) % 60; dma16(wb[grp % 3] + (size_t)((u32)kk * WPER), wvo[grp % 3], (pos == 56 ? wl1 : wl0) + pos * 1024); };
  auto rot = [&](int kk) { return kk + 1 >= 10 ? kk - 9 : kk + 1; };
  const int k1 = rot(k0), k2 = rot(k1);
  dma_w(k0, 0); dma_w(k0, 1); dma_w(k0, 2);
  u32 ep;
  asm volatile("s_load_dword %0, %1, 0x0" : "=s"(ep) : "s"(ws + EP_OFF) : "memory");

  auto bphys = [&](int b) { int v = b + tg; v = v >= 3 ? v - 3 : v; return v >= 3 ? v - 3 : v; };
  u32 xa[2][4], wa[3][2];
  {
    const int ksw = TYPE == 2 ? (tg & 1) : 0;
#pragma unroll
    for (int t = 0; t < 2; ++t) {
      const int row = 64 * tg + 32 * (TYPE == 2 ? t ^ cgp : t) + n;
#pragma unroll
      for (int k = 0; k < 4; ++k) xa[t][k] = row * 128 + 16 * ((2 * (k ^ ksw) + h) ^ ((row >> 1) & 7));
    }
    const int blk = (l >> 4) & 1, q = (l & 15) >> 2, p = l & 3, rowb = L_W + (8 * h + q) * 192;
    if (TYPE == 2) {
      const int a = cgp ? 3 : 0;
      const u32 b0 = rowb + (a + blk) * 32 + 8 * p, b1 = 6144 + rowb + ((a + blk) ^ 1) * 32 + 8 * p;
      const u32 b2 = rowb + (p >> 1) * 6144 + ((4 + (cgp ^ blk)) ^ (p & 2)) * 16 + 8 * (p & 1);
#pragma unroll
      for (int k = 0; k < 2; ++k) { const u32 o = (k ^ ksw) * 3072; wa[0][k] = b0 + o; wa[1][k] = b1 + o; wa[2][k] = b2 + o; }
    } else {
#pragma unroll
      for (int b = 0; b < 3; ++b) {
        const u32 in32 = 16 * (p & 1) + 8 * (p >> 1);
        if (TYPE == 0) wa[b][0] = rowb + blk * 6144 + ((3 * cgp + bphys(b)) ^ blk) * 32 + in32;
        else wa[b][0] = rowb + cgp * 6144 + ((2 * (cgp ? (b == 2 ? 2 : b ^ (tg & 1)) : bphys(b)) + blk) ^ cgp) * 32 + in32;
        wa[b][1] = 0;
      }
    }
  }
  auto rd_w = [&](int b, int k, int off) { return tr_frag((TYPE == 2 ? wa[b][k] : wa[b][0] + k * 3072) + off, 768); };
  auto rd_x = [&](int t, int k4, int off) {
    return TYPE == 2 ? lds_ld<u32x4>(xa[t][k4] + off) : lds_ld<u32x4>(xa[0][k4] + off + t * 4096);
  };
  auto fill_rd = [&](Frag& f, int i, int st) {
    const int wo_ = (st % 5) * 12288, xo = xslot(st / 2), k4 = 2 * (st & 1);
    if (i == 0) { f.w[0][0] = rd_w(0, 0, wo_); f.x[0][0] = rd_x(0, k4, xo); }
    if (i == 1) { f.x[1][0] = rd_x(1, k4, xo); f.w[1][0] = rd_w(1, 0, wo_); }
    if (i == 2) { f.w[0][1] = rd_w(0, 1, wo_); f.x[0][1] = rd_x(0, k4 + 1, xo); }
    if (i == 3) { f.x[1][1] = rd_x(1, k4 + 1, xo); f.w[1][1] = rd_w(1, 1, wo_); }
    if (i == 4) { f.w[2][0] = rd_w(2, 0, wo_); f.w[2][1] = rd_w(2, 1, wo_); }
  };
  f32x16 acc[3][2];
#pragma unroll
  for (int b = 0; b < 3; ++b)
#pragma unroll
    for (int t = 0; t < 2; ++t)
#pragma unroll
      for (int i = 0; i < 16; ++i) acc[b][t][i] = 0.f;
  Frag fa, fb;
  STAMP(1);
  asm volatile("s_waitcnt vmcnt(%1) lgkmcnt(0)" : "+s"(ep) : "n"(vm_need(0, 0, -1)) : "memory");
  bar(); SB0;
  const u32 tag = ep + 1u;
  tag_out = tag;
#pragma unroll
  for (int i = 0; i < 5; ++i) fill_rd(fa, i, 0);
  SB0;
#pragma unroll
  for (int xi = 0; xi < 4; ++xi) dma_x(k1, xi, XB1);
  dma_w(k1, 3); dma_w(k1, 4);
  SB0;
  STAMP(2);
  const bool third = cgp == 0;
  float cs[3] = {0.f, 0.f, 0.f};
  auto colsum = [&](const Frag& f, int i) {
    constexpr u32 ones = 0x3f803f80u;
    if (TYPE == 2) {
      if (i < 6) { const int b = i >> 1; cs[b] = dot2(f.w[b][0][2 * (i & 1)], ones, cs[b]); cs[b] = dot2(f.w[b][0][2 * (i & 1) + 1], ones, cs[b]); asm volatile("" : "+v"(cs[b])); }
    } else { cs[0] = dot2(f.w[0][i >> 2][i & 3], ones, cs[0]); asm volatile("" : "+v"(cs[0])); }
  };
  auto colid = [&](int b) {
    if (TYPE == 2) return b == 2 ? ((n >> 3) & 1) * 96 + 32 + 8 * (cgp ^ (n >> 4)) + (n & 7) : b * 96 + (cgp ? 48 : 0) + n;
    const int p = (n & 15) >> 2, c16 = 8 * (p & 1) + 4 * (p >> 1) + (n & 3);
    if (TYPE == 0) return (n >> 4) * 96 + (3 * cgp + bphys(b)) * 16 + c16;
    return cgp * 96 + (cgp ? (b == 2 ? 2 : b ^ (tg & 1)) : bphys(b)) * 32 + (n & 16) + c16;
  };
  GAS u64* csg = (GAS u64*)(ws + CSG_OFF) + ct * 1536 + rt;
  const rsrc_t rs = __builtin_amdgcn_make_buffer_rsrc(ws, 0, WS_END, 0x00020000);
  int ka = k2, kb = rot(k2);
  const char* wdc = (const char*)(wd + 80 * c2);
  u32x4 gr[2];
  u32 goff[2], pvo[5];
  float gs[2];
  auto gload = [&]() {
#pragma unroll
    for (int k = 0; k < 2; ++k) asm volatile("global_load_dwordx4 %0, %1, %2 sc1" : "=v"(gr[k]) : "v"(goff[k]), "s"(ws) : "memory");
  };
  auto gcheck = [&]() {
    asm volatile("" : "+v"(gr[0]), "+v"(gr[1]));
    return __all(gr[0][1] == tag && gr[0][3] == tag && gr[1][1] == tag && gr[1][3] == tag);
  };
  auto quad_sum = [&](float v) {
    v += __uint_as_float(__builtin_amdgcn_mov_dpp(__float_as_uint(v), 0xB1, 0xf, 0xf, true));
    return v + __uint_as_float(__builtin_amdgcn_mov_dpp(__float_as_uint(v), 0x4E, 0xf, 0xf, true));
  };
  auto pf2 = [&](int base) {
    constexpr int NP = TYPE == 2 ? 100 : 140;
    const bool clip = base + 8 > NP;
    const int piece = base + (clip ? w & 3 : w), blk = piece / 5;
    u32 vo = pvo[base % 5];
    if (clip) { u32 v1 = pvo[(base + 1) % 5]; asm volatile("" : "+v"(v1)); vo = w < 4 ? vo : v1; }
    const int lo = blk / 5, hi = (blk - 20) >> 1;
    const int st = base + 8 <= 100 || clip && base < 100 ? p2_start(lo) + blk - 5 * lo : base >= 100 ? p2_start(hi) + 5 + (blk & 1) : (w < 4 ? 45 : 5);
    dma16(wdc + (u32)st * 40960u, vo, piece * 1024);
  };
  constexpr int N16 = TYPE == 2 ? 1 : 3, NX = TYPE == 2 ? 514 : 434;
  auto bp_of = [&](int b) { return TYPE == 0 ? bphys(b) : (cgp ? (b == 2 ? 2 : b ^ (tg & 1)) : bphys(b)); };
  auto oaddr = [&](int k) {
    const u32 dst = ACT_OFF + (u32)(16 * rt + 4 * tg + (n >> 4)) * (NSTEP * 1024) + (n & 15) * 16;
    if (TYPE == 0) { const int id = 12 * j + 6 * cgp + 2 * bp_of(k >> 1) + h; return dst + (k & 1) * 2 * NSTEP * 1024 + (id >> 2) * 1024 + (id & 3) * 256; }
    const int ch = 4 * (3 * cgp + bp_of(k >> 2)) + 2 * (k & 1) + h, g4 = ch / 5;
    return (k >= 8 && cgp) ? OOB : dst + ((k >> 1) & 1) * 2 * NSTEP * 1024 + (54 + 5 * j + ch - 5 * g4) * 1024 + g4 * 256;
  };
  static_for<0, 18>([&](auto SC) {
    constexpr int S = decltype(SC)::value, IT = S / 2;
    Frag& cur = (S & 1) ? fb : fa; Frag& nxt = (S & 1) ? fa : fb;
    wait_lgkm0(); wait_vm<vm_need(NX, S + 1, S)>(); bar(); SSTAMP(16 + S); SB0;
    p1_step<TYPE>(acc, cur, third, [&](int i) {
      if (!ABL_NOREAD) fill_rd(nxt, i, S + 1);
      if (!ABL_NODMA) {
        if (S == 0) { if (i == 0) dma_w(k1, 5); if (i == 1 || i == 2) dma_x(k2, i - 1, XB2); if (i == 3) dma_w(k2, 6); }
        if (S > 14) { if (S == 16 ? (i >= 5 && i < 5 + N16) : i >= 4) pf2(S == 15 ? 8 * (i - 4) : S == 16 ? 96 + 8 * (i - 5) : 8 * i); }
        else if ((S & 1) == 0) { if (i == 4 || i == 5) dma_w(ka, 3 + 3 * IT + i); if (i >= 6) dma_x(ka, i - 4, xslot(IT + 2)); }
        else { if (i == 5) dma_w(kb, 9 + 3 * IT); if (i >= 6) dma_x(kb, i - 6, xslot(IT)); if (i == 7) { ka = kb; kb = rot(kb); } }
      }
      if (S < 2) colsum(cur, i);
      if ((S == 2 || S == 3) && i == 5 && (rt & 3) == 3) {
#pragma unroll
        for (int e = 0; e < 8; ++e) colsum(cur, e);
      }
      if (S == 4 && i == 2) {
        if (TYPE == 2) {
#pragma unroll
          for (int b = 0; b < 3; ++b) { const float v = xsum32(cs[b]); if (l < 32 && tg < 2) lds_st<float>(L_SP + (tg * 192 + colid(b)) * 4, v); }
        } else {
          const float v = xsum32(cs[0]);
          if (l < 32) __hip_atomic_store(csg + colid(0) * 8, ((u64)tag << 32) | __float_as_uint(v), RLX_AGENT);
        }
      }
      if (TYPE == 2 && S == 5 && i == 2 && tid < 192)
        __hip_atomic_store(csg + tid * 8, ((u64)tag << 32) | __float_as_uint(lds_ld<float>(L_SP + tid * 4) + lds_ld<float>(L_SP + 768 + tid * 4)), RLX_AGENT);
      if (S == 12 && i == 1) {
        const int hi = (TYPE == 1 && tid >= 128) ? tid - 128 : tid;
        goff[0] = CSG_OFF + ct * 12288 + tid * 16;
        goff[1] = w < 4 ? CSG_OFF + ct * 12288 + 8192 + hi * 16 : STG_OFF + 2048 * rt + (tid & 127) * 16;
      }
      if (S == 13 && i == 0) gload();
      if ((S == 13 || S == 14) && i >= 1 && i < (S == 13 ? 4 : 3)) {
        int p5 = w + (S - 13) * 3 + i - 1; p5 = p5 >= 10 ? p5 - 10 : p5 >= 5 ? p5 - 5 : p5; p5 = p5 >= 5 ? p5 - 5 : p5;
        pvo[(S - 13) * 3 + i - 1] = p2_voff(p5, l);
      }
      if (TYPE == 2 && S == 15 && i == 1 && tid < 480) bstore16(u32x4{SENT, SENT, SENT, SENT}, rs, KVR_OFF + (u32)((2 * j + (tid >= 240)) * 8 + rt) * 3840 + (tid >= 240 ? tid - 240 : tid) * 16);
      if (S == 16) {
        if (i == 1) {
          wait_vm<TYPE == 2 ? 12 : 11>();
          if (!gcheck()) {
#pragma unroll 1
            for (int spin = 0; spin < SPIN_CAP; ++spin) { __builtin_amdgcn_s_sleep(2); gload(); wait_vm<0>(); if (gcheck()) break; }
          }
        }
        if (i == 2 || i == 3) gs[i - 2] = quad_sum(__uint_as_float(gr[i - 2][0]) + __uint_as_float(gr[i - 2][2]));
        if (i == 4) lds_st<float>(L_SP + 2048 + (tid >> 2) * 4, gs[0]);
        if (i == 5) {
          if (w < 4) lds_st<float>(L_SP + 2560 + (tid >> 2) * 4, gs[1]);
          else {
            lds_st<f32x2>(L_SP + (tid & 127) * 8, f32x2{bf_lo(gr[1][0]), bf_lo(gr[1][2])});
            lds_st<f32x2>(L_SP + 1024 + (tid & 127) * 8, f32x2{__builtin_amdgcn_rcpf(bf_hi(gr[1][0])), __builtin_amdgcn_rcpf(bf_hi(gr[1][2]))});
          }
        }
      }
    });
  });
  auto mm = [&](int b, int t, int q) { const Frag& f = q < 2 ? fa : fb; acc[b][t] = p1_mfma<TYPE>(f.w[b][q & 1], f.x[t][q & 1], acc[b][t]); };
  wait_lgkm0(); wait_vm<vm_need(NX, 19, 18)>(); bar(); SSTAMP(34); SB0;
  if constexpr (TYPE != 2) {
    float mean[2], rstd[2], ce[2], sd[2];
    f32x4 cv[2][4];
    u32 ob[8];
    auto ld_cv = [&](int b) {
      const u32 a = L_SP + 2048 + (TYPE == 0 ? (3 * cgp + bp_of(b)) * 16 : cgp * 96 + bp_of(b) * 32) * 4 + 32 * h;
#pragma unroll
      for (int u = 0; u < 4; ++u) cv[b & 1][u] = lds_ld<f32x4>(a + (u >> 1) * (TYPE == 0 ? 384 : 64) + (u & 1) * 16);
    };
    auto epi = [&](int e) {
      const int b = e >> 4, t = (e >> 3) & 1, i = e & 7;
      const f32x4 (&c)[4] = cv[b & 1];
      if (TYPE == 0) {
        const float ex = __builtin_amdgcn_exp2f((acc[b][t][i] - mean[t] * c[i >> 2][i & 3]) * ce[t]);
        const float hv = (acc[b][t][8 + i] - mean[t] * c[2 + (i >> 2)][i & 3]) * __builtin_amdgcn_rcpf(ex * sd[t] + sd[t]);
        if (i & 1) ob[i >> 1] = pk_bf16(__uint_as_float(ob[i >> 1]), hv); else ob[i >> 1] = __float_as_uint(hv);
        asm volatile("" : "+v"(ob[i >> 1]));
        if (i == 7) bstore16(u32x4{ob[0], ob[1], ob[2], ob[3]}, rs, oaddr(2 * b + t));
      } else {
        const float u0 = acc[b][t][2 * i] - mean[t] * c[i >> 1][(2 * i) & 3], u1 = acc[b][t][2 * i + 1] - mean[t] * c[i >> 1][(2 * i + 1) & 3];
        ob[i] = pk_bf16(fmaxf(u0, sd[t]) * rstd[t] + exp2_le1(u0 * ce[t]), fmaxf(u1, sd[t]) * rstd[t] + exp2_le1(u1 * ce[t]));
        asm volatile("" : "+v"(ob[i]));
        if ((i & 3) == 3) bstore16(u32x4{ob[i - 3], ob[i - 2], ob[i - 1], ob[i]}, rs, oaddr(4 * b + 2 * t + (i >> 2)));
      }
    };
#pragma unroll
    for (int i = 0; i < 6; ++i) {
      mm(0, i < 4 ? i >> 1 : 0, i < 4 ? i & 1 : i - 2);
      if (i < 5) fill_rd(fb, i == 1 ? 2 : i == 2 ? 1 : i, 19);
      if (i == 0) {
#pragma unroll
        for (int t = 0; t < 2; ++t) { mean[t] = lds_ld<float>(L_SP + (64 * tg + 32 * t + n) * 4); rstd[t] = lds_ld<float>(L_SP + 1024 + (64 * tg + 32 * t + n) * 4); }
      }
      if (i == 2) ld_cv(0);
      if (i == 3) {
#pragma unroll
        for (int t = 0; t < 2; ++t) { ce[t] = (TYPE == 0 ? -LOG2E : LOG2E) * rstd[t]; sd[t] = (TYPE == 0 ? 1.f : 1e-6f) * __builtin_amdgcn_rcpf(rstd[t]); }
      }
      if (i >= 3) pf2(96 + 8 * i);
      if (i == 5) { ld_cv(1); if (tid < 2) lds_st<u32x4>(L_CNT + tid * 16, u32x4{0u, 0u, 0u, 0u}); }
      SB0;
    }
    wait_lgkm0(); bar(); SB0;
#pragma unroll
    for (int i = 0; i < 10; ++i) {
      mm(i < 2 ? 0 : 1, i < 2 ? 1 : (i - 2) >> 2, i < 2 ? 2 + i : (i - 2) & 3);
      epi(2 * i); epi(2 * i + 1);
      if (i < 4) pf2(64 + 8 * i);
      if (i == 7) ld_cv(2);
      SB0;
    }
    if (TYPE == 0 || third) {
#pragma unroll
      for (int i = 0; i < 8; ++i) { mm(2, i >> 2, i & 3); epi(20 + 2 * i); epi(21 + 2 * i); SB0; }
      STAMP(3);
#pragma unroll
      for (int e = 36; e < 48; ++e) epi(e);
    } else {
      STAMP(3);
#pragma unroll
      for (int e = 20; e < 32; ++e) epi(e);
    }
    STAMP(4);
    publish<8, 2048>(ws, FH_OFF + rt * 256 + (TYPE == 0 ? j : 18 + j) * 4, tag);
    STAMP(5);
  } else {
    float cv[3];
    u32x4 xp[3][2][2];
    f32x4 sm[2], sr[2];
    float zs[2] = {0.f, 0.f};
    constexpr int EB[5] = {2, 0, 0, 1, 1}, ET[6] = {0, 0, 1, 0, 1, 0};
    auto ld_st = [&](int p) {
      const u32 a = L_SP + (64 * tg + 32 * (ET[p >> 2] ^ cgp) + 8 * (p & 3) + 4 * h) * 4;
      sm[p & 1] = lds_ld<f32x4>(a); sr[p & 1] = lds_ld<f32x4>(a + 1024);
    };
    auto epi2 = [&](int p) {
      const int b = EB[p >> 2], t = ET[p >> 2], r0 = 4 * (p & 3);
      const f32x4 m = sm[p & 1], r = sr[p & 1];
      float v[4];
#pragma unroll
      for (int e = 0; e < 4; ++e) {
        v[e] = (acc[b][t][r0 + e] - m[e] * cv[b]) * r[e];
        if (b == 0) v[e] = phi_f(v[e]);
        if (b == 2) { const float pv = phi_f(v[e]); v[e] = (n & 8) ? v[e] : pv; }
      }
      if (p + 2 < 20) ld_st(p + 2);
      if (b != 1) { zs[b >> 1] += (v[0] + v[1]) + (v[2] + v[3]); asm volatile("" : "+v"(zs[b >> 1])); }
      u32 o0 = pk_bf16(v[0], v[1]), o1 = pk_bf16(v[2], v[3]);
      asm volatile("" : "+v"(o0), "+v"(o1));
      xp[b][t][(p >> 1) & 1][2 * (p & 1)] = o0; xp[b][t][(p >> 1) & 1][2 * (p & 1) + 1] = o1;
    };
#pragma unroll
    for (int i = 0; i < 6; ++i) {
      mm(i < 2 || i >= 4 ? 2 : 0, 0, i < 4 ? i & 1 : i - 2);
      if (i < 5) fill_rd(fb, i == 0 ? 4 : i == 1 ? 0 : i == 2 ? 2 : i == 3 ? 1 : 3, 19);
      if (i == 3) {
#pragma unroll
        for (int b = 0; b < 3; ++b) cv[b] = lds_ld<float>(L_SP + 2048 + colid(b) * 4);
      }
      if (i >= 4) ld_st(i - 4);
      SB0;
    }
#pragma unroll
    for (int i = 0; i < 6; ++i) { mm(0, i >= 2, i < 2 ? 2 + i : i - 2); epi2(i); SB0; }
    if (n >= 16) {
#pragma unroll
      for (int k = 0; k < 2; ++k) lds_st<u32x4>(L_ATT + ((w * 2 + k) * 32 + (n - 16) + 16 * h) * 16, xp[2][0][k]);
    }
    publish<1, 0, 5>(ws, FK_OFF + j * 256 + rt * 4, tag); SB0;
#pragma unroll
    for (int k = 0; k < 2; ++k) xp[2][1][k] = lds_ld<u32x4>(L_ATT + (((w ^ 4) * 2 + k) * 32 + (n & 15) + 16 * h) * 16);
    f32x16 z16;
#pragma unroll
    for (int i = 0; i < 16; ++i) z16[i] = 0.f;
    f32x16 pmk = z16, pmm = z16, pvk = z16, pvm = z16;
    constexpr u32 IMG = L_ATT + 8192, DUMP = IMG + 8 * 3840;
    const int lo8 = 8 * cgp, mix = 32 - 32 * cgp;
    u32 avk[4], amk, avm[2], amm;
    auto img_at = [&](int kc, int vc, bool on) { return (on && (kc >= 20) == (vc >= 20)) ? IMG + w * 3840 + kc * 96 + (vc >= 20 ? vc - 20 : vc) * 4 : DUMP + l * 16; };
#pragma unroll
    for (int i = 0; i < 8; ++i) {
      mm(1, i >> 2, i & 3); epi2(6 + i);
      if (i < 4) pf2(64 + 8 * i);
      if (i >= 2 && i < 6) pmm = mfma32(xp[2][(i - 2) >> 1][i & 1], xp[2][(i - 2) >> 1][i & 1], pmm);
      if (i == 6) { amm = img_at(mix + n, mix + 4 * h, n < 8); amk = img_at(lo8 + n, mix + 4 * h, true); asm volatile("" : "+v"(amm), "+v"(amk)); }
      if (i == 7) {
#pragma unroll
        for (int q = 0; q < 2; ++q) { avm[q] = img_at(mix + n, lo8 + 16 * (1 - cgp) + 8 * q + 4 * h, n < 8); asm volatile("" : "+v"(avm[q])); }
      }
      SB0;
    }
    STAMP(3);
#pragma unroll
    for (int i = 0; i < 6; ++i) {
      if (i < 4) pmk = mfma32(xp[2][i >> 1][i & 1], xp[0][i >> 1][i & 1], pmk);
      if (i >= 2 && i < 4) { pvk = mfma32(xp[1][0][i & 1], xp[0][0][i & 1], pvk); pvm = mfma32(xp[1][0][i & 1], xp[2][0][i & 1], pvm); }
      epi2(14 + i);
      if (i < 4) { avk[i] = img_at(lo8 + n, lo8 + 8 * i + 4 * h, true); asm volatile("" : "+v"(avk[i])); }
      if (i == 1) lds_st<f32x4>(amm, f32x4{pmm[4], pmm[5], pmm[6], pmm[7]});
      if (i == 4) {
        const float z0 = xsum32(zs[0]), z1 = xsum32(zs[1]);
        lds_st<float>(h == 0 ? IMG + w * 3840 + (lo8 + n) * 96 + 80 : (n & 8) ? DUMP + l * 16 : (n < 8 ? IMG + w * 3840 + (mix + n) * 96 + 80 : IMG + (w ^ 4) * 3840 + (32 - mix + n - 16) * 96 + 84), h == 0 ? z0 : z1);
      }
      if (i == 5) lds_st<f32x4>(amk, f32x4{pmk[4], pmk[5], pmk[6], pmk[7]});
      SB0;
    }
#pragma unroll
    for (int k = 0; k < 2; ++k) { pvk = mfma32(xp[1][1][k], xp[0][1][k], pvk); pvm = mfma32(xp[1][1][k], xp[2][1][k], pvm); }
    if (tid < 2) lds_st<u32x4>(L_CNT + tid * 16, u32x4{0u, 0u, 0u, 0u});
#pragma unroll
    for (int q = 0; q < 4; ++q) lds_st<f32x4>(avk[q], f32x4{pvk[4 * q], pvk[4 * q + 1], pvk[4 * q + 2], pvk[4 * q + 3]});
#pragma unroll
    for (int q = 0; q < 2; ++q) lds_st<f32x4>(avm[q], cgp ? f32x4{pvm[4 * q], pvm[4 * q + 1], pvm[4 * q + 2], pvm[4 * q + 3]} : f32x4{pvm[8 + 4 * q], pvm[9 + 4 * q], pvm[10 + 4 * q], pvm[11 + 4 * q]});
    __syncthreads();
    if (tid < 480) {
      const int cg = tid >= 240, o = (tid - 240 * cg) * 16;
      f32x4 sum = lds_ld<f32x4>(IMG + cg * 15360 + o);
#pragma unroll
      for (int k = 1; k < 4; ++k) sum += lds_ld<f32x4>(IMG + cg * 15360 + k * 3840 + o);
      bstore16(nosent4(__builtin_bit_cast(u32x4, sum)), rs, KVR_OFF + (u32)((2 * j + cg) * 8 + rt) * 3840 + o);
    }
    STAMP(4);
    wait_vm<1>(); wait_lgkm0(); bar();
    STAMP(5);
  }
}

DI void lds_ticket(u32 a) {
  asm volatile("s_mov_b64 exec, 1\n\tds_add_u32 %0, %1\n\ts_mov_b64 exec, -1" ::"v"(a), "v"(1u) : "memory");
}
DI void lds_wait(u32 a, u32 want) {
#pragma unroll 1
  for (int spin = 0; spin < SPIN_CAP * 16; ++spin) {
    if (__builtin_amdgcn_readfirstlane(__hip_atomic_load((LAS u32*)(size_t)a, __ATOMIC_RELAXED, __HIP_MEMORY_SCOPE_WORKGROUP)) >= want) break;
    __builtin_amdgcn_s_sleep(2);
  }
  asm volatile("" ::: "memory");
}

DI void lds_meet(u32 a, u32 want) { asm volatile("s_waitcnt lgkmcnt(0)" ::: "memory"); lds_ticket(a); lds_wait(a, want); }

DI void mjob(int pair, u32 tag, char* ws, rsrc_t rs, const u32x4 (&WO)[5][2]) {
  int tid = threadIdx.x;
  asm volatile("" : "+v"(tid));
  const int l = tid & 63, w = __builtin_amdgcn_readfirstlane(tid >> 6);
  constexpr u32 KVL = L_MJ;
  __builtin_amdgcn_s_setprio(3);
  STAMP(5);
  if (w < 4) {
    wait_flags<16>(ws, FK_OFF + (pair >> 1) * 256 + (l & 7) * 4, l < 8, tag);
    const int t4 = min(tid, 239);
    u32x4 pa[8];
#pragma unroll 1
    for (int spin = 0; spin < SPIN_CAP; ++spin) {
      u32 mx = 0;
#pragma unroll
      for (int k = 0; k < 8; ++k) pa[k] = bload(rs, KVR_OFF + (u32)pair * 30720 + t4 * 16, k * 3840);
#pragma unroll
      for (int k = 0; k < 8; ++k) mx = max4(mx, pa[k]);
      if (!__any(mx == SENT)) break;
      __builtin_amdgcn_s_sleep(4);
      asm volatile("" ::: "memory");
    }
    f32x4 sum = __builtin_bit_cast(f32x4, pa[0]);
#pragma unroll
    for (int k = 1; k < 8; ++k) sum += __builtin_bit_cast(f32x4, pa[k]);
    const bool zc = t4 % 6 == 5;
    lds_st<u32x2>(KVL + t4 * 8, zc ? u32x2{0u, 0u} : u32x2{pk_bf16(sum[0], sum[1]), pk_bf16(sum[2], sum[3])});
    if (zc) { const float z = sum[0] + (((pair & 1) ? t4 < 48 : t4 >= 192) ? sum[1] : 0.f); lds_st<u16>(KVL + 1920 + (t4 / 6) * 2, (u16)pk_bf16(z, z)); }
  }
  wait_lgkm0(); bar();
  asm volatile("" ::: "memory");
  if (w >= 5) { const int e = tid - 320; bstore16(nosent4(lds_ld<u32x4>(KVL + 1920 + (e % 5) * 16)), rs, e < 160 ? ZB_OFF + (e / 5) * 1280 + pair * 80 + (e % 5) * 16 : OOB); }
  STAMP(6);
  const int g = l >> 4, c = l & 15;
  const u32x4 zero4 = {0u, 0u, 0u, 0u};
  const int kx = 8 * (c >> 2) + (c & 3), ky = kx + 4, kz = 32 + (c & 7), go = min(g, 2) * 16;
  const u32x4 rx = lds_ld<u32x4>(KVL + kx * 48 + go), ry = lds_ld<u32x4>(KVL + ky * 48 + go), rz = lds_ld<u32x4>(KVL + kz * 48 + go);
  const u32x4 ax0 = (kx < 20 && g < 3) ? rx : zero4, ax1 = (kx >= 20 && g < 3) ? rx : zero4, ay0 = (ky < 20 && g < 3) ? ry : zero4, ay1 = (ky >= 20 && g < 3) ? ry : zero4, az = g < 3 ? rz : zero4;
  const u32 mo = MT_OFF + (u32)(100 * w + 5 * (pair >> 2)) * 1024 + (c + 16 * (pair & 3)) * 16;
  f32x4 dx[5], dy[5], dz[5];
#pragma unroll
  for (int cb = 0; cb < 5; ++cb) { const f32x4 z4 = {0.f, 0.f, 0.f, 0.f}; dx[cb] = mfma16(ax0, WO[cb][0], z4); dy[cb] = mfma16(ay0, WO[cb][0], z4); dz[cb] = mfma16(az, WO[cb][1], z4); }
#pragma unroll
  for (int cb = 0; cb < 5; ++cb) { dx[cb] = mfma16(ax1, WO[cb][1], dx[cb]); dy[cb] = mfma16(ay1, WO[cb][1], dy[cb]); }
  SB0;
#pragma unroll
  for (int cb = 0; cb < 5; ++cb) {
    bstore16(nosent4(u32x4{pk_bf16(dx[cb][0], dx[cb][1]), pk_bf16(dx[cb][2], dx[cb][3]), pk_bf16(dy[cb][0], dy[cb][1]), pk_bf16(dy[cb][2], dy[cb][3])}), rs, mo + g * 1024 + cb * 20480);
    bstore8(u32x2{nosent(pk_bf16(dz[cb][0], dz[cb][1])), nosent(pk_bf16(dz[cb][2], dz[cb][3]))}, rs, g < 2 ? mo + 4096 + 8 * g + cb * 20480 : OOB);
  }
  __builtin_amdgcn_s_setprio(0);
  STAMP(7);
}

DI int wt_step(int i) { const int q = i < 7 ? 0 : i < 14 ? 1 : i < 20 ? 2 : 3; return i + 7 * (q + 1); }

DI void spare(int sg, int c2, const u16* x, const u16* wo, const u16* wd, char* ws, u32& tag_out, u32x4 (&WO)[5][2]) {
  int tid = threadIdx.x;
  asm volatile("" : "+v"(tid));
  const int l = tid & 63, w = __builtin_amdgcn_readfirstlane(tid >> 6);
  u32x4 v[4][5];
  const int row0 = 128 * sg + 16 * w + (l >> 4);
#pragma unroll
  for (int a = 0; a < 4; ++a)
#pragma unroll
    for (int k = 0; k < 5; ++k) v[a][k] = *(const u32x4*)((const char*)x + (size_t)(row0 + 4 * a) * 1280 + k * 256 + (l & 15) * 16);
  u32 ep;
  asm volatile("s_load_dword %0, %1, 0x0\n\ts_waitcnt lgkmcnt(0)" : "=s"(ep) : "s"(ws + EP_OFF) : "memory");
  const u32 tag = ep + 1u;
  tag_out = tag;
  const int nst = sg < 10 ? 2 : 1;
#pragma unroll
  for (int a = 0; a < 4; ++a) {
    float s = 0.f, q = 0.f;
#pragma unroll
    for (int k = 0; k < 5; ++k)
#pragma unroll
      for (int e = 0; e < 4; ++e) { s = dot2(v[a][k][e], 0x3f803f80u, s); q = dot2(v[a][k][e], v[a][k][e], q); }
#pragma unroll
    for (int o = 1; o < 16; o <<= 1) { s += __shfl_xor(s, o); q += __shfl_xor(q, o); }
    const float mean = s * (1.f / 640.f);
    const float sd = __builtin_amdgcn_sqrtf(round_bf(round_bf(fmaxf(q * (1.f / 640.f) - mean * mean, 0.f)) + 1e-5f));
    if ((l & 15) == 0) __hip_atomic_store((GAS u64*)(ws + STG_OFF) + row0 + 4 * a, ((u64)tag << 32) | pk_bf16(mean, sd), RLX_AGENT);
  }
  STAMP(1);
#pragma unroll 1
  for (int a = 0; a < nst; ++a)
#pragma unroll
    for (int i = 0; i < 5; ++i) dma16((const char*)wd + (size_t)wt_step(sg + 16 * a) * 40960 + (w * 5 + i) * 1024, l * 16, a * 40960 + (w * 5 + i) * 1024);
#pragma unroll 1
  for (int pc = w; pc < 50; pc += 8) dma16((const char*)(wo + (size_t)sg * 40 * 640) + pc * 1024, l * 16, 81920 + pc * 1024);
  wait_vm<0>();
  __syncthreads();
  STAMP(2);
  const rsrc_t rs = __builtin_amdgcn_make_buffer_rsrc(ws, 0, WS_END, 0x00020000);
#pragma unroll 1
  for (int a = 0; a < nst; ++a)
#pragma unroll
    for (int i = 0; i < 5; ++i) {
      const int cb = w * 5 + i;
      const u32x4 f = tr_frag(a * 40960 + (8 * (l >> 4) + ((l & 15) >> 2)) * 1280 + cb * 32 + 8 * (l & 3), 5120);
      bstore16(f, rs, WT_OFF + (u32)(cb * 54 + wt_step(sg + 16 * a)) * 1024 + l * 16);
    }
  {
    const u32 ra = 81920 + min(8 * (l >> 4) + ((l & 15) >> 2), 19) * 1280 + w * 160 + 8 * (l & 3), rb = 81920 + min(8 * (l >> 4) + 4 + ((l & 15) >> 2), 19) * 1280 + w * 160 + 8 * (l & 3);
    const u32 mo = MT_OFF + (u32)(100 * w + 5 * (sg >> 2)) * 1024 + ((l & 15) + 16 * (sg & 3)) * 16;
#pragma unroll
    for (int cb = 0; cb < 5; ++cb) {
#pragma unroll
      for (int hh = 0; hh < 2; ++hh) { const u32x2 lo = tr_read(ra + cb * 32 + hh * 25600), hi = tr_read(rb + cb * 32 + hh * 25600); WO[cb][hh] = u32x4{lo[0], lo[1], hi[0], hi[1]}; }
      bstore16(u32x4{SENT, SENT, SENT, SENT}, rs, mo + (l >> 4) * 1024 + cb * 20480);
      bstore16(u32x4{SENT, SENT, SENT, SENT}, rs, l < 16 ? mo + 4096 + cb * 20480 : OOB);
    }
    if (w >= 5) { const int e = tid - 320; bstore16(u32x4{SENT, SENT, SENT, SENT}, rs, e < 160 ? ZB_OFF + (e / 5) * 1280 + sg * 80 + (e % 5) * 16 : OOB); }
  }
  STAMP(3);
  publish<8, 256>(ws, FW_OFF + sg * 4, tag);
  STAMP(4);
#pragma unroll 1
  for (int pc = w; pc < 140; pc += 8) p2_piece((const char*)(wd + 80 * c2), pc, l);
  if (tid < 2) lds_st<u32x4>(L_CNT + tid * 16, u32x4{0u, 0u, 0u, 0u});
  if (sg == 0 && w < 4) {
    const u32 off = tid < 240 ? CSG_OFF + (tid >> 3) * 12288 + (tid & 7) * 8 + 4 : STG_OFF + (tid - 240) * 1024 + 4;
    wait_flags(ws, off, true, tag);
  }
  wait_vm<0>();
  __syncthreads();
  if (sg == 0 && tid == 0) __hip_atomic_store((GAS u32*)(ws + EP_OFF), tag, RLX_AGENT);
}

DI u32 p2_attblk(int u, int j) { return j < 10 ? L_ATT + u * 10240 + j * 1024 : u * 25600 + 20480 + (j - 10) * 1024; }
constexpr int h_step(int t) { return t < 4 ? 7 + t : t < 11 ? t - 4 : t; }
constexpr int a_first(int t) { return t < 0 ? 0 : t < 4 ? 4 + 2 * t : t < 6 ? 8 + t : 14; }

template <int T> DI void p2_hsteps(f32x4 (&acc)[5][4], u32x4 (&A)[14][4], u32x4 (&B)[4][5], u32x4& bl, rsrc_t rs, u32 va, const u32 (&so)[4], u32 vb, u32 wl, u32 wl2, bool extra, Poll& pl, const u16* xp, u32x2 (&xr)[5]) {
  if constexpr (T < 14) {
#pragma unroll
    for (int cb = 0; cb < 5; ++cb) {
      const u32x4 bc = (T >= 4 && T < 11) ? bl : B[T < 4 ? T : T - 11][cb];
      const int nt = (cb < 4 ? T : T + 1) - 4, nc = cb < 4 ? cb + 1 : 0;
      if (nt >= 0 && nt < 7) bl = tr_frag((nt < 5 ? wl + nt * 5120 : wl2 + (nt - 5) * 5120) + nc * 32, 1280);
#pragma unroll
      for (int t = 0; t < 4; ++t) acc[cb][t] = mfma16(bc, A[T][t], acc[cb][t]);
      SB0;
    }
    if constexpr (T == 0) poll_done(pl);
    if constexpr (T == 9) {
#pragma unroll
      for (int cb = 0; cb < 5; ++cb) xr[cb] = *(const u32x2*)(xp + 16 * cb);
    }
    if constexpr (T == 10) lds_ticket(L_CNT);
#pragma unroll
    for (int n = a_first(T); n < a_first(T + 1); ++n)
#pragma unroll
      for (int t = 0; t < 4; ++t) A[n][t] = bload(rs, n == 13 ? (extra ? va : OOB) : va, so[t] + h_step(n) * 1024);
    if constexpr (T >= 6 && T < 9) {
#pragma unroll
      for (int cb = 0; cb < 5; ++cb) B[T - 6][cb] = bload(rs, T == 8 ? (extra ? vb : OOB) : vb, cb * 55296 + (T + 5) * 1024);
    }
    SB0;
    p2_hsteps<T + 1>(acc, A, B, bl, rs, va, so, vb, wl, wl2, extra, pl, xp, xr);
  }
}

template <bool JOB> DI void phase2(int r, int c, int job, bool late, u32 tag, const u16* x, const u16* wd, u16* out, char* ws, const u32x4 (&WO)[5][2]) {
  int tid = threadIdx.x;
  asm volatile("" : "+v"(tid), "+s"(ws), "+s"(tag));
  const int l = tid & 63, w = __builtin_amdgcn_readfirstlane(tid >> 6);
  const rsrc_t rs = __builtin_amdgcn_make_buffer_rsrc(ws, 0, WS_END, 0x00020000);
  const int g = l >> 4, n = l & 15;
  auto orow = [&]() { int t = threadIdx.x; asm volatile("" : "+v"(t)); return (u32)((64 * r + 16 * (w & 3) + (t & 15)) * 640 + 80 * c + ((t >> 2) & 12)); };
  const u32 fh = FH_OFF + c * 2048 + (r >> 2) * 256;
  STAMP(8);
  if (w < 4) {
    if (late) {
#pragma unroll 1
      for (int pc = 0; pc < 10; ++pc) p2_piece((const char*)(wd + 80 * c), 100 + 10 * w + pc, l);
    }
    const int start = p2_start(w);
    const u32 vb = WT_OFF + (u32)(5 * c * 54 + start) * 1024u + l * 16;
    u32x4 B[4][5];
    wait_flags<24>(ws, FW_OFF + c * 256 + n * 4, l < 16, tag);
#pragma unroll
    for (int s = 0; s < 4; ++s) {
#pragma unroll
      for (int cb = 0; cb < 5; ++cb) B[s][cb] = bload(rs, vb, cb * 55296 + (7 + s) * 1024);
      SB0;
    }
    const u32 va = ACT_OFF + (u32)(4 * r * NSTEP + start) * 1024u + l * 16;
    u32 so[4];
#pragma unroll
    for (int t = 0; t < 4; ++t) so[t] = ((t + w) & 3) * (NSTEP * 1024);
    const u32 wi = (16 * (g >> 1) + 4 * (g & 1) + (n >> 2)) * 160 + 8 * (l & 3), wl = w * 25600 + wi, wl2 = L_ATT + w * 10240 + wi;
    u32x4 A[14][4], bl;
    if (JOB) mjob(job, tag, ws, rs, WO);
    Poll pl = {};
    if (w == 0) { poll_flags(pl, ws, fh + min(l, 17) * 4, true, tag); lds_ticket(L_CNT + 16); } else lds_wait(L_CNT + 16, 1);
    STAMP(9);
#pragma unroll
    for (int s = 0; s < 4; ++s) {
#pragma unroll
      for (int t = 0; t < 4; ++t) A[s][t] = bload(rs, va, so[t] + h_step(s) * 1024);
      SB0;
    }
    f32x4 acc[5][4];
#pragma unroll
    for (int cb = 0; cb < 5; ++cb)
#pragma unroll
      for (int t = 0; t < 4; ++t) acc[cb][t] = f32x4{0.f, 0.f, 0.f, 0.f};
    const u32 oo = (u32)((64 * r + 16 * w + n) * 640 + 80 * c + 4 * g);
    u32x2 xr[5];
    p2_hsteps<0>(acc, A, B, bl, rs, va, so, vb, wl, wl2, w < 2, pl, x + oo, xr);
    STAMP(10);
#pragma unroll
    for (int cb = 0; cb < 5; ++cb)
#pragma unroll
      for (int t = 1; t < 4; ++t) lds_st<f32x4>(w * 25600 + (cb * 3 + (t - 1)) * 1024 + l * 16, acc[cb][t]);
#pragma unroll
    for (int cb = 0; cb < 5; ++cb) { acc[cb][0] += f32x4{bf_lo(xr[cb][0]), bf_hi(xr[cb][0]), bf_lo(xr[cb][1]), bf_hi(xr[cb][1])}; asm volatile("" : "+v"(acc[cb][0])); }
    lds_meet(L_CNT + 8, 4);
#pragma unroll
    for (int cb = 0; cb < 5; ++cb) {
#pragma unroll
      for (int o = 1; o < 4; ++o) acc[cb][0] += lds_ld<f32x4>(((w + o) & 3) * 25600 + (cb * 3 + (3 - o)) * 1024 + l * 16);
      asm volatile("" : "+v"(acc[cb][0]));
    }
    STAMP(11);
    __syncthreads();
    STAMP(12);
#pragma unroll
    for (int cb = 0; cb < 5; ++cb) {
      f32x4 s = acc[cb][0];
      s += lds_ld<f32x4>(w * 25600 + (15 + cb) * 1024 + l * 16);
#pragma unroll
      for (int o = 1; o < 4; ++o) s += lds_ld<f32x4>(p2_attblk((w + o) & 3, cb * 3 + (3 - o)) + l * 16);
      *(u32x2*)(out + oo + 16 * cb) = u32x2{pk_bf16(s[0], s[1]), pk_bf16(s[2], s[3])};
    }
    STAMP(13);
    __builtin_amdgcn_endpgm();
  } else {
    const int u = w - 4;
    u32x4 P[4][5];
    auto load_p = [&]() {
#pragma unroll
      for (int t = 0; t < 4; ++t) {
#pragma unroll
        for (int s = 0; s < 5; ++s) P[t][s] = bload(rs, ACT_OFF + (u32)(4 * r * NSTEP + 54 + 5 * u) * 1024u + l * 16, ((t + u) & 3) * (NSTEP * 1024) + s * 1024);
        SB0;
      }
    };
    __builtin_amdgcn_s_setprio(2);
    bool havep = true;
    if (JOB) {
#pragma unroll 1
      for (int spin = 0; spin < SPIN_CAP; ++spin) {
        const u32 v = __builtin_amdgcn_raw_buffer_load_b32(rs, l == 0 ? fh + (18 + u) * 4 : KVR_OFF + (u32)(job * 8 + (l & 7)) * 3840 + 3836, 0, 16);
        havep = __builtin_amdgcn_readfirstlane(v) == tag;
        if (havep || (__ballot(v == SENT) >> 8 & 0xffu) == 0) break;
        __builtin_amdgcn_s_sleep(16);
        asm volatile("" ::: "memory");
      }
      if (!havep) mjob(job, tag, ws, rs, WO);
    }
    if (!JOB || !havep) {
      if (JOB || u == 0) wait_flags<8>(ws, l < 16 ? FW_OFF + c * 256 + l * 4 : fh + (18 + (l & 3)) * 4, l < 20, tag);
      if (!JOB) { if (u == 0) lds_ticket(L_CNT + 20); else lds_wait(L_CNT + 20, 1); }
    }
    load_p();
    __builtin_amdgcn_s_setprio(0);
    if (JOB && havep) { mjob(job, tag, ws, rs, WO); wait_flags<8>(ws, FW_OFF + c * 256 + n * 4, l < 16, tag); }
    STAMP(9);
    STAMP(14);
    const u32 mhint = MT_OFF + (u32)((5 * c + 4) * 20 + 5 * u + 4) * 1024u + g * 256 + 252;
    float rd[4][2];
    bool early;
    {
      u32x4 Z[5];
#pragma unroll 1
      for (int spin = 0; spin < SPIN_CAP; ++spin) {
        u32 mx = 0;
#pragma unroll
        for (int s = 0; s < 5; ++s) Z[s] = bload(rs, ZB_OFF + r * 1280 + 320 * u + 80 * g, s * 16);
        early = !__any(__builtin_amdgcn_raw_buffer_load_b32(rs, mhint, 0, 16) == SENT);
#pragma unroll
        for (int s = 0; s < 5; ++s) mx = max4(mx, Z[s]);
        if (!__any(mx == SENT)) break;
        __builtin_amdgcn_s_sleep(10);
        asm volatile("" ::: "memory");
      }
      __builtin_amdgcn_s_setprio(2);
      const bool m0 = n == 4 * g, m1 = n == 4 * g + 1;
#pragma unroll
      for (int s = 0; s < 5; ++s)
#pragma unroll
        for (int q = 0; q < 4; ++q) Z[s][q] = ((8 * s + 2 * q >= 20) ? m1 : m0) ? Z[s][q] : 0u;
#pragma unroll
      for (int t = 0; t < 4; ++t) {
        f32x4 den = {0.f, 0.f, 0.f, 0.f};
#pragma unroll
        for (int s = 0; s < 5; ++s) den = mfma16(Z[s], P[t][s], den);
        rd[t][0] = __builtin_amdgcn_rcpf(den[0] + 1e-6f); rd[t][1] = __builtin_amdgcn_rcpf(den[1] + 1e-6f);
      }
    }
    auto scale = [&]() {
#pragma unroll
      for (int t = 0; t < 4; ++t)
#pragma unroll
        for (int s = 0; s < 5; ++s) {
#pragma unroll
          for (int q = 0; q < 4; ++q) { const float rr = rd[t][8 * s + 2 * q >= 20]; P[t][s][q] = pk_bf16(bf_lo(P[t][s][q]) * rr, bf_hi(P[t][s][q]) * rr); }
          asm volatile("" : "+v"(P[t][s]));
        }
    };
    STAMP(11);
    if (!early) { scale(); wait_data<4>(rs, mhint); }
    STAMP(10);
    u32x4 M[5][5];
    const u32 vm = MT_OFF + (u32)(5 * c * 20 + 5 * u) * 1024u + l * 16;
    auto load_m = [&](int c0) {
#pragma unroll
      for (int cb = 0; cb < 5; ++cb) {
        if (cb < c0) continue;
#pragma unroll
        for (int s = 0; s < 5; ++s) M[cb][s] = bload(rs, vm, (cb * 20 + s) * 1024);
        SB0;
      }
    };
    load_m(0);
    if (early) scale();
    lds_wait(L_CNT, 4);
    STAMP(17);
#pragma unroll
    for (int cb = 0; cb < 5; ++cb) {
      auto bad = [&]() {
        u32 mx = 0;
#pragma unroll
        for (int s = 0; s < 5; ++s) mx = max4(mx, M[cb][s]);
        return __any(mx == SENT);
      };
      if (__builtin_expect(bad(), 0)) {
#pragma unroll 1
        for (int spin = 0; spin < SPIN_CAP; ++spin) { asm volatile("" ::: "memory"); load_m(cb); if (!bad()) break; }
      }
      f32x4 a[4];
#pragma unroll
      for (int t = 0; t < 4; ++t) a[t] = f32x4{0.f, 0.f, 0.f, 0.f};
#pragma unroll
      for (int s = 0; s < 5; ++s)
#pragma unroll
        for (int t = 0; t < 4; ++t) a[t] = mfma16(M[cb][s], P[t][s], a[t]);
      lds_st<f32x4>(u * 25600 + (15 + cb) * 1024 + l * 16, a[0]);
#pragma unroll
      for (int t = 1; t < 4; ++t) lds_st<f32x4>(p2_attblk(u, cb * 3 + (t - 1)) + l * 16, a[t]);
    }
    STAMP(15);
    __syncthreads();
    __builtin_amdgcn_endpgm();
  }
}

__host__ __device__ inline void decode(int bid, int& rt, int& ct) {
  const int xcd = bid & 7, idx = bid >> 3, cgrp = xcd >> 1, ci = idx >> 2;
  rt = 4 * (xcd & 1) + (idx & 3);
  ct = cgrp < 2 ? cgrp * 8 + ci : ci == 7 ? 30 : ci < 4 ? 18 + 4 * (cgrp - 2) + ci : ci < 6 ? 26 + 2 * (cgrp - 2) + ci - 4 : 16 + (cgrp - 2);
  if (ct == 30) rt += 8 * (cgrp - 2);
}

__global__ __launch_bounds__(512, 2) void fused_kernel(const u16* __restrict__ x, const u16* __restrict__ wqkv, const u16* __restrict__ wo,
                                                       const u16* __restrict__ wg, const u16* __restrict__ wu, const u16* __restrict__ wd,
                                                       u16* __restrict__ out, char* ws) {
  STAMP(0);
  const int bid = blockIdx.x, xcd = bid & 7, idx = bid >> 3;
  const int r2 = xcd * 4 + (idx >> 3), c2 = idx & 7;
  int rt, ct;
  decode(bid, rt, ct);
  u32 tag;
  u32x4 WO[5][2];
  if (ct == 30) {
    spare(rt, c2, x, wo, wd, ws, tag, WO);
    phase2<true>(r2, c2, rt, false, tag, x, wd, out, ws, WO);
  } else {
    if (ct < 18) phase1<0>(rt, ct, ct, c2, x, wqkv, wo, wg, wu, wd, ws, tag);
    else if (ct < 26) phase1<2>(rt, ct, ct - 18, c2, x, wqkv, wo, wg, wu, wd, ws, tag);
    else phase1<1>(rt, ct, ct - 26, c2, x, wqkv, wo, wg, wu, wd, ws, tag);
    phase2<false>(r2, c2, -1, ct >= 18 && ct < 26, tag, x, wd, out, ws, WO);
  }
}

extern "C" void kernel_launch(void* const* d_in, const int* in_sizes, int n_in, void* d_out, int out_size, void* d_ws, size_t ws_size, hipStream_t stream) {
  (void)hipFuncSetAttribute((const void*)fused_kernel, hipFuncAttributeMaxDynamicSharedMemorySize, LDS_BYTES);
  fused_kernel<<<256, 512, LDS_BYTES, stream>>>((const u16*)d_in[0], (const u16*)d_in[1], (const u16*)d_in[2], (const u16*)d_in[4], (const u16*)d_in[5],
                                                (const u16*)d_in[6], (u16*)d_out, (char*)d_ws);
}
```

```cpp
#include <hip/hip_runtime.h>
#include <stdint.h>
#include <type_traits>

#define ABL_NOREAD 0
#define ABL_NODMA 0
#define APRIO 0
#define HPRIO 1
#define MAPPING 2
#define ABL_NOROT 0
typedef unsigned short u16;
typedef unsigned int u32;
typedef unsigned long long u64;
typedef __bf16 bf16x8 __attribute__((ext_vector_type(8)));
typedef __bf16 bf16x2 __attribute__((ext_vector_type(2)));
typedef short s16x4 __attribute__((ext_vector_type(4)));
typedef float f32x2 __attribute__((ext_vector_type(2)));
typedef float f32x4 __attribute__((ext_vector_type(4)));
typedef float f32x16 __attribute__((ext_vector_type(16)));
typedef u32 u32x2 __attribute__((ext_vector_type(2)));
typedef u32 u32x4 __attribute__((ext_vector_type(4)));
typedef __amdgpu_buffer_rsrc_t rsrc_t;
#define LAS __attribute__((address_space(3)))
#define GAS __attribute__((address_space(1)))
#define DI __device__ __forceinline__
#define RLX_AGENT __ATOMIC_RELAXED, __HIP_MEMORY_SCOPE_AGENT
#define SB0 __builtin_amdgcn_sched_barrier(0)

constexpr int NSTEP = 74;
constexpr u32 ACT_OFF = 0;
constexpr u32 MT_OFF  = ACT_OFF + 128u * NSTEP * 1024u;
constexpr u32 WT_OFF  = MT_OFF + 40u * 20u * 1024u;
constexpr u32 KVR_OFF = WT_OFF + 40u * 54u * 1024u;
constexpr u32 ZB_OFF  = KVR_OFF + 16u * 8u * 3840u;
constexpr u32 STG_OFF = ZB_OFF + 32u * 1280u;
constexpr u32 CSG_OFF = STG_OFF + 2048u * 8u;
constexpr u32 FH_OFF  = CSG_OFF + 30u * 8u * 192u * 8u;
constexpr u32 FK_OFF  = FH_OFF + 64u * 256u;
constexpr u32 FW_OFF  = FK_OFF + 8u * 256u;
constexpr u32 EP_OFF  = FW_OFF + 8u * 256u;
constexpr u32 WS_END  = EP_OFF + 256u;
constexpr u32 OOB = 0x7ffffff0u;
constexpr int SPIN_CAP = 1 << 15;

constexpr int L_W = 98304, L_SP = 159744, L_ATT = 102400, L_KX = 119808, L_MJ = 143360, L_CNT = 163808, LDS_BYTES = 163840;
constexpr int XB0 = 65536, XB1 = 0, XB2 = 32768;

#define STAMP(i) do {} while (0)
#define SSTAMP(i) do {} while (0)

DI float bf_lo(u32 v) { return __uint_as_float(v << 16); }
DI float bf_hi(u32 v) { return __uint_as_float(v & 0xffff0000u); }
DI u32 pk_bf16(float a, float b) { f32x2 v = {a, b}; return __builtin_bit_cast(u32, __builtin_convertvector(v, bf16x2)); }
DI float round_bf(float a) { return bf_lo(pk_bf16(a, a)); }
DI float dot2(u32 a, u32 b, float c) { return __builtin_amdgcn_fdot2_f32_bf16(__builtin_bit_cast(bf16x2, a), __builtin_bit_cast(bf16x2, b), c, false); }
DI float xsum32(float v) { auto r = __builtin_amdgcn_permlane32_swap(__float_as_uint(v), __float_as_uint(v), false, false); return __uint_as_float(r[0]) + __uint_as_float(r[1]); }
DI float xsum16(float v) { auto r = __builtin_amdgcn_permlane16_swap(__float_as_uint(v), __float_as_uint(v), false, false); return __uint_as_float(r[0]) + __uint_as_float(r[1]); }
DI f32x16 mfma32(u32x4 a, u32x4 b, f32x16 c) { return __builtin_amdgcn_mfma_f32_32x32x16_bf16(__builtin_bit_cast(bf16x8, a), __builtin_bit_cast(bf16x8, b), c, 0, 0, 0); }
DI f32x4 mfma16(u32x4 a, u32x4 b, f32x4 c) { return __builtin_amdgcn_mfma_f32_16x16x32_bf16(__builtin_bit_cast(bf16x8, a), __builtin_bit_cast(bf16x8, b), c, 0, 0, 0); }
DI u32x2 tr_read(u32 a) { return __builtin_bit_cast(u32x2, __builtin_amdgcn_ds_read_tr16_b64_v4i16((LAS s16x4*)(size_t)a)); }
DI u32x4 tr_frag(u32 a, int d) { const u32x2 lo = tr_read(a), hi = tr_read(a + d); return u32x4{lo[0], lo[1], hi[0], hi[1]}; }
template <class T> DI T lds_ld(u32 a) { return *(const LAS T*)(size_t)a; }
template <class T> DI void lds_st(u32 a, T v) { *(LAS T*)(size_t)a = v; }
DI void dma16(const char* sbase, u32 voff, u32 ldst) {
  asm volatile("s_mov_b32 m0, %2\n\ts_nop 0\n\tglobal_load_lds_dwordx4 %0, %1" ::"v"(voff), "s"(sbase), "s"(ldst) : "memory");
}
template <int N> DI void wait_vm() { asm volatile("s_waitcnt vmcnt(%0)" ::"n"(N) : "memory"); }
DI void wait_lgkm0() { __builtin_amdgcn_s_waitcnt(0xC07F); }
DI void bar() { __builtin_amdgcn_s_barrier(); }
DI u32x4 bload(rsrc_t r, u32 voff, u32 soff) { return __builtin_amdgcn_raw_buffer_load_b128(r, voff, soff, 16); }
DI void bstore16(u32x4 v, rsrc_t r, u32 voff) { __builtin_amdgcn_raw_buffer_store_b128(v, r, voff, 0, 16); }
DI void bstore8(u32x2 v, rsrc_t r, u32 voff) { __builtin_amdgcn_raw_buffer_store_b64(v, r, voff, 0, 16); }
constexpr u32 SENT = 0xffffffffu;
DI u32 nosent(u32 v) { return min(v, SENT - 1u); }
DI u32x4 nosent4(u32x4 v) { return u32x4{nosent(v[0]), nosent(v[1]), nosent(v[2]), nosent(v[3])}; }
DI u32 max4(u32 m, u32x4 v) { return max(max(m, max(v[0], v[1])), max(v[2], v[3])); }
constexpr float LOG2E = 1.4426950408889634f;
template <int SLEEP = 4> DI void wait_flags(const char* ws, u32 off, bool on, u32 tag) {
  GAS u32* p = (GAS u32*)(ws + off);
#pragma unroll 1
  for (int spin = 0; spin < SPIN_CAP; ++spin) {
    const u32 v = on ? __hip_atomic_load(p, RLX_AGENT) : tag;
    if (__all(v == tag)) break;
    __builtin_amdgcn_s_sleep(SLEEP);
  }
  __builtin_amdgcn_fence(__ATOMIC_ACQUIRE, "wavefront");
  asm volatile("" ::: "memory");
}
template <int SLEEP> DI void wait_data(rsrc_t rs, u32 off) {
#pragma unroll 1
  for (int spin = 0; spin < SPIN_CAP; ++spin) {
    if (!__any(__builtin_amdgcn_raw_buffer_load_b32(rs, off, 0, 16) == SENT)) break;
    __builtin_amdgcn_s_sleep(SLEEP);
    asm volatile("" ::: "memory");
  }
  asm volatile("" ::: "memory");
}
struct Poll { u32 v[2]; };
DI void poll_flags(Poll& p, const char* ws, u32 off, u32 tag) {
  u32 cnt = SPIN_CAP;
#define POLL_STEP(LD, CK) "s_sleep 4\n\tglobal_load_dword %" #LD ", %3, %4 sc1\n\ts_waitcnt vmcnt(1)\n\tv_cmp_eq_u32 vcc, %5, %" #CK "\n\ts_nop 0\n\ts_cmp_eq_u64 vcc, -1\n\ts_cbranch_scc1 .Lpd%=\n\t"
  asm volatile("global_load_dword %0, %3, %4 sc1\n"
               ".Lpl%=:\n\t" POLL_STEP(1, 0) POLL_STEP(0, 1)
               "s_sub_u32 %2, %2, 1\n\ts_cmp_lg_u32 %2, 0\n\ts_cbranch_scc1 .Lpl%=\n"
               ".Lpd%=:"
               : "=&v"(p.v[0]), "=&v"(p.v[1]), "+s"(cnt) : "v"(off), "s"(ws), "s"(tag) : "vcc", "scc", "memory");
#undef POLL_STEP
}
DI void poll_done(Poll& p) { asm volatile("" ::"v"(p.v[0]), "v"(p.v[1])); }
template <int REP> DI void raise_flag(char* ws, u32 off, u32 tag) {
  asm volatile("s_mov_b64 exec, %3\n\tglobal_store_dword %0, %1, %2 sc1\n\ts_mov_b64 exec, -1" ::"v"(off), "v"(tag), "s"(ws), "s"((1ull << REP) - 1) : "memory");
}
template <int REP = 1, int STRIDE = 0, int KEEP = 0> DI void publish(char* ws, u32 off, u32 tag) {
  wait_vm<KEEP>();
  wait_lgkm0(); bar();
  asm volatile("" ::: "memory");
  if (__builtin_amdgcn_readfirstlane(threadIdx.x) == 0) raise_flag<REP>(ws, off + threadIdx.x * STRIDE, tag);
}

struct Sched { int op[96]; int first[20]; };
constexpr Sched make_sched(int n16) {
  Sched r{}; int n = 0;
  for (int k = 0; k < 4; ++k) r.op[n++] = 100 + k;
  r.op[n++] = 0; r.op[n++] = 1; r.op[n++] = 2;
  for (int k = 0; k < 4; ++k) r.op[n++] = 104 + k;
  r.op[n++] = 3; r.op[n++] = 4;
  for (int s = 0; s < 18; ++s) {
    r.first[s + 1] = n;
    const int it = s / 2;
    if (s == 13) { r.op[n++] = 1000; r.op[n++] = 1000; }
    if (s == 0) { r.op[n++] = 5; r.op[n++] = 108; r.op[n++] = 109; r.op[n++] = 6; }
    if (s <= 14) {
      if ((s & 1) == 0) { r.op[n++] = 7 + 3 * it; r.op[n++] = 8 + 3 * it; r.op[n++] = 100 + 4 * (it + 2) + 2; r.op[n++] = 100 + 4 * (it + 2) + 3; }
      else { r.op[n++] = 9 + 3 * it; r.op[n++] = 100 + 4 * (it + 3); r.op[n++] = 100 + 4 * (it + 3) + 1; }
    } else for (int k = 0; k < (s == 15 ? n16 / 100 : s == 16 ? n16 / 10 % 10 : n16 % 10); ++k) r.op[n++] = 1000;
  }
  r.first[19] = n;
  return r;
}
constexpr int vm_need(int n16, int t, int b) {
  const Sched sch = make_sched(n16);
  const int issued = b < 0 ? 7 : sch.first[b + 1];
  int last = -1;
  for (int k = 0; k < issued; ++k) {
    const int op = sch.op[k];
    if (op < 100 ? (8 * op <= 12 * t + 11) : (op < 1000 && (op - 100) / 4 <= t / 2)) last = k;
  }
  return issued - 1 - last;
}

struct Frag { u32x4 x[2][2]; u32x4 w[3][2]; };

DI int p2_start(int i) { return i * 13 + min(i, 2); }
DI int p2_blkstep(int blk) { const int i = blk < 20 ? blk / 5 : (blk - 20) >> 1; return p2_start(i) + (blk < 20 ? blk - 5 * i : 5 + (blk & 1)); }
DI u32 p2_voff(int p5, int l) { const int n = p5 * 64 + l, r = n / 10, c = n - 10 * r; return (u32)((r & 19) | ((r & 4) << 1) | ((r & 8) >> 1)) * 1280u + c * 16; }
DI void p2_piece(const char* wdc, int piece, int l) {
  const int blk = piece / 5;
  dma16(wdc + (size_t)p2_blkstep(blk) * 40960, p2_voff(piece - 5 * blk, l), piece * 1024);
}

template <int TYPE> DI f32x16 p1_mfma(u32x4 w, u32x4 x, f32x16 c) { return TYPE == 2 ? mfma32(x, w, c) : mfma32(w, x, c); }

template <int TYPE, class F> DI void p1_step(f32x16 (&acc)[3][2], const Frag& cur, bool third, F&& fill) {
#pragma unroll
  for (int i = 0; i < 8; ++i) {
    const int b = (i >> 1) & 1, k = i >> 2, t = i & 1;
    acc[b][t] = p1_mfma<TYPE>(cur.w[b][k], cur.x[t][k], acc[b][t]);
    fill(i);
    SB0;
  }
  if (TYPE != 1 || third) {
#pragma unroll
    for (int i = 8; i < 12; ++i) {
      const int k = (i >> 1) & 1, t = i & 1;
      if (TYPE == 2 && t == 1) continue;
      acc[2][t] = p1_mfma<TYPE>(cur.w[2][k], cur.x[t][k], acc[2][t]);
      SB0;
    }
  }
}
template <int I, int N, class F> DI void static_for(F&& f) { if constexpr (I < N) { f(std::integral_constant<int, I>{}); static_for<I + 1, N>(f); } }
constexpr int xslot(int i) { return i % 3 == 0 ? XB0 : (i % 3 == 1 ? XB1 : XB2); }

DI float exp2_le1(float t) { return __builtin_amdgcn_fmed3f(__builtin_amdgcn_exp2f(t), 0.f, 1.f); }
DI float phi_f(float t) { return fmaxf(t, 1e-6f) + exp2_le1(t * LOG2E); }

template <int TYPE>
DI void phase1(int rt, int ct, int j, int c2, const u16* x, const u16* wqkv, const u16* wo, const u16* wg, const u16* wu, const u16* wd, char* ws, u32& tag_out) {
  const int tid = threadIdx.x, l = tid & 63, w = __builtin_amdgcn_readfirstlane(tid >> 6);
  const int tg = w & 3, cgp = w >> 2, h = l >> 5, n = l & 31;
  const int k0 = ABL_NOROT ? 0 : (rt * 10) >> 3;
  const char* xg = (const char*)x + (size_t)rt * 256 * 1280;
  const u32 xvo = (u32)(8 * w + (l >> 3)) * 1280 + (((l & 7) ^ ((4 * (w & 1) + (l >> 4)) & 7)) * 16);
  auto dma_x = [&](int kk, int xi, u32 slot) { dma16(xg + (xi * 64 * 1280 + kk * 128), xvo, slot + xi * 8192 + w * 1024); };
#pragma unroll
  for (int xi = 0; xi < 4; ++xi) dma_x(k0, xi, XB0);
  const char* wb[3]; u32 wvo[3];
  constexpr u32 STRIDE = TYPE == 0 ? 3456 : 3840, WPER = 64 * STRIDE;
  {
    const char* b0 = TYPE == 0 ? (const char*)(wg + 96 * j) : TYPE == 1 ? (const char*)(wqkv + 160 * j) : (const char*)(wqkv + 640 + 80 * j);
    const char* b1 = TYPE == 0 ? (const char*)(wu + 96 * j) : TYPE == 1 ? b0 + 192 : b0 + 1280;
#pragma unroll
    for (int i = 0; i < 3; ++i) {
      const int pp = 8 * i + w, st = pp >= 12, pi = pp - 12 * st, part = pi >= 6;
      const int m = (pi - 6 * part) * 64 + l, row = m / 12, cp = m - 12 * row;
      int ch = part ? cp ^ 2 : cp;
      if (TYPE == 1 && part && ch >= 8) ch -= 4;
      if (TYPE == 2 && ch >= 10) ch -= 4;
      wb[i] = part ? b1 : b0;
      wvo[i] = (u32)(st * 32 + row) * STRIDE + ch * 16;
    }
  }
  const u32 wl0 = L_W + w * 1024, wl1 = w >= 4 ? wl0 - 61440 : wl0;
  auto dma_w = [&](int kk, int grp) { const int pos = (8 * grp) % 60; dma16(wb[grp % 3] + (size_t)((u32)kk * WPER), wvo[grp % 3], (pos == 56 ? wl1 : wl0) + pos * 1024); };
  auto rot = [&](int kk) { return kk + 1 >= 10 ? kk - 9 : kk + 1; };
  const int k1 = rot(k0), k2 = rot(k1);
  dma_w(k0, 0); dma_w(k0, 1); dma_w(k0, 2);
  u32 ep;
  asm volatile("s_load_dword %0, %1, 0x0" : "=s"(ep) : "s"(ws + EP_OFF) : "memory");

  auto bphys = [&](int b) { int v = b + tg; v = v >= 3 ? v - 3 : v; return v >= 3 ? v - 3 : v; };
  u32 xa[2][4], wa[3][2];
  {
    const int ksw = TYPE == 2 ? (tg & 1) : 0;
#pragma unroll
    for (int t = 0; t < 2; ++t) {
      const int row = 64 * tg + 32 * (TYPE == 2 ? t ^ cgp : t) + n;
#pragma unroll
      for (int k = 0; k < 4; ++k) xa[t][k] = row * 128 + 16 * ((2 * (k ^ ksw) + h) ^ ((row >> 1) & 7));
    }
    const int blk = (l >> 4) & 1, q = (l & 15) >> 2, p = l & 3, rowb = L_W + (8 * h + q) * 192;
    if (TYPE == 2) {
      const int a = cgp ? 3 : 0;
      const u32 b0 = rowb + (a + blk) * 32 + 8 * p, b1 = 6144 + rowb + ((a + blk) ^ 1) * 32 + 8 * p;
      const u32 b2 = rowb + (p >> 1) * 6144 + ((4 + (cgp ^ blk)) ^ (p & 2)) * 16 + 8 * (p & 1);
#pragma unroll
      for (int k = 0; k < 2; ++k) { const u32 o = (k ^ ksw) * 3072; wa[0][k] = b0 + o; wa[1][k] = b1 + o; wa[2][k] = b2 + o; }
    } else {
#pragma unroll
      for (int b = 0; b < 3; ++b) {
        const u32 in32 = 16 * (p & 1) + 8 * (p >> 1);
        if (TYPE == 0) wa[b][0] = rowb + blk * 6144 + ((3 * cgp + bphys(b)) ^ blk) * 32 + in32;
        else wa[b][0] = rowb + cgp * 6144 + ((2 * (cgp ? (b == 2 ? 2 : b ^ (tg & 1)) : bphys(b)) + blk) ^ cgp) * 32 + in32;
        wa[b][1] = 0;
      }
    }
  }
  auto rd_w = [&](int b, int k, int off) { return tr_frag((TYPE == 2 ? wa[b][k] : wa[b][0] + k * 3072) + off, 768); };
  auto rd_x = [&](int t, int k4, int off) {
    return TYPE == 2 ? lds_ld<u32x4>(xa[t][k4] + off) : lds_ld<u32x4>(xa[0][k4] + off + t * 4096);
  };
  auto fill_rd = [&](Frag& f, int i, int st) {
    const int wo_ = (st % 5) * 12288, xo = xslot(st / 2), k4 = 2 * (st & 1);
    if (i == 0) { f.w[0][0] = rd_w(0, 0, wo_); f.x[0][0] = rd_x(0, k4, xo); }
    if (i == 1) { f.x[1][0] = rd_x(1, k4, xo); f.w[1][0] = rd_w(1, 0, wo_); }
    if (i == 2) { f.w[0][1] = rd_w(0, 1, wo_); f.x[0][1] = rd_x(0, k4 + 1, xo); }
    if (i == 3) { f.x[1][1] = rd_x(1, k4 + 1, xo); f.w[1][1] = rd_w(1, 1, wo_); }
    if (i == 4) { f.w[2][0] = rd_w(2, 0, wo_); f.w[2][1] = rd_w(2, 1, wo_); }
  };
  f32x16 acc[3][2];
#pragma unroll
  for (int b = 0; b < 3; ++b)
#pragma unroll
    for (int t = 0; t < 2; ++t)
#pragma unroll
      for (int i = 0; i < 16; ++i) acc[b][t][i] = 0.f;
  Frag fa, fb;
  STAMP(1);
  asm volatile("s_waitcnt vmcnt(%1) lgkmcnt(0)" : "+s"(ep) : "n"(vm_need(0, 0, -1)) : "memory");
  bar(); SB0;
  const u32 tag = ep + 1u;
  tag_out = tag;
#pragma unroll
  for (int i = 0; i < 5; ++i) fill_rd(fa, i, 0);
  SB0;
#pragma unroll
  for (int xi = 0; xi < 4; ++xi) dma_x(k1, xi, XB1);
  dma_w(k1, 3); dma_w(k1, 4);
  SB0;
  STAMP(2);
  const bool third = cgp == 0;
  float cs[3] = {0.f, 0.f, 0.f};
  auto colsum = [&](const Frag& f, int i) {
    constexpr u32 ones = 0x3f803f80u;
    if (TYPE == 2) {
      if (i < 6) { const int b = i >> 1; cs[b] = dot2(f.w[b][0][2 * (i & 1)], ones, cs[b]); cs[b] = dot2(f.w[b][0][2 * (i & 1) + 1], ones, cs[b]); asm volatile("" : "+v"(cs[b])); }
    } else { cs[0] = dot2(f.w[0][i >> 2][i & 3], ones, cs[0]); asm volatile("" : "+v"(cs[0])); }
  };
  auto colid = [&](int b) {
    if (TYPE == 2) return b == 2 ? ((n >> 3) & 1) * 96 + 32 + 8 * (cgp ^ (n >> 4)) + (n & 7) : b * 96 + (cgp ? 48 : 0) + n;
    const int p = (n & 15) >> 2, c16 = 8 * (p & 1) + 4 * (p >> 1) + (n & 3);
    if (TYPE == 0) return (n >> 4) * 96 + (3 * cgp + bphys(b)) * 16 + c16;
    return cgp * 96 + (cgp ? (b == 2 ? 2 : b ^ (tg & 1)) : bphys(b)) * 32 + (n & 16) + c16;
  };
  GAS u64* csg = (GAS u64*)(ws + CSG_OFF) + ct * 1536 + rt * 192;
  const rsrc_t rs = __builtin_amdgcn_make_buffer_rsrc(ws, 0, WS_END, 0x00020000);
  int ka = k2, kb = rot(k2);
  const char* wdc = (const char*)(wd + 80 * c2);
  u32x4 gr[2];
  u32 goff[2], pvo[5];
  f32x2 gs[2];
  auto gload = [&]() {
#pragma unroll
    for (int k = 0; k < 2; ++k) asm volatile("global_load_dwordx4 %0, %1, %2 sc1" : "=v"(gr[k]) : "v"(goff[k]), "s"(ws) : "memory");
  };
  auto gcheck = [&]() {
    asm volatile("" : "+v"(gr[0]), "+v"(gr[1]));
    return __all(gr[0][1] == tag && gr[0][3] == tag && gr[1][1] == tag && gr[1][3] == tag);
  };
  auto oct_sum = [&](u32 u) {
    float v = __uint_as_float(u);
    v += __uint_as_float(__builtin_amdgcn_mov_dpp(__float_as_uint(v), 0xB1, 0xf, 0xf, true));
    v += __uint_as_float(__builtin_amdgcn_mov_dpp(__float_as_uint(v), 0x4E, 0xf, 0xf, true));
    return v + __uint_as_float(__builtin_amdgcn_mov_dpp(__float_as_uint(v), 0x141, 0xf, 0xf, true));
  };
  auto pf2 = [&](int base) {
    constexpr int NP = TYPE == 2 ? 116 : 140;
    const bool clip = base + 8 > NP;
    const int piece = base + (clip ? w & 3 : w), blk = piece / 5;
    u32 vo = pvo[base % 5];
    if (clip) { u32 v1 = pvo[(base + 1) % 5]; asm volatile("" : "+v"(v1)); vo = w < 4 ? vo : v1; }
    const int lo = blk / 5, hi = (blk - 20) >> 1;
    const int st = base + 8 <= 100 || clip && base < 100 ? p2_start(lo) + blk - 5 * lo : base >= 100 ? p2_start(hi) + 5 + (blk & 1) : (w < 4 ? 45 : 5);
    dma16(wdc + (u32)st * 40960u, vo, piece * 1024);
  };
  constexpr int N16 = 3, NX = TYPE == 2 ? 534 : 434;
  auto bp_of = [&](int b) { return TYPE == 0 ? bphys(b) : (cgp ? (b == 2 ? 2 : b ^ (tg & 1)) : bphys(b)); };
  auto oaddr = [&](int k) {
    const u32 dst = ACT_OFF + (u32)(16 * rt + 4 * tg + (n >> 4)) * (NSTEP * 1024) + (n & 15) * 16;
    if (TYPE == 0) { const int id = 12 * j + 6 * cgp + 2 * bp_of(k >> 1) + h; return dst + (k & 1) * 2 * NSTEP * 1024 + (id >> 2) * 1024 + (id & 3) * 256; }
    const int ch = 4 * (3 * cgp + bp_of(k >> 2)) + 2 * (k & 1) + h, g4 = ch / 5;
    return (k >= 8 && cgp) ? OOB : dst + ((k >> 1) & 1) * 2 * NSTEP * 1024 + (54 + 5 * j + ch - 5 * g4) * 1024 + g4 * 256;
  };
  static_for<0, 18>([&](auto SC) {
    constexpr int S = decltype(SC)::value, IT = S / 2;
    Frag& cur = (S & 1) ? fb : fa; Frag& nxt = (S & 1) ? fa : fb;
    wait_lgkm0(); wait_vm<vm_need(NX, S + 1, S)>(); bar(); SSTAMP(16 + S); SB0;
    p1_step<TYPE>(acc, cur, third, [&](int i) {
      if (!ABL_NOREAD) fill_rd(nxt, i, S + 1);
      if (!ABL_NODMA) {
        if (S == 0) { if (i == 0) dma_w(k1, 5); if (i == 1 || i == 2) dma_x(k2, i - 1, XB2); if (i == 3) dma_w(k2, 6); }
        if (S > 14) { if (S == 16 ? (i >= 5 && i < 5 + N16) : i >= 4) pf2(S == 15 ? 8 * (i - 4) : S == 16 ? 96 + 8 * (i - 5) : 8 * i); }
        else if ((S & 1) == 0) { if (i == 4 || i == 5) dma_w(ka, 3 + 3 * IT + i); if (i >= 6) dma_x(ka, i - 4, xslot(IT + 2)); }
        else { if (i == 5) dma_w(kb, 9 + 3 * IT); if (i >= 6) dma_x(kb, i - 6, xslot(IT)); if (i == 7) { ka = kb; kb = rot(kb); } }
      }
      if (S < 2) colsum(cur, i);
      if ((S == 2 || S == 3) && i == 5 && (rt & 3) == 3) {
#pragma unroll
        for (int e = 0; e < 8; ++e) colsum(cur, e);
      }
      if (S == 4 && i == 2) {
        if (TYPE == 2) {
#pragma unroll
          for (int b = 0; b < 3; ++b) { const float v = xsum32(cs[b]); if (l < 32 && tg < 2) lds_st<float>(L_SP + (tg * 192 + colid(b)) * 4, v); }
        } else {
          const float v = xsum32(cs[0]);
          if (l < 32) __hip_atomic_store(csg + colid(0), ((u64)tag << 32) | __float_as_uint(v), RLX_AGENT);
        }
      }
      if (TYPE == 2 && S == 5 && i == 2 && tid < 192)
        __hip_atomic_store(csg + tid, ((u64)tag << 32) | __float_as_uint(lds_ld<float>(L_SP + tid * 4) + lds_ld<float>(L_SP + 768 + tid * 4)), RLX_AGENT);
      if (S == 12 && i == 1) {
        const int hi = (TYPE == 1 && tid >= 128) ? tid - 128 : tid;
        goff[0] = CSG_OFF + ct * 12288 + (tid & 7) * 1536 + (tid >> 3) * 16;
        goff[1] = w < 4 ? CSG_OFF + ct * 12288 + (tid & 7) * 1536 + 1024 + (hi >> 3) * 16 : STG_OFF + 2048 * rt + (tid & 127) * 16;
      }
      if (S == 13 && i == 0) gload();
      if ((S == 13 || S == 14) && i >= 1 && i < (S == 13 ? 4 : 3)) {
        int p5 = w + (S - 13) * 3 + i - 1; p5 = p5 >= 10 ? p5 - 10 : p5 >= 5 ? p5 - 5 : p5; p5 = p5 >= 5 ? p5 - 5 : p5;
        pvo[(S - 13) * 3 + i - 1] = p2_voff(p5, l);
      }
      if (TYPE == 2 && S == 15 && i == 1 && tid < 480) bstore16(u32x4{SENT, SENT, SENT, SENT}, rs, KVR_OFF + (u32)((2 * j + (tid >= 240)) * 8 + rt) * 3840 + (tid >= 240 ? tid - 240 : tid) * 16);
      if (S == 16) {
        if (i == 1) {
          wait_vm<TYPE == 2 ? 12 : 11>();
          if (!gcheck()) {
#pragma unroll 1
            for (int spin = 0; spin < SPIN_CAP; ++spin) { __builtin_amdgcn_s_sleep(2); gload(); wait_vm<0>(); if (gcheck()) break; }
          }
        }
        if (i == 2 || i == 3) gs[i - 2] = f32x2{oct_sum(gr[i - 2][0]), oct_sum(gr[i - 2][2])};
        if (i == 4) lds_st<f32x2>(L_SP + 2048 + (tid >> 3) * 8, gs[0]);
        if (i == 5) {
          if (w < 4) lds_st<f32x2>(L_SP + 2560 + (tid >> 3) * 8, gs[1]);
          else {
            lds_st<f32x2>(L_SP + (tid & 127) * 8, f32x2{bf_lo(gr[1][0]), bf_lo(gr[1][2])});
            lds_st<f32x2>(L_SP + 1024 + (tid & 127) * 8, f32x2{__builtin_amdgcn_rcpf(bf_hi(gr[1][0])), __builtin_amdgcn_rcpf(bf_hi(gr[1][2]))});
          }
        }
      }
    });
  });
  auto mm = [&](int b, int t, int q) { const Frag& f = q < 2 ? fa : fb; acc[b][t] = p1_mfma<TYPE>(f.w[b][q & 1], f.x[t][q & 1], acc[b][t]); };
  wait_lgkm0(); wait_vm<vm_need(NX, 19, 18)>(); bar(); SSTAMP(34); SB0;
  if constexpr (TYPE != 2) {
    float mean[2], rstd[2], ce[2], sd[2];
    f32x4 cv[2][4];
    u32 ob[8];
    auto ld_cv = [&](int b) {
      const u32 a = L_SP + 2048 + (TYPE == 0 ? (3 * cgp + bp_of(b)) * 16 : cgp * 96 + bp_of(b) * 32) * 4 + 32 * h;
#pragma unroll
      for (int u = 0; u < 4; ++u) cv[b & 1][u] = lds_ld<f32x4>(a + (u >> 1) * (TYPE == 0 ? 384 : 64) + (u & 1) * 16);
    };
    auto epi = [&](int e) {
      const int b = e >> 4, t = (e >> 3) & 1, i = e & 7;
      const f32x4 (&c)[4] = cv[b & 1];
      if (TYPE == 0) {
        const float ex = __builtin_amdgcn_exp2f((acc[b][t][i] - mean[t] * c[i >> 2][i & 3]) * ce[t]);
        const float hv = (acc[b][t][8 + i] - mean[t] * c[2 + (i >> 2)][i & 3]) * __builtin_amdgcn_rcpf(ex * sd[t] + sd[t]);
        if (i & 1) ob[i >> 1] = pk_bf16(__uint_as_float(ob[i >> 1]), hv); else ob[i >> 1] = __float_as_uint(hv);
        asm volatile("" : "+v"(ob[i >> 1]));
        if (i == 7) bstore16(u32x4{ob[0], ob[1], ob[2], ob[3]}, rs, oaddr(2 * b + t));
      } else {
        const float u0 = acc[b][t][2 * i] - mean[t] * c[i >> 1][(2 * i) & 3], u1 = acc[b][t][2 * i + 1] - mean[t] * c[i >> 1][(2 * i + 1) & 3];
        ob[i] = pk_bf16(fmaxf(u0, sd[t]) * rstd[t] + exp2_le1(u0 * ce[t]), fmaxf(u1, sd[t]) * rstd[t] + exp2_le1(u1 * ce[t]));
        asm volatile("" : "+v"(ob[i]));
        if ((i & 3) == 3) bstore16(u32x4{ob[i - 3], ob[i - 2], ob[i - 1], ob[i]}, rs, oaddr(4 * b + 2 * t + (i >> 2)));
      }
    };
#pragma unroll
    for (int i = 0; i < 6; ++i) {
      mm(0, i < 4 ? i >> 1 : 0, i < 4 ? i & 1 : i - 2);
      if (i < 5) fill_rd(fb, i == 1 ? 2 : i == 2 ? 1 : i, 19);
      if (i == 0) {
#pragma unroll
        for (int t = 0; t < 2; ++t) { mean[t] = lds_ld<float>(L_SP + (64 * tg + 32 * t + n) * 4); rstd[t] = lds_ld<float>(L_SP + 1024 + (64 * tg + 32 * t + n) * 4); }
      }
      if (i == 2) ld_cv(0);
      if (i == 3) {
#pragma unroll
        for (int t = 0; t < 2; ++t) { ce[t] = (TYPE == 0 ? -LOG2E : LOG2E) * rstd[t]; sd[t] = (TYPE == 0 ? 1.f : 1e-6f) * __builtin_amdgcn_rcpf(rstd[t]); }
      }
      if (i >= 3) pf2(96 + 8 * i);
      if (i == 5) { ld_cv(1); if (tid < 2) lds_st<u32x4>(L_CNT + tid * 16, u32x4{0u, 0u, 0u, 0u}); }
      SB0;
    }
    wait_lgkm0(); bar(); SB0;
    if (cgp) __builtin_amdgcn_s_setprio(1);
#pragma unroll
    for (int i = 0; i < 10; ++i) {
      mm(i < 2 ? 0 : 1, i < 2 ? 1 : (i - 2) >> 2, i < 2 ? 2 + i : (i - 2) & 3);
      epi(2 * i); epi(2 * i + 1);
      if (i < 4) pf2(64 + 8 * i);
      if (i == 7) ld_cv(2);
      SB0;
    }
    __builtin_amdgcn_s_setprio(0);
    if (TYPE == 0 || third) {
#pragma unroll
      for (int i = 0; i < 8; ++i) { mm(2, i >> 2, i & 3); epi(20 + 2 * i); epi(21 + 2 * i); SB0; }
      STAMP(3);
#pragma unroll
      for (int e = 36; e < 48; ++e) epi(e);
    } else {
      STAMP(3);
#pragma unroll
      for (int e = 20; e < 32; ++e) epi(e);
    }
    STAMP(4);
    publish<8, 2048>(ws, FH_OFF + rt * 256 + (TYPE == 0 ? j : 18 + j) * 4, tag);
    STAMP(5);
  } else {
    float cv[3];
    u32x4 xp[3][2][2];
    f32x4 sm[2], sr[2];
    float zs[2] = {0.f, 0.f};
    constexpr int EB[5] = {2, 0, 0, 1, 1}, ET[6] = {0, 0, 1, 0, 1, 0};
    auto ld_st = [&](int p) {
      const u32 a = L_SP + (64 * tg + 32 * (ET[p >> 2] ^ cgp) + 8 * (p & 3) + 4 * h) * 4;
      sm[p & 1] = lds_ld<f32x4>(a); sr[p & 1] = lds_ld<f32x4>(a + 1024);
    };
    auto epi2 = [&](int p) {
      const int b = EB[p >> 2], t = ET[p >> 2], r0 = 4 * (p & 3);
      const f32x4 m = sm[p & 1], r = sr[p & 1];
      float v[4];
#pragma unroll
      for (int e = 0; e < 4; ++e) {
        v[e] = (acc[b][t][r0 + e] - m[e] * cv[b]) * r[e];
        if (b == 0) v[e] = phi_f(v[e]);
        if (b == 2) { const float pv = phi_f(v[e]); v[e] = (n & 8) ? v[e] : pv; }
      }
      if (p + 2 < 20) ld_st(p + 2);
      if (b != 1) { zs[b >> 1] += (v[0] + v[1]) + (v[2] + v[3]); asm volatile("" : "+v"(zs[b >> 1])); }
      u32 o0 = pk_bf16(v[0], v[1]), o1 = pk_bf16(v[2], v[3]);
      asm volatile("" : "+v"(o0), "+v"(o1));
      xp[b][t][(p >> 1) & 1][2 * (p & 1)] = o0; xp[b][t][(p >> 1) & 1][2 * (p & 1) + 1] = o1;
    };
#pragma unroll
    for (int i = 0; i < 6; ++i) {
      mm(i < 2 || i >= 4 ? 2 : 0, 0, i < 4 ? i & 1 : i - 2);
      if (i < 5) fill_rd(fb, i == 0 ? 4 : i == 1 ? 0 : i == 2 ? 2 : i == 3 ? 1 : 3, 19);
      if (i == 3) {
#pragma unroll
        for (int b = 0; b < 3; ++b) cv[b] = lds_ld<float>(L_SP + 2048 + colid(b) * 4);
      }
      if (i >= 4) ld_st(i - 4);
      SB0;
    }
#pragma unroll
    for (int i = 0; i < 6; ++i) { mm(0, i >= 2, i < 2 ? 2 + i : i - 2); epi2(i); SB0; }
    if (n >= 16) {
#pragma unroll
      for (int k = 0; k < 2; ++k) lds_st<u32x4>(L_KX + ((w * 2 + k) * 32 + (n - 16) + 16 * h) * 16, xp[2][0][k]);
    }
    publish<1, 0, 7>(ws, FK_OFF + j * 256 + rt * 4, tag); SB0;
#pragma unroll
    for (int k = 0; k < 2; ++k) xp[2][1][k] = lds_ld<u32x4>(L_KX + (((w ^ 4) * 2 + k) * 32 + (n & 15) + 16 * h) * 16);
    f32x16 z16;
#pragma unroll
    for (int i = 0; i < 16; ++i) z16[i] = 0.f;
    f32x16 pmk = z16, pmm = z16, pvk = z16, pvm = z16;
    constexpr u32 IMG = L_KX + 8192, DUMP = IMG + 8 * 3840;
    const int lo8 = 8 * cgp, mix = 32 - 32 * cgp;
    u32 avk[4], amk, avm[2], amm;
    auto img_at = [&](int kc, int vc, bool on) { return (on && (kc >= 20) == (vc >= 20)) ? IMG + w * 3840 + kc * 96 + (vc >= 20 ? vc - 20 : vc) * 4 : DUMP + l * 16; };
#pragma unroll
    for (int i = 0; i < 8; ++i) {
      mm(1, i >> 2, i & 3); epi2(6 + i);
      if (i < 4) pf2(64 + 8 * i);
      if (i >= 2 && i < 6) pmm = mfma32(xp[2][(i - 2) >> 1][i & 1], xp[2][(i - 2) >> 1][i & 1], pmm);
      if (i == 6) { amm = img_at(mix + n, mix + 4 * h, n < 8); amk = img_at(lo8 + n, mix + 4 * h, true); asm volatile("" : "+v"(amm), "+v"(amk)); }
      if (i == 7) {
#pragma unroll
        for (int q = 0; q < 2; ++q) { avm[q] = img_at(mix + n, lo8 + 16 * (1 - cgp) + 8 * q + 4 * h, n < 8); asm volatile("" : "+v"(avm[q])); }
      }
      SB0;
    }
    STAMP(3);
#pragma unroll
    for (int i = 0; i < 6; ++i) {
      if (i < 4) pmk = mfma32(xp[2][i >> 1][i & 1], xp[0][i >> 1][i & 1], pmk);
      if (i >= 2 && i < 4) { pvk = mfma32(xp[1][0][i & 1], xp[0][0][i & 1], pvk); pvm = mfma32(xp[1][0][i & 1], xp[2][0][i & 1], pvm); }
      epi2(14 + i);
      if (i < 4) { avk[i] = img_at(lo8 + n, lo8 + 8 * i + 4 * h, true); asm volatile("" : "+v"(avk[i])); }
      if (i == 1) lds_st<f32x4>(amm, f32x4{pmm[4], pmm[5], pmm[6], pmm[7]});
      if (i == 4) {
        const float z0 = xsum32(zs[0]), z1 = xsum32(zs[1]);
        lds_st<float>(h == 0 ? IMG + w * 3840 + (lo8 + n) * 96 + 80 : (n & 8) ? DUMP + l * 16 : (n < 8 ? IMG + w * 3840 + (mix + n) * 96 + 80 : IMG + (w ^ 4) * 3840 + (32 - mix + n - 16) * 96 + 84), h == 0 ? z0 : z1);
      }
      if (i == 5) lds_st<f32x4>(amk, f32x4{pmk[4], pmk[5], pmk[6], pmk[7]});
      SB0;
    }
#pragma unroll
    for (int k = 0; k < 2; ++k) { pvk = mfma32(xp[1][1][k], xp[0][1][k], pvk); pvm = mfma32(xp[1][1][k], xp[2][1][k], pvm); }
    if (tid < 2) lds_st<u32x4>(L_CNT + tid * 16, u32x4{0u, 0u, 0u, 0u});
#pragma unroll
    for (int q = 0; q < 4; ++q) lds_st<f32x4>(avk[q], f32x4{pvk[4 * q], pvk[4 * q + 1], pvk[4 * q + 2], pvk[4 * q + 3]});
#pragma unroll
    for (int q = 0; q < 2; ++q) lds_st<f32x4>(avm[q], cgp ? f32x4{pvm[4 * q], pvm[4 * q + 1], pvm[4 * q + 2], pvm[4 * q + 3]} : f32x4{pvm[8 + 4 * q], pvm[9 + 4 * q], pvm[10 + 4 * q], pvm[11 + 4 * q]});
    __syncthreads();
    if (tid < 480) {
      const int cg = tid >= 240, o = (tid - 240 * cg) * 16;
      f32x4 sum = lds_ld<f32x4>(IMG + cg * 15360 + o);
#pragma unroll
      for (int k = 1; k < 4; ++k) sum += lds_ld<f32x4>(IMG + cg * 15360 + k * 3840 + o);
      bstore16(nosent4(__builtin_bit_cast(u32x4, sum)), rs, KVR_OFF + (u32)((2 * j + cg) * 8 + rt) * 3840 + o);
    }
    STAMP(4);
    wait_vm<1>(); wait_lgkm0(); bar();
    STAMP(5);
  }
}

DI void lds_ticket(u32 a) {
  asm volatile("s_mov_b64 exec, 1\n\tds_add_u32 %0, %1\n\ts_mov_b64 exec, -1" ::"v"(a), "v"(1u) : "memory");
}
DI void lds_wait(u32 a, u32 want) {
#pragma unroll 1
  for (int spin = 0; spin < SPIN_CAP * 16; ++spin) {
    if (__builtin_amdgcn_readfirstlane(__hip_atomic_load((LAS u32*)(size_t)a, __ATOMIC_RELAXED, __HIP_MEMORY_SCOPE_WORKGROUP)) >= want) break;
    __builtin_amdgcn_s_sleep(2);
  }
  asm volatile("" ::: "memory");
}

DI void lds_meet(u32 a, u32 want) { asm volatile("s_waitcnt lgkmcnt(0)" ::: "memory"); lds_ticket(a); lds_wait(a, want); }

DI void mjob(int pair, u32 tag, char* ws, rsrc_t rs, const u32x4 (&WO)[5][2]) {
  int tid = threadIdx.x;
  asm volatile("" : "+v"(tid));
  const int l = tid & 63, w = __builtin_amdgcn_readfirstlane(tid >> 6);
  constexpr u32 KVL = L_MJ;
  __builtin_amdgcn_s_setprio(3);
  STAMP(5);
  if (w < 4) {
    wait_flags<16>(ws, FK_OFF + (pair >> 1) * 256 + (l & 7) * 4, l < 8, tag);
    const int t4 = min(tid, 239);
    u32x4 pa[8];
#pragma unroll 1
    for (int spin = 0; spin < SPIN_CAP; ++spin) {
      u32 mx = 0;
#pragma unroll
      for (int k = 0; k < 8; ++k) pa[k] = bload(rs, KVR_OFF + (u32)pair * 30720 + t4 * 16, k * 3840);
#pragma unroll
      for (int k = 0; k < 8; ++k) mx = max4(mx, pa[k]);
      if (!__any(mx == SENT)) break;
      __builtin_amdgcn_s_sleep(4);
      asm volatile("" ::: "memory");
    }
    f32x4 sum = __builtin_bit_cast(f32x4, pa[0]);
#pragma unroll
    for (int k = 1; k < 8; ++k) sum += __builtin_bit_cast(f32x4, pa[k]);
    const bool zc = t4 % 6 == 5;
    lds_st<u32x2>(KVL + t4 * 8, zc ? u32x2{0u, 0u} : u32x2{pk_bf16(sum[0], sum[1]), pk_bf16(sum[2], sum[3])});
    if (zc) { const float z = sum[0] + (((pair & 1) ? t4 < 48 : t4 >= 192) ? sum[1] : 0.f); lds_st<u16>(KVL + 1920 + (t4 / 6) * 2, (u16)pk_bf16(z, z)); }
  }
  wait_lgkm0(); bar();
  asm volatile("" ::: "memory");
  if (w >= 5) { const int e = tid - 320; bstore16(nosent4(lds_ld<u32x4>(KVL + 1920 + (e % 5) * 16)), rs, e < 160 ? ZB_OFF + (e / 5) * 1280 + pair * 80 + (e % 5) * 16 : OOB); }
  STAMP(6);
  const int g = l >> 4, c = l & 15;
  const u32x4 zero4 = {0u, 0u, 0u, 0u};
  const int kx = 8 * (c >> 2) + (c & 3), ky = kx + 4, kz = 32 + (c & 7), go = min(g, 2) * 16;
  const u32x4 rx = lds_ld<u32x4>(KVL + kx * 48 + go), ry = lds_ld<u32x4>(KVL + ky * 48 + go), rz = lds_ld<u32x4>(KVL + kz * 48 + go);
  const u32x4 ax0 = (kx < 20 && g < 3) ? rx : zero4, ax1 = (kx >= 20 && g < 3) ? rx : zero4, ay0 = (ky < 20 && g < 3) ? ry : zero4, ay1 = (ky >= 20 && g < 3) ? ry : zero4, az = g < 3 ? rz : zero4;
  const u32 mo = MT_OFF + (u32)(100 * w + 5 * (pair >> 2)) * 1024 + (c + 16 * (pair & 3)) * 16;
  f32x4 dx[5], dy[5], dz[5];
#pragma unroll
  for (int cb = 0; cb < 5; ++cb) { const f32x4 z4 = {0.f, 0.f, 0.f, 0.f}; dx[cb] = mfma16(ax0, WO[cb][0], z4); dy[cb] = mfma16(ay0, WO[cb][0], z4); dz[cb] = mfma16(az, WO[cb][1], z4); }
#pragma unroll
  for (int cb = 0; cb < 5; ++cb) { dx[cb] = mfma16(ax1, WO[cb][1], dx[cb]); dy[cb] = mfma16(ay1, WO[cb][1], dy[cb]); }
  SB0;
#pragma unroll
  for (int cb = 0; cb < 5; ++cb) {
    bstore16(nosent4(u32x4{pk_bf16(dx[cb][0], dx[cb][1]), pk_bf16(dx[cb][2], dx[cb][3]), pk_bf16(dy[cb][0], dy[cb][1]), pk_bf16(dy[cb][2], dy[cb][3])}), rs, mo + g * 1024 + cb * 20480);
    bstore8(u32x2{nosent(pk_bf16(dz[cb][0], dz[cb][1])), nosent(pk_bf16(dz[cb][2], dz[cb][3]))}, rs, g < 2 ? mo + 4096 + 8 * g + cb * 20480 : OOB);
  }
  __builtin_amdgcn_s_setprio(0);
  STAMP(7);
}

DI int wt_step(int i) { const int q = i < 7 ? 0 : i < 14 ? 1 : i < 20 ? 2 : 3; return i + 7 * (q + 1); }

DI void spare(int sg, int c2, const u16* x, const u16* wo, const u16* wd, char* ws, u32& tag_out, u32x4 (&WO)[5][2]) {
  int tid = threadIdx.x;
  asm volatile("" : "+v"(tid));
  const int l = tid & 63, w = __builtin_amdgcn_readfirstlane(tid >> 6);
  u32x4 v[4][5];
  const int row0 = 128 * sg + 16 * w + (l >> 4);
#pragma unroll
  for (int a = 0; a < 4; ++a)
#pragma unroll
    for (int k = 0; k < 5; ++k) v[a][k] = *(const u32x4*)((const char*)x + (size_t)(row0 + 4 * a) * 1280 + k * 256 + (l & 15) * 16);
  u32 ep;
  asm volatile("s_load_dword %0, %1, 0x0\n\ts_waitcnt lgkmcnt(0)" : "=s"(ep) : "s"(ws + EP_OFF) : "memory");
  const u32 tag = ep + 1u;
  tag_out = tag;
  const int nst = sg < 10 ? 2 : 1;
#pragma unroll
  for (int a = 0; a < 4; ++a) {
    float s = 0.f, q = 0.f;
#pragma unroll
    for (int k = 0; k < 5; ++k)
#pragma unroll
      for (int e = 0; e < 4; ++e) { s = dot2(v[a][k][e], 0x3f803f80u, s); q = dot2(v[a][k][e], v[a][k][e], q); }
#pragma unroll
    for (int o = 1; o < 16; o <<= 1) { s += __shfl_xor(s, o); q += __shfl_xor(q, o); }
    const float mean = s * (1.f / 640.f);
    const float sd = __builtin_amdgcn_sqrtf(round_bf(round_bf(fmaxf(q * (1.f / 640.f) - mean * mean, 0.f)) + 1e-5f));
    if ((l & 15) == 0) __hip_atomic_store((GAS u64*)(ws + STG_OFF) + row0 + 4 * a, ((u64)tag << 32) | pk_bf16(mean, sd), RLX_AGENT);
  }
  STAMP(1);
#pragma unroll 1
  for (int a = 0; a < nst; ++a)
#pragma unroll
    for (int i = 0; i < 5; ++i) dma16((const char*)wd + (size_t)wt_step(sg + 16 * a) * 40960 + (w * 5 + i) * 1024, l * 16, a * 40960 + (w * 5 + i) * 1024);
#pragma unroll 1
  for (int pc = w; pc < 50; pc += 8) dma16((const char*)(wo + (size_t)sg * 40 * 640) + pc * 1024, l * 16, 81920 + pc * 1024);
  wait_vm<0>();
  __syncthreads();
  STAMP(2);
  const rsrc_t rs = __builtin_amdgcn_make_buffer_rsrc(ws, 0, WS_END, 0x00020000);
#pragma unroll 1
  for (int a = 0; a < nst; ++a)
#pragma unroll
    for (int i = 0; i < 5; ++i) {
      const int cb = w * 5 + i;
      const u32x4 f = tr_frag(a * 40960 + (8 * (l >> 4) + ((l & 15) >> 2)) * 1280 + cb * 32 + 8 * (l & 3), 5120);
      bstore16(f, rs, WT_OFF + (u32)(cb * 54 + wt_step(sg + 16 * a)) * 1024 + l * 16);
    }
  {
    const u32 ra = 81920 + min(8 * (l >> 4) + ((l & 15) >> 2), 19) * 1280 + w * 160 + 8 * (l & 3), rb = 81920 + min(8 * (l >> 4) + 4 + ((l & 15) >> 2), 19) * 1280 + w * 160 + 8 * (l & 3);
    const u32 mo = MT_OFF + (u32)(100 * w + 5 * (sg >> 2)) * 1024 + ((l & 15) + 16 * (sg & 3)) * 16;
#pragma unroll
    for (int cb = 0; cb < 5; ++cb) {
#pragma unroll
      for (int hh = 0; hh < 2; ++hh) { const u32x2 lo = tr_read(ra + cb * 32 + hh * 25600), hi = tr_read(rb + cb * 32 + hh * 25600); WO[cb][hh] = u32x4{lo[0], lo[1], hi[0], hi[1]}; }
      bstore16(u32x4{SENT, SENT, SENT, SENT}, rs, mo + (l >> 4) * 1024 + cb * 20480);
      bstore16(u32x4{SENT, SENT, SENT, SENT}, rs, l < 16 ? mo + 4096 + cb * 20480 : OOB);
    }
    if (w >= 5) { const int e = tid - 320; bstore16(u32x4{SENT, SENT, SENT, SENT}, rs, e < 160 ? ZB_OFF + (e / 5) * 1280 + sg * 80 + (e % 5) * 16 : OOB); }
  }
  STAMP(3);
  publish<8, 256>(ws, FW_OFF + sg * 4, tag);
  STAMP(4);
#pragma unroll 1
  for (int pc = w; pc < 140; pc += 8) p2_piece((const char*)(wd + 80 * c2), pc, l);
  if (tid < 2) lds_st<u32x4>(L_CNT + tid * 16, u32x4{0u, 0u, 0u, 0u});
  if (sg == 0 && w < 4) {
    const u32 off = tid < 240 ? CSG_OFF + tid * 1536 + 4 : STG_OFF + (tid - 240) * 1024 + 4;
    wait_flags(ws, off, true, tag);
  }
  wait_vm<0>();
  __syncthreads();
  if (sg == 0 && tid == 0) __hip_atomic_store((GAS u32*)(ws + EP_OFF), tag, RLX_AGENT);
}

DI u32 p2_attblk(int u, int j) { return j < 14 ? L_ATT + (u * 14 + j) * 1024 : u * 25600 + 20480; }
constexpr int h_step(int t) { return t < 6 ? 5 + t : t < 11 ? t - 6 : t; }
constexpr bool h_lds(int t) { return t < 2 || (t >= 6 && t < 11); }
constexpr int a_first(int t) { return t < 0 ? 0 : t < 2 ? 4 + t : t < 5 ? 2 + 2 * t : 12; }
constexpr int lds_seq(int t, int cb) { return (t < 2 ? t : t - 4) * 5 + cb; }
template <int T> DI void p2_hsteps(f32x4 (&acc)[5][4], u32x4 (&A)[12][4], u32x4 (&B)[4][5], u32x4 (&bl)[3], rsrc_t rs, u32 va, const u32 (&so)[4], u32 vb, u32 wl, u32 wl2, bool extra, Poll& pl, const u16* xp, u32x2 (&xr)[5]) {
  if constexpr (T < 12) {
#pragma unroll
    for (int cb = 0; cb < 5; ++cb) {
      const u32x4 bc = h_lds(T) ? bl[lds_seq(T, cb) % 3] : B[T < 6 ? T - 2 : T - 11][cb];
      const int q = h_lds(T) ? lds_seq(T, cb) + 2 : (T == 5 && cb >= 3) ? 7 + cb : -1;
      if (q >= 0 && q < 35 && !(T == 1 && q >= 10)) bl[q % 3] = tr_frag((q < 10 ? wl2 + (q / 5) * 5120 : wl + (q / 5 - 2) * 5120) + (q % 5) * 32, 1280);
#pragma unroll
      for (int t = 0; t < 4; ++t) acc[cb][t] = mfma16(bc, A[T][t], acc[cb][t]);
      SB0;
    }
    if constexpr (T == 0) poll_done(pl);
    if constexpr (T == 6) {
#pragma unroll
      for (int cb = 0; cb < 5; ++cb) xr[cb] = *(const u32x2*)(xp + 16 * cb);
    }
    if constexpr (T == 1) lds_ticket(L_CNT + 4);
    if constexpr (T == 10) lds_ticket(L_CNT);
#pragma unroll
    for (int n = a_first(T); n < a_first(T + 1); ++n)
#pragma unroll
      for (int t = 0; t < 4; ++t) A[n][t] = bload(rs, n == 11 ? (extra ? va : OOB) : va, so[t] + h_step(n) * 1024);
    if constexpr (T == 5) {
#pragma unroll
      for (int cb = 0; cb < 5; ++cb) B[0][cb] = bload(rs, extra ? vb : OOB, cb * 55296 + 11 * 1024);
    }
    SB0;
    p2_hsteps<T + 1>(acc, A, B, bl, rs, va, so, vb, wl, wl2, extra, pl, xp, xr);
  }
}

template <bool JOB> DI void phase2(int r, int c, int job, bool late, u32 tag, const u16* x, const u16* wd, u16* out, char* ws, const u32x4 (&WO)[5][2]) {
  int tid = threadIdx.x;
  asm volatile("" : "+v"(tid), "+s"(ws), "+s"(tag));
  const int l = tid & 63, w = __builtin_amdgcn_readfirstlane(tid >> 6);
  const rsrc_t rs = __builtin_amdgcn_make_buffer_rsrc(ws, 0, WS_END, 0x00020000);
  const int g = l >> 4, n = l & 15;
  auto orow = [&]() { int t = threadIdx.x; asm volatile("" : "+v"(t)); return (u32)((64 * r + 16 * (w & 3) + (t & 15)) * 640 + 80 * c + ((t >> 2) & 12)); };
  const u32 fh = FH_OFF + c * 2048 + (r >> 2) * 256;
  STAMP(8);
  const int start = p2_start(w & 3);
  const u32 vb = WT_OFF + (u32)(5 * c * 54 + start) * 1024u + l * 16;
  u32x4 B[4][5];
  if (w < 4) {
    if (late) {
#pragma unroll 1
      for (int pc = max(100 + 10 * w, 116); pc < 110 + 10 * w; ++pc) p2_piece((const char*)(wd + 80 * c), pc, l);
    }
    wait_flags<24>(ws, FW_OFF + c * 256 + n * 4, l < 16, tag);
#pragma unroll
    for (int s = 0; s < 4; ++s) {
#pragma unroll
      for (int cb = 0; cb < 5; ++cb) B[s][cb] = bload(rs, vb, cb * 55296 + (7 + s) * 1024);
      SB0;
    }
  }
  if (JOB) mjob(job, tag, ws, rs, WO);
  if (w >= 4) {
    const int u = w - 4;
    u32x4 P[4][5];
    auto load_p = [&]() {
#pragma unroll
      for (int t = 0; t < 4; ++t) {
#pragma unroll
        for (int s = 0; s < 5; ++s) P[t][s] = bload(rs, ACT_OFF + (u32)(4 * r * NSTEP + 54 + 5 * u) * 1024u + l * 16, ((t + u) & 3) * (NSTEP * 1024) + s * 1024);
        SB0;
      }
    };
    __builtin_amdgcn_s_setprio(2);
    if (u == 0) { wait_flags<8>(ws, l < 16 ? FW_OFF + c * 256 + l * 4 : fh + (18 + (l & 3)) * 4, l < 20, tag); lds_ticket(L_CNT + 20); } else lds_wait(L_CNT + 20, 1);
    load_p();
    const int st2 = start + 11 + (u < 2);
    u32x4 B2[2][5];
#pragma unroll
    for (int s = 0; s < 2; ++s)
#pragma unroll
      for (int cb = 0; cb < 5; ++cb) B2[s][cb] = bload(rs, WT_OFF + (u32)(5 * c * 54 + st2) * 1024u + l * 16, cb * 55296 + s * 1024);
    SB0;
    __builtin_amdgcn_s_setprio(0);
    STAMP(9);
    f32x4 acc[5][4];
    const u32 mhint = MT_OFF + (u32)((5 * c + 4) * 20 + 5 * u + 4) * 1024u + g * 256 + 252;
    float rd[4][2];
    u32 mh, mh2;
    {
      u32x4 A2[2][4], Z[5];
      auto load_z = [&]() {
#pragma unroll
        for (int s = 0; s < 5; ++s) Z[s] = bload(rs, ZB_OFF + r * 1280 + 320 * u + 80 * g, s * 16);
        mh = __builtin_amdgcn_raw_buffer_load_b32(rs, mhint, 0, 16);
      };
      auto bad = [&]() {
        u32 mx = 0;
#pragma unroll
        for (int s = 0; s < 5; ++s) mx = max4(mx, Z[s]);
        return __any(mx == SENT);
      };
      lds_wait(L_CNT + 16, 1);
#pragma unroll
      for (int s = 0; s < 2; ++s)
#pragma unroll
        for (int t = 0; t < 4; ++t) A2[s][t] = bload(rs, ACT_OFF + (u32)(4 * r * NSTEP + st2) * 1024u + l * 16, ((t + u) & 3) * (NSTEP * 1024) + s * 1024);
      SB0;
      load_z();
      SB0;
#pragma unroll
      for (int cb = 0; cb < 5; ++cb) {
#pragma unroll
        for (int t = 0; t < 4; ++t) { acc[cb][t] = mfma16(B2[1][cb], A2[1][t], mfma16(B2[0][cb], A2[0][t], f32x4{0.f, 0.f, 0.f, 0.f})); asm volatile("" : "+v"(acc[cb][t])); }
        if (cb == 0) { SB0; asm volatile("" ::: "memory"); mh2 = __builtin_amdgcn_raw_buffer_load_b32(rs, mhint, 0, 16); SB0; }
      }
      STAMP(14);
      if (bad()) {
#pragma unroll 1
        for (int spin = 0; spin < SPIN_CAP; ++spin) { __builtin_amdgcn_s_sleep(8); asm volatile("" ::: "memory"); load_z(); if (!bad()) break; }
      }
      const bool m0 = n == 4 * g, m1 = n == 4 * g + 1;
#pragma unroll
      for (int s = 0; s < 5; ++s)
#pragma unroll
        for (int q = 0; q < 4; ++q) Z[s][q] = ((8 * s + 2 * q >= 20) ? m1 : m0) ? Z[s][q] : 0u;
#pragma unroll
      for (int t = 0; t < 4; ++t) {
        f32x4 den = {0.f, 0.f, 0.f, 0.f};
#pragma unroll
        for (int s = 0; s < 5; ++s) den = mfma16(Z[s], P[t][s], den);
        rd[t][0] = __builtin_amdgcn_rcpf(den[0] + 1e-6f); rd[t][1] = __builtin_amdgcn_rcpf(den[1] + 1e-6f);
      }
    }
    STAMP(11);
    if (__any(mh == SENT) && __any(mh2 == SENT)) wait_data<4>(rs, mhint);
    STAMP(10);
    u32x4 M[5][5];
    const u32 vm = MT_OFF + (u32)(5 * c * 20 + 5 * u) * 1024u + l * 16;
    auto load_m = [&](int c0, int c1) {
#pragma unroll
      for (int cb = 0; cb < 5; ++cb) {
        if (cb < c0 || cb >= c1) continue;
#pragma unroll
        for (int s = 0; s < 5; ++s) M[cb][s] = bload(rs, vm, (cb * 20 + s) * 1024);
      }
    };
    __builtin_amdgcn_s_setprio(2);
#pragma unroll
    for (int t = 0; t < 4; ++t)
#pragma unroll
      for (int s = 0; s < 5; ++s) {
        if (t < 3) M[t][s] = bload(rs, vm, (t * 20 + s) * 1024);
#pragma unroll
        for (int q = 0; q < 4; ++q) { const float rr = rd[t][8 * s + 2 * q >= 20]; P[t][s][q] = pk_bf16(bf_lo(P[t][s][q]) * rr, bf_hi(P[t][s][q]) * rr); }
        asm volatile("" : "+v"(P[t][s]));
        SB0;
      }
    __builtin_amdgcn_s_setprio(APRIO);
    lds_wait(L_CNT + 4, 4);
    STAMP(17);
#pragma unroll
    for (int cb = 0; cb < 5; ++cb) {
      auto bad = [&]() {
        u32 mx = 0;
#pragma unroll
        for (int s = 0; s < 5; ++s) mx = max4(mx, M[cb][s]);
        return __any(mx == SENT);
      };
      if (__builtin_expect(bad(), 0)) {
#pragma unroll 1
        for (int spin = 0; spin < SPIN_CAP; ++spin) { asm volatile("" ::: "memory"); load_m(cb, cb + 3); if (!bad()) break; }
      }
#pragma unroll
      for (int s = 0; s < 5; ++s)
#pragma unroll
        for (int t = 0; t < 4; ++t) acc[cb][t] = mfma16(M[cb][s], P[t][s], acc[cb][t]);
      if (cb < 2) { SB0; load_m(cb + 3, cb + 4); }
#pragma unroll
      for (int t = 1; t < 4; ++t) if (!(cb == 4 && t == 3)) lds_st<f32x4>(p2_attblk(u, cb * 3 + (t - 1)) + l * 16, acc[cb][t]);
    }
    STAMP(16);
    lds_wait(L_CNT, 4);
#pragma unroll
    for (int k = 0; k < 6; ++k) lds_st<f32x4>(u * 25600 + (15 + k) * 1024 + l * 16, k < 5 ? acc[k][0] : acc[4][3]);
    STAMP(15);
    __syncthreads();
    if constexpr (!JOB) asm volatile(".Lfk_end_a:");
    __builtin_amdgcn_endpgm();
  } else {
    const u32 va = ACT_OFF + (u32)(4 * r * NSTEP + start) * 1024u + l * 16;
    u32 so[4];
#pragma unroll
    for (int t = 0; t < 4; ++t) so[t] = ((t + w) & 3) * (NSTEP * 1024);
    const u32 wi = (16 * (g >> 1) + 4 * (g & 1) + (n >> 2)) * 160 + 8 * (l & 3), wl = w * 25600 + wi, wl2 = L_ATT + w * 10240 + wi;
    u32x4 A[12][4], bl[3];
    bl[0] = tr_frag(wl2, 1280); bl[1] = tr_frag(wl2 + 32, 1280);
    Poll pl = {};
    if (w == 0) { poll_flags(pl, ws, fh + min(l, 17) * 4, tag); lds_ticket(L_CNT + 16); } else lds_wait(L_CNT + 16, 1);
    STAMP(9);
    __builtin_amdgcn_s_setprio(HPRIO);
#pragma unroll
    for (int s = 0; s < 4; ++s) {
#pragma unroll
      for (int t = 0; t < 4; ++t) A[s][t] = bload(rs, va, so[t] + h_step(s) * 1024);
      SB0;
    }
    f32x4 acc[5][4];
#pragma unroll
    for (int cb = 0; cb < 5; ++cb)
#pragma unroll
      for (int t = 0; t < 4; ++t) acc[cb][t] = f32x4{0.f, 0.f, 0.f, 0.f};
    const u32 oo = (u32)((64 * r + 16 * w + n) * 640 + 80 * c + 4 * g);
    u32x2 xr[5];
    p2_hsteps<0>(acc, A, B, bl, rs, va, so, vb, wl, wl2, w < 2, pl, x + oo, xr);
    STAMP(10);
#pragma unroll
    for (int cb = 0; cb < 5; ++cb)
#pragma unroll
      for (int t = 1; t < 4; ++t) lds_st<f32x4>(w * 25600 + (cb * 3 + (t - 1)) * 1024 + l * 16, acc[cb][t]);
#pragma unroll
    for (int cb = 0; cb < 5; ++cb) { acc[cb][0] += f32x4{bf_lo(xr[cb][0]), bf_hi(xr[cb][0]), bf_lo(xr[cb][1]), bf_hi(xr[cb][1])}; asm volatile("" : "+v"(acc[cb][0])); }
    lds_meet(L_CNT + 8, 4);
#pragma unroll
    for (int cb = 0; cb < 5; ++cb) {
#pragma unroll
      for (int o = 1; o < 4; ++o) acc[cb][0] += lds_ld<f32x4>(((w + o) & 3) * 25600 + (cb * 3 + (3 - o)) * 1024 + l * 16);
      asm volatile("" : "+v"(acc[cb][0]));
    }
    STAMP(11);
    __syncthreads();
    STAMP(12);
#pragma unroll
    for (int cb = 0; cb < 5; ++cb) {
      f32x4 s = acc[cb][0];
      s += lds_ld<f32x4>(w * 25600 + (15 + cb) * 1024 + l * 16);
#pragma unroll
      for (int o = 1; o < 4; ++o) s += lds_ld<f32x4>(p2_attblk((w + o) & 3, cb * 3 + (3 - o)) + l * 16);
      *(u32x2*)(out + oo + 16 * cb) = u32x2{pk_bf16(s[0], s[1]), pk_bf16(s[2], s[3])};
    }
    STAMP(13);
    if constexpr (!JOB) asm volatile(".Lfk_end_h:");
    __builtin_amdgcn_endpgm();
  }
}

__host__ __device__ inline void decode(int bid, int& rt, int& ct) {
  const int xcd = bid & 7, idx = bid >> 3, cgrp = xcd >> 1, ci = idx >> 2;
  rt = 4 * (xcd & 1) + (idx & 3);
  ct = cgrp < 2 ? cgrp * 8 + ci : ci == 7 ? 30 : ci < 4 ? 18 + 4 * (cgrp - 2) + ci : ci < 6 ? 26 + 2 * (cgrp - 2) + ci - 4 : 16 + (cgrp - 2);
  if (ct == 30) rt += 8 * (cgrp - 2);
}

__global__ __launch_bounds__(512, 2) void fused_kernel(const u16* __restrict__ x, const u16* __restrict__ wqkv, const u16* __restrict__ wo,
                                                       const u16* __restrict__ wg, const u16* __restrict__ wu, const u16* __restrict__ wd,
                                                       u16* __restrict__ out, char* ws) {
  STAMP(0);
  const int bid = blockIdx.x, xcd = bid & 7, idx = bid >> 3;
  const int r2 = xcd * 4 + (idx >> 3), c2 = idx & 7;
  {
    const char* pc; u32 n0, n1;
    asm volatile("s_getpc_b64 %0\n.Lfk_beg:\n\ts_mov_b32 %1, .Lfk_end_h-.Lfk_beg\n\ts_mov_b32 %2, .Lfk_end_a-.Lfk_beg" : "=s"(pc), "=s"(n0), "=s"(n1));
    const int w = __builtin_amdgcn_readfirstlane(threadIdx.x >> 6);
    const u32 o = (u32)(idx * 4 + (w & 3)) * 1024u;
    if (w < 4 && o + 1024u <= max(n0, n1)) dma16((const char*)((size_t)pc & ~(size_t)15) + o, (threadIdx.x & 63) * 16, L_SP + (w & 3) * 1024);
  }
  int rt, ct;
  decode(bid, rt, ct);
  u32 tag;
  u32x4 WO[5][2];
  if (ct == 30) {
    spare(rt, c2, x, wo, wd, ws, tag, WO);
    phase2<true>(r2, c2, rt, false, tag, x, wd, out, ws, WO);
  } else {
    if (ct < 18) phase1<0>(rt, ct, ct, c2, x, wqkv, wo, wg, wu, wd, ws, tag);
    else if (ct < 26) phase1<2>(rt, ct, ct - 18, c2, x, wqkv, wo, wg, wu, wd, ws, tag);
    else phase1<1>(rt, ct, ct - 26, c2, x, wqkv, wo, wg, wu, wd, ws, tag);
    phase2<false>(r2, c2, -1, ct >= 18 && ct < 26, tag, x, wd, out, ws, WO);
  }
}

extern "C" void kernel_launch(void* const* d_in, const int* in_sizes, int n_in, void* d_out, int out_size, void* d_ws, size_t ws_size, hipStream_t stream) {
  (void)hipFuncSetAttribute((const void*)fused_kernel, hipFuncAttributeMaxDynamicSharedMemorySize, LDS_BYTES);
  fused_kernel<<<256, 512, LDS_BYTES, stream>>>((const u16*)d_in[0], (const u16*)d_in[1], (const u16*)d_in[2], (const u16*)d_in[4], (const u16*)d_in[5],
                                                (const u16*)d_in[6], (u16*)d_out, (char*)d_ws);
}
```

```cpp
#include <hip/hip_runtime.h>
#include <stdint.h>
#include <type_traits>

#define ABL_NOREAD 0
#define ABL_NODMA 0
#define APRIO 0
#define HPRIO 1
#define MAPPING 2
#define ABL_NOROT 0
typedef unsigned short u16;
typedef unsigned int u32;
typedef unsigned long long u64;
typedef __bf16 bf16x8 __attribute__((ext_vector_type(8)));
typedef __bf16 bf16x2 __attribute__((ext_vector_type(2)));
typedef short s16x4 __attribute__((ext_vector_type(4)));
typedef float f32x2 __attribute__((ext_vector_type(2)));
typedef float f32x4 __attribute__((ext_vector_type(4)));
typedef float f32x16 __attribute__((ext_vector_type(16)));
typedef u32 u32x2 __attribute__((ext_vector_type(2)));
typedef u32 u32x4 __attribute__((ext_vector_type(4)));
typedef __amdgpu_buffer_rsrc_t rsrc_t;
#define LAS __attribute__((address_space(3)))
#define GAS __attribute__((address_space(1)))
#define DI __device__ __forceinline__
#define RLX_AGENT __ATOMIC_RELAXED, __HIP_MEMORY_SCOPE_AGENT
#define SB0 __builtin_amdgcn_sched_barrier(0)

constexpr int NSTEP = 74;
constexpr u32 ACT_OFF = 0;
constexpr u32 MT_OFF  = ACT_OFF + 128u * NSTEP * 1024u;
constexpr u32 WT_OFF  = MT_OFF + 40u * 20u * 1024u;
constexpr u32 KVR_OFF = WT_OFF + 40u * 54u * 1024u;
constexpr u32 ZB_OFF  = KVR_OFF + 16u * 8u * 3840u;
constexpr u32 STG_OFF = ZB_OFF + 32u * 1280u;
constexpr u32 CSG_OFF = STG_OFF + 2048u * 8u;
constexpr u32 FH_OFF  = CSG_OFF + 30u * 8u * 192u * 8u;
constexpr u32 FK_OFF  = FH_OFF + 64u * 256u;
constexpr u32 FW_OFF  = FK_OFF + 8u * 256u;
constexpr u32 EP_OFF  = FW_OFF + 8u * 256u;
constexpr u32 WS_END  = EP_OFF + 256u;
constexpr u32 OOB = 0x7ffffff0u;
constexpr int SPIN_CAP = 1 << 15;

constexpr int L_W = 98304, L_SP = 159744, L_ATT = 102400, L_KX = 119808, L_MJ = 143360, L_CNT = 163808, LDS_BYTES = 163840;
constexpr int XB0 = 65536, XB1 = 0, XB2 = 32768;

#define STAMP(i) do {} while (0)
#define SSTAMP(i) do {} while (0)

DI float bf_lo(u32 v) { return __uint_as_float(v << 16); }
DI float bf_hi(u32 v) { return __uint_as_float(v & 0xffff0000u); }
DI u32 pk_bf16(float a, float b) { f32x2 v = {a, b}; return __builtin_bit_cast(u32, __builtin_convertvector(v, bf16x2)); }
DI float round_bf(float a) { return bf_lo(pk_bf16(a, a)); }
DI float dot2(u32 a, u32 b, float c) { return __builtin_amdgcn_fdot2_f32_bf16(__builtin_bit_cast(bf16x2, a), __builtin_bit_cast(bf16x2, b), c, false); }
DI float xsum32(float v) { auto r = __builtin_amdgcn_permlane32_swap(__float_as_uint(v), __float_as_uint(v), false, false); return __uint_as_float(r[0]) + __uint_as_float(r[1]); }
DI float xsum16(float v) { auto r = __builtin_amdgcn_permlane16_swap(__float_as_uint(v), __float_as_uint(v), false, false); return __uint_as_float(r[0]) + __uint_as_float(r[1]); }
DI f32x16 mfma32(u32x4 a, u32x4 b, f32x16 c) { return __builtin_amdgcn_mfma_f32_32x32x16_bf16(__builtin_bit_cast(bf16x8, a), __builtin_bit_cast(bf16x8, b), c, 0, 0, 0); }
DI f32x4 mfma16(u32x4 a, u32x4 b, f32x4 c) { return __builtin_amdgcn_mfma_f32_16x16x32_bf16(__builtin_bit_cast(bf16x8, a), __builtin_bit_cast(bf16x8, b), c, 0, 0, 0); }
DI u32x2 tr_read(u32 a) { return __builtin_bit_cast(u32x2, __builtin_amdgcn_ds_read_tr16_b64_v4i16((LAS s16x4*)(size_t)a)); }
DI u32x4 tr_frag(u32 a, int d) { const u32x2 lo = tr_read(a), hi = tr_read(a + d); return u32x4{lo[0], lo[1], hi[0], hi[1]}; }
template <class T> DI T lds_ld(u32 a) { return *(const LAS T*)(size_t)a; }
template <class T> DI void lds_st(u32 a, T v) { *(LAS T*)(size_t)a = v; }
DI void dma16(const char* sbase, u32 voff, u32 ldst) {
  asm volatile("s_mov_b32 m0, %2\n\ts_nop 0\n\tglobal_load_lds_dwordx4 %0, %1" ::"v"(voff), "s"(sbase), "s"(ldst) : "memory");
}
template <int N> DI void wait_vm() { asm volatile("s_waitcnt vmcnt(%0)" ::"n"(N) : "memory"); }
DI void wait_lgkm0() { __builtin_amdgcn_s_waitcnt(0xC07F); }
DI void bar() { __builtin_amdgcn_s_barrier(); }
DI u32x4 bload(rsrc_t r, u32 voff, u32 soff) { return __builtin_amdgcn_raw_buffer_load_b128(r, voff, soff, 16); }
DI void bstore16(u32x4 v, rsrc_t r, u32 voff) { __builtin_amdgcn_raw_buffer_store_b128(v, r, voff, 0, 16); }
DI void bstore8(u32x2 v, rsrc_t r, u32 voff) { __builtin_amdgcn_raw_buffer_store_b64(v, r, voff, 0, 16); }
constexpr u32 SENT = 0xffffffffu;
DI u32 nosent(u32 v) { return min(v, SENT - 1u); }
DI u32x4 nosent4(u32x4 v) { return u32x4{nosent(v[0]), nosent(v[1]), nosent(v[2]), nosent(v[3])}; }
DI u32 max4(u32 m, u32x4 v) { return max(max(m, max(v[0], v[1])), max(v[2], v[3])); }
constexpr float LOG2E = 1.4426950408889634f;
template <int SLEEP = 4> DI void wait_flags(const char* ws, u32 off, bool on, u32 tag) {
  GAS u32* p = (GAS u32*)(ws + off);
#pragma unroll 1
  for (int spin = 0; spin < SPIN_CAP; ++spin) {
    const u32 v = on ? __hip_atomic_load(p, RLX_AGENT) : tag;
    if (__all(v == tag)) break;
    __builtin_amdgcn_s_sleep(SLEEP);
  }
  __builtin_amdgcn_fence(__ATOMIC_ACQUIRE, "wavefront");
  asm volatile("" ::: "memory");
}
template <int SLEEP> DI void wait_data(rsrc_t rs, u32 off) {
#pragma unroll 1
  for (int spin = 0; spin < SPIN_CAP; ++spin) {
    if (!__any(__builtin_amdgcn_raw_buffer_load_b32(rs, off, 0, 16) == SENT)) break;
    __builtin_amdgcn_s_sleep(SLEEP);
    asm volatile("" ::: "memory");
  }
  asm volatile("" ::: "memory");
}
struct Poll { u32 v[2]; };
DI void poll_flags(Poll& p, const char* ws, u32 off, u32 tag) {
  u32 cnt = SPIN_CAP;
#define POLL_STEP(LD, CK) "s_sleep 4\n\tglobal_load_dword %" #LD ", %3, %4 sc1\n\ts_waitcnt vmcnt(1)\n\tv_cmp_eq_u32 vcc, %5, %" #CK "\n\ts_nop 0\n\ts_cmp_eq_u64 vcc, -1\n\ts_cbranch_scc1 .Lpd%=\n\t"
  asm volatile("global_load_dword %0, %3, %4 sc1\n"
               ".Lpl%=:\n\t" POLL_STEP(1, 0) POLL_STEP(0, 1)
               "s_sub_u32 %2, %2, 1\n\ts_cmp_lg_u32 %2, 0\n\ts_cbranch_scc1 .Lpl%=\n"
               ".Lpd%=:"
               : "=&v"(p.v[0]), "=&v"(p.v[1]), "+s"(cnt) : "v"(off), "s"(ws), "s"(tag) : "vcc", "scc", "memory");
#undef POLL_STEP
}
DI void poll_done(Poll& p) { asm volatile("" ::"v"(p.v[0]), "v"(p.v[1])); }
template <int REP> DI void raise_flag(char* ws, u32 off, u32 tag) {
  asm volatile("s_mov_b64 exec, %3\n\tglobal_store_dword %0, %1, %2 sc1\n\ts_mov_b64 exec, -1" ::"v"(off), "v"(tag), "s"(ws), "s"((1ull << REP) - 1) : "memory");
}
template <int REP = 1, int STRIDE = 0, int KEEP = 0> DI void publish(char* ws, u32 off, u32 tag) {
  wait_vm<KEEP>();
  wait_lgkm0(); bar();
  asm volatile("" ::: "memory");
  if (__builtin_amdgcn_readfirstlane(threadIdx.x) == 0) raise_flag<REP>(ws, off + threadIdx.x * STRIDE, tag);
}

struct Sched { int op[96]; int first[20]; };
constexpr Sched make_sched(int n16) {
  Sched r{}; int n = 0;
  for (int k = 0; k < 4; ++k) r.op[n++] = 100 + k;
  r.op[n++] = 0; r.op[n++] = 1; r.op[n++] = 2;
  for (int k = 0; k < 4; ++k) r.op[n++] = 104 + k;
  r.op[n++] = 3; r.op[n++] = 4;
  for (int s = 0; s < 18; ++s) {
    r.first[s + 1] = n;
    const int it = s / 2;
    if (s == 13) { r.op[n++] = 1000; r.op[n++] = 1000; }
    if (s == 0) { r.op[n++] = 5; r.op[n++] = 108; r.op[n++] = 109; r.op[n++] = 6; }
    if (s <= 14) {
      if ((s & 1) == 0) { r.op[n++] = 7 + 3 * it; r.op[n++] = 8 + 3 * it; r.op[n++] = 100 + 4 * (it + 2) + 2; r.op[n++] = 100 + 4 * (it + 2) + 3; }
      else { r.op[n++] = 9 + 3 * it; r.op[n++] = 100 + 4 * (it + 3); r.op[n++] = 100 + 4 * (it + 3) + 1; }
    } else for (int k = 0; k < (s == 15 ? n16 / 100 : s == 16 ? n16 / 10 % 10 : n16 % 10); ++k) r.op[n++] = 1000;
  }
  r.first[19] = n;
  return r;
}
constexpr int vm_need(int n16, int t, int b) {
  const Sched sch = make_sched(n16);
  const int issued = b < 0 ? 7 : sch.first[b + 1];
  int last = -1;
  for (int k = 0; k < issued; ++k) {
    const int op = sch.op[k];
    if (op < 100 ? (8 * op <= 12 * t + 11) : (op < 1000 && (op - 100) / 4 <= t / 2)) last = k;
  }
  return issued - 1 - last;
}

struct Frag { u32x4 x[2][2]; u32x4 w[3][2]; };

DI int p2_start(int i) { return i * 13 + min(i, 2); }
DI int p2_blkstep(int blk) { const int i = blk < 20 ? blk / 5 : (blk - 20) >> 1; return p2_start(i) + (blk < 20 ? blk - 5 * i : 5 + (blk & 1)); }
DI u32 p2_voff(int p5, int l) { const int n = p5 * 64 + l, r = n / 10, c = n - 10 * r; return (u32)((r & 19) | ((r & 4) << 1) | ((r & 8) >> 1)) * 1280u + c * 16; }
DI void p2_piece(const char* wdc, int piece, int l) {
  const int blk = piece / 5;
  dma16(wdc + (size_t)p2_blkstep(blk) * 40960, p2_voff(piece - 5 * blk, l), piece * 1024);
}

template <int TYPE> DI f32x16 p1_mfma(u32x4 w, u32x4 x, f32x16 c) { return TYPE == 2 ? mfma32(x, w, c) : mfma32(w, x, c); }

template <int TYPE, class F> DI void p1_step(f32x16 (&acc)[3][2], const Frag& cur, bool third, F&& fill) {
#pragma unroll
  for (int i = 0; i < 8; ++i) {
    const int b = (i >> 1) & 1, k = i >> 2, t = i & 1;
    acc[b][t] = p1_mfma<TYPE>(cur.w[b][k], cur.x[t][k], acc[b][t]);
    fill(i);
    SB0;
  }
  if (TYPE != 1 || third) {
#pragma unroll
    for (int i = 8; i < 12; ++i) {
      const int k = (i >> 1) & 1, t = i & 1;
      if (TYPE == 2 && t == 1) continue;
      acc[2][t] = p1_mfma<TYPE>(cur.w[2][k], cur.x[t][k], acc[2][t]);
      SB0;
    }
  }
}
template <int I, int N, class F> DI void static_for(F&& f) { if constexpr (I < N) { f(std::integral_constant<int, I>{}); static_for<I + 1, N>(f); } }
constexpr int xslot(int i) { return i % 3 == 0 ? XB0 : (i % 3 == 1 ? XB1 : XB2); }

DI float exp2_le1(float t) { return __builtin_amdgcn_fmed3f(__builtin_amdgcn_exp2f(t), 0.f, 1.f); }
DI float phi_f(float t) { return fmaxf(t, 1e-6f) + exp2_le1(t * LOG2E); }

template <int TYPE>
DI void phase1(u64 T0_, int rt, int ct, int j, int c2, const u16* x, const u16* wqkv, const u16* wo, const u16* wg, const u16* wu, const u16* wd, char* ws, u32& tag_out) {
  const int tid = threadIdx.x, l = tid & 63, w = __builtin_amdgcn_readfirstlane(tid >> 6);
  const int tg = w & 3, cgp = w >> 2, h = l >> 5, n = l & 31;
  const int k0 = ABL_NOROT ? 0 : (rt * 10) >> 3;
  const char* xg = (const char*)x + (size_t)rt * 256 * 1280;
  const u32 xvo = (u32)(8 * w + (l >> 3)) * 1280 + (((l & 7) ^ ((4 * (w & 1) + (l >> 4)) & 7)) * 16);
  auto dma_x = [&](int kk, int xi, u32 slot) { dma16(xg + (xi * 64 * 1280 + kk * 128), xvo, slot + xi * 8192 + w * 1024); };
#pragma unroll
  for (int xi = 0; xi < 4; ++xi) dma_x(k0, xi, XB0);
  const char* wb[3]; u32 wvo[3];
  constexpr u32 STRIDE = TYPE == 0 ? 3456 : 3840, WPER = 64 * STRIDE;
  {
    const char* b0 = TYPE == 0 ? (const char*)(wg + 96 * j) : TYPE == 1 ? (const char*)(wqkv + 160 * j) : (const char*)(wqkv + 640 + 80 * j);
    const char* b1 = TYPE == 0 ? (const char*)(wu + 96 * j) : TYPE == 1 ? b0 + 192 : b0 + 1280;
#pragma unroll
    for (int i = 0; i < 3; ++i) {
      const int pp = 8 * i + w, st = pp >= 12, pi = pp - 12 * st, part = pi >= 6;
      const int m = (pi - 6 * part) * 64 + l, row = m / 12, cp = m - 12 * row;
      int ch = part ? cp ^ 2 : cp;
      if (TYPE == 1 && part && ch >= 8) ch -= 4;
      if (TYPE == 2 && ch >= 10) ch -= 4;
      wb[i] = part ? b1 : b0;
      wvo[i] = (u32)(st * 32 + row) * STRIDE + ch * 16;
    }
  }
  const u32 wl0 = L_W + w * 1024, wl1 = w >= 4 ? wl0 - 61440 : wl0;
  auto dma_w = [&](int kk, int grp) { const int pos = (8 * grp) % 60; dma16(wb[grp % 3] + (size_t)((u32)kk * WPER), wvo[grp % 3], (pos == 56 ? wl1 : wl0) + pos * 1024); };
  auto rot = [&](int kk) { return kk + 1 >= 10 ? kk - 9 : kk + 1; };
  const int k1 = rot(k0), k2 = rot(k1);
  dma_w(k0, 0); dma_w(k0, 1); dma_w(k0, 2);
  u32 ep;
  asm volatile("s_load_dword %0, %1, 0x0" : "=s"(ep) : "s"(ws + EP_OFF) : "memory");

  auto bphys = [&](int b) { int v = b + tg; v = v >= 3 ? v - 3 : v; return v >= 3 ? v - 3 : v; };
  u32 xa[2][4], wa[3][2];
  {
    const int ksw = TYPE == 2 ? (tg & 1) : 0;
#pragma unroll
    for (int t = 0; t < 2; ++t) {
      const int row = 64 * tg + 32 * (TYPE == 2 ? t ^ cgp : t) + n;
#pragma unroll
      for (int k = 0; k < 4; ++k) xa[t][k] = row * 128 + 16 * ((2 * (k ^ ksw) + h) ^ ((row >> 1) & 7));
    }
    const int blk = (l >> 4) & 1, q = (l & 15) >> 2, p = l & 3, rowb = L_W + (8 * h + q) * 192;
    if (TYPE == 2) {
      const int a = cgp ? 3 : 0;
      const u32 b0 = rowb + (a + blk) * 32 + 8 * p, b1 = 6144 + rowb + ((a + blk) ^ 1) * 32 + 8 * p;
      const u32 b2 = rowb + (p >> 1) * 6144 + ((4 + (cgp ^ blk)) ^ (p & 2)) * 16 + 8 * (p & 1);
#pragma unroll
      for (int k = 0; k < 2; ++k) { const u32 o = (k ^ ksw) * 3072; wa[0][k] = b0 + o; wa[1][k] = b1 + o; wa[2][k] = b2 + o; }
    } else {
#pragma unroll
      for (int b = 0; b < 3; ++b) {
        const u32 in32 = 16 * (p & 1) + 8 * (p >> 1);
        if (TYPE == 0) wa[b][0] = rowb + blk * 6144 + ((3 * cgp + bphys(b)) ^ blk) * 32 + in32;
        else wa[b][0] = rowb + cgp * 6144 + ((2 * (cgp ? (b == 2 ? 2 : b ^ (tg & 1)) : bphys(b)) + blk) ^ cgp) * 32 + in32;
        wa[b][1] = 0;
      }
    }
  }
  auto rd_w = [&](int b, int k, int off) { return tr_frag((TYPE == 2 ? wa[b][k] : wa[b][0] + k * 3072) + off, 768); };
  auto rd_x = [&](int t, int k4, int off) {
    return TYPE == 2 ? lds_ld<u32x4>(xa[t][k4] + off) : lds_ld<u32x4>(xa[0][k4] + off + t * 4096);
  };
  auto fill_rd = [&](Frag& f, int i, int st) {
    const int wo_ = (st % 5) * 12288, xo = xslot(st / 2), k4 = 2 * (st & 1);
    if (i == 0) { f.w[0][0] = rd_w(0, 0, wo_); f.x[0][0] = rd_x(0, k4, xo); }
    if (i == 1) { f.x[1][0] = rd_x(1, k4, xo); f.w[1][0] = rd_w(1, 0, wo_); }
    if (i == 2) { f.w[0][1] = rd_w(0, 1, wo_); f.x[0][1] = rd_x(0, k4 + 1, xo); }
    if (i == 3) { f.x[1][1] = rd_x(1, k4 + 1, xo); f.w[1][1] = rd_w(1, 1, wo_); }
    if (i == 4) { f.w[2][0] = rd_w(2, 0, wo_); f.w[2][1] = rd_w(2, 1, wo_); }
  };
  f32x16 acc[3][2];
#pragma unroll
  for (int b = 0; b < 3; ++b)
#pragma unroll
    for (int t = 0; t < 2; ++t)
#pragma unroll
      for (int i = 0; i < 16; ++i) acc[b][t][i] = 0.f;
  Frag fa, fb;
  STAMP(1);
  asm volatile("s_waitcnt vmcnt(%1) lgkmcnt(0)" : "+s"(ep) : "n"(vm_need(0, 0, -1)) : "memory");
  bar(); SB0;
  const u32 tag = ep + 1u;
  tag_out = tag;
#pragma unroll
  for (int i = 0; i < 5; ++i) fill_rd(fa, i, 0);
  SB0;
#pragma unroll
  for (int xi = 0; xi < 4; ++xi) dma_x(k1, xi, XB1);
  dma_w(k1, 3); dma_w(k1, 4);
  SB0;
  STAMP(2);
  const bool third = cgp == 0;
  float cs[3] = {0.f, 0.f, 0.f};
  auto colsum = [&](const Frag& f, int i) {
    constexpr u32 ones = 0x3f803f80u;
    if (TYPE == 2) {
      if (i < 6) { const int b = i >> 1; cs[b] = dot2(f.w[b][0][2 * (i & 1)], ones, cs[b]); cs[b] = dot2(f.w[b][0][2 * (i & 1) + 1], ones, cs[b]); asm volatile("" : "+v"(cs[b])); }
    } else { cs[0] = dot2(f.w[0][i >> 2][i & 3], ones, cs[0]); asm volatile("" : "+v"(cs[0])); }
  };
  auto colid = [&](int b) {
    if (TYPE == 2) return b == 2 ? ((n >> 3) & 1) * 96 + 32 + 8 * (cgp ^ (n >> 4)) + (n & 7) : b * 96 + (cgp ? 48 : 0) + n;
    const int p = (n & 15) >> 2, c16 = 8 * (p & 1) + 4 * (p >> 1) + (n & 3);
    if (TYPE == 0) return (n >> 4) * 96 + (3 * cgp + bphys(b)) * 16 + c16;
    return cgp * 96 + (cgp ? (b == 2 ? 2 : b ^ (tg & 1)) : bphys(b)) * 32 + (n & 16) + c16;
  };
  GAS u64* csg = (GAS u64*)(ws + CSG_OFF) + ct * 1536 + rt * 192;
  const rsrc_t rs = __builtin_amdgcn_make_buffer_rsrc(ws, 0, WS_END, 0x00020000);
  int ka = k2, kb = rot(k2);
  const char* wdc = (const char*)(wd + 80 * c2);
  u32x4 gr[2];
  u32 goff[2], pvo[5];
  f32x2 gs[2];
  auto gload = [&]() {
#pragma unroll
    for (int k = 0; k < 2; ++k) asm volatile("global_load_dwordx4 %0, %1, %2 sc1" : "=v"(gr[k]) : "v"(goff[k]), "s"(ws) : "memory");
  };
  auto gcheck = [&]() {
    asm volatile("" : "+v"(gr[0]), "+v"(gr[1]));
    return __all(gr[0][1] == tag && gr[0][3] == tag && gr[1][1] == tag && gr[1][3] == tag);
  };
  auto oct_sum = [&](u32 u) {
    float v = __uint_as_float(u);
    v += __uint_as_float(__builtin_amdgcn_mov_dpp(__float_as_uint(v), 0xB1, 0xf, 0xf, true));
    v += __uint_as_float(__builtin_amdgcn_mov_dpp(__float_as_uint(v), 0x4E, 0xf, 0xf, true));
    return v + __uint_as_float(__builtin_amdgcn_mov_dpp(__float_as_uint(v), 0x141, 0xf, 0xf, true));
  };
  auto pf2 = [&](int base) {
    constexpr int NP = TYPE == 2 ? 116 : 140;
    const bool clip = base + 8 > NP;
    const int piece = base + (clip ? w & 3 : w), blk = piece / 5;
    u32 vo = pvo[base % 5];
    if (clip) { u32 v1 = pvo[(base + 1) % 5]; asm volatile("" : "+v"(v1)); vo = w < 4 ? vo : v1; }
    const int lo = blk / 5, hi = (blk - 20) >> 1;
    const int st = base + 8 <= 100 || clip && base < 100 ? p2_start(lo) + blk - 5 * lo : base >= 100 ? p2_start(hi) + 5 + (blk & 1) : (w < 4 ? 45 : 5);
    dma16(wdc + (u32)st * 40960u, vo, piece * 1024);
  };
  constexpr int N16 = 3, NX = TYPE == 2 ? 534 : 434;
  auto bp_of = [&](int b) { return TYPE == 0 ? bphys(b) : (cgp ? (b == 2 ? 2 : b ^ (tg & 1)) : bphys(b)); };
  auto oaddr = [&](int k) {
    const u32 dst = ACT_OFF + (u32)(16 * rt + 4 * tg + (n >> 4)) * (NSTEP * 1024) + (n & 15) * 16;
    if (TYPE == 0) { const int id = 12 * j + 6 * cgp + 2 * bp_of(k >> 1) + h; return dst + (k & 1) * 2 * NSTEP * 1024 + (id >> 2) * 1024 + (id & 3) * 256; }
    const int ch = 4 * (3 * cgp + bp_of(k >> 2)) + 2 * (k & 1) + h, g4 = ch / 5;
    return (k >= 8 && cgp) ? OOB : dst + ((k >> 1) & 1) * 2 * NSTEP * 1024 + (54 + 5 * j + ch - 5 * g4) * 1024 + g4 * 256;
  };
  static_for<0, 18>([&](auto SC) {
    constexpr int S = decltype(SC)::value, IT = S / 2;
    Frag& cur = (S & 1) ? fb : fa; Frag& nxt = (S & 1) ? fa : fb;
    wait_lgkm0(); wait_vm<vm_need(NX, S + 1, S)>(); bar(); SSTAMP(16 + S); SB0;
    p1_step<TYPE>(acc, cur, third, [&](int i) {
      if (!ABL_NOREAD) fill_rd(nxt, i, S + 1);
      if (!ABL_NODMA) {
        if (S == 0) { if (i == 0) dma_w(k1, 5); if (i == 1 || i == 2) dma_x(k2, i - 1, XB2); if (i == 3) dma_w(k2, 6); }
        if (S > 14) { if (S == 16 ? (i >= 5 && i < 5 + N16) : i >= 4) pf2(S == 15 ? 8 * (i - 4) : S == 16 ? 96 + 8 * (i - 5) : 8 * i); }
        else if ((S & 1) == 0) { if (i == 4 || i == 5) dma_w(ka, 3 + 3 * IT + i); if (i >= 6) dma_x(ka, i - 4, xslot(IT + 2)); }
        else { if (i == 5) dma_w(kb, 9 + 3 * IT); if (i >= 6) dma_x(kb, i - 6, xslot(IT)); if (i == 7) { ka = kb; kb = rot(kb); } }
      }
      if (S < 2) colsum(cur, i);
      if ((S == 2 || S == 3) && i == 5 && (rt & 3) == 3) {
#pragma unroll
        for (int e = 0; e < 8; ++e) colsum(cur, e);
      }
      if (S == 4 && i == 2) {
        if (TYPE == 2) {
#pragma unroll
          for (int b = 0; b < 3; ++b) { const float v = xsum32(cs[b]); if (l < 32 && tg < 2) lds_st<float>(L_SP + (tg * 192 + colid(b)) * 4, v); }
        } else {
          const float v = xsum32(cs[0]);
          if (l < 32) __hip_atomic_store(csg + colid(0), ((u64)tag << 32) | __float_as_uint(v), RLX_AGENT);
        }
      }
      if (TYPE == 2 && S == 5 && i == 2 && tid < 192)
        __hip_atomic_store(csg + tid, ((u64)tag << 32) | __float_as_uint(lds_ld<float>(L_SP + tid * 4) + lds_ld<float>(L_SP + 768 + tid * 4)), RLX_AGENT);
      if (S == 12 && i == 1) {
        const int hi = (TYPE == 1 && tid >= 128) ? tid - 128 : tid;
        goff[0] = CSG_OFF + ct * 12288 + (tid & 7) * 1536 + (tid >> 3) * 16;
        goff[1] = w < 4 ? CSG_OFF + ct * 12288 + (tid & 7) * 1536 + 1024 + (hi >> 3) * 16 : STG_OFF + 2048 * rt + (tid & 127) * 16;
      }
      if (S == 13 && i == 0) gload();
      if ((S == 13 || S == 14) && i >= 1 && i < (S == 13 ? 4 : 3)) {
        int p5 = w + (S - 13) * 3 + i - 1; p5 = p5 >= 10 ? p5 - 10 : p5 >= 5 ? p5 - 5 : p5; p5 = p5 >= 5 ? p5 - 5 : p5;
        pvo[(S - 13) * 3 + i - 1] = p2_voff(p5, l);
      }
      if (TYPE == 2 && S == 15 && i == 1 && tid < 480) bstore16(u32x4{SENT, SENT, SENT, SENT}, rs, KVR_OFF + (u32)((2 * j + (tid >= 240)) * 8 + rt) * 3840 + (tid >= 240 ? tid - 240 : tid) * 16);
      if (S == 16) {
        if (i == 1) {
          wait_vm<TYPE == 2 ? 12 : 11>();
          if (!gcheck()) {
#pragma unroll 1
            for (int spin = 0; spin < SPIN_CAP; ++spin) { __builtin_amdgcn_s_sleep(2); gload(); wait_vm<0>(); if (gcheck()) break; }
          }
        }
        if (i == 2 || i == 3) gs[i - 2] = f32x2{oct_sum(gr[i - 2][0]), oct_sum(gr[i - 2][2])};
        if (i == 4) lds_st<f32x2>(L_SP + 2048 + (tid >> 3) * 8, gs[0]);
        if (i == 5) {
          if (w < 4) lds_st<f32x2>(L_SP + 2560 + (tid >> 3) * 8, gs[1]);
          else {
            lds_st<f32x2>(L_SP + (tid & 127) * 8, f32x2{bf_lo(gr[1][0]), bf_lo(gr[1][2])});
            lds_st<f32x2>(L_SP + 1024 + (tid & 127) * 8, f32x2{__builtin_amdgcn_rcpf(bf_hi(gr[1][0])), __builtin_amdgcn_rcpf(bf_hi(gr[1][2]))});
          }
        }
      }
    });
  });
  auto mm = [&](int b, int t, int q) { const Frag& f = q < 2 ? fa : fb; acc[b][t] = p1_mfma<TYPE>(f.w[b][q & 1], f.x[t][q & 1], acc[b][t]); };
  wait_lgkm0(); wait_vm<vm_need(NX, 19, 18)>(); bar(); SSTAMP(34); SB0;
  if constexpr (TYPE != 2) {
    float mean[2], rstd[2], ce[2], sd[2];
    f32x4 cv[2][4];
    u32 ob[8];
    auto ld_cv = [&](int b) {
      const u32 a = L_SP + 2048 + (TYPE == 0 ? (3 * cgp + bp_of(b)) * 16 : cgp * 96 + bp_of(b) * 32) * 4 + 32 * h;
#pragma unroll
      for (int u = 0; u < 4; ++u) cv[b & 1][u] = lds_ld<f32x4>(a + (u >> 1) * (TYPE == 0 ? 384 : 64) + (u & 1) * 16);
    };
    auto epi = [&](int e) {
      const int b = e >> 4, t = (e >> 3) & 1, i = e & 7;
      const f32x4 (&c)[4] = cv[b & 1];
      if (TYPE == 0) {
        const float ex = __builtin_amdgcn_exp2f((acc[b][t][i] - mean[t] * c[i >> 2][i & 3]) * ce[t]);
        const float hv = (acc[b][t][8 + i] - mean[t] * c[2 + (i >> 2)][i & 3]) * __builtin_amdgcn_rcpf(ex * sd[t] + sd[t]);
        if (i & 1) ob[i >> 1] = pk_bf16(__uint_as_float(ob[i >> 1]), hv); else ob[i >> 1] = __float_as_uint(hv);
        asm volatile("" : "+v"(ob[i >> 1]));
        if (i == 7) bstore16(u32x4{ob[0], ob[1], ob[2], ob[3]}, rs, oaddr(2 * b + t));
      } else {
        const float u0 = acc[b][t][2 * i] - mean[t] * c[i >> 1][(2 * i) & 3], u1 = acc[b][t][2 * i + 1] - mean[t] * c[i >> 1][(2 * i + 1) & 3];
        ob[i] = pk_bf16(fmaxf(u0, sd[t]) * rstd[t] + exp2_le1(u0 * ce[t]), fmaxf(u1, sd[t]) * rstd[t] + exp2_le1(u1 * ce[t]));
        asm volatile("" : "+v"(ob[i]));
        if ((i & 3) == 3) bstore16(u32x4{ob[i - 3], ob[i - 2], ob[i - 1], ob[i]}, rs, oaddr(4 * b + 2 * t + (i >> 2)));
      }
    };
#pragma unroll
    for (int i = 0; i < 6; ++i) {
      mm(0, i < 4 ? i >> 1 : 0, i < 4 ? i & 1 : i - 2);
      if (i < 5) fill_rd(fb, i == 1 ? 2 : i == 2 ? 1 : i, 19);
      if (i == 0) {
#pragma unroll
        for (int t = 0; t < 2; ++t) { mean[t] = lds_ld<float>(L_SP + (64 * tg + 32 * t + n) * 4); rstd[t] = lds_ld<float>(L_SP + 1024 + (64 * tg + 32 * t + n) * 4); }
      }
      if (i == 2) ld_cv(0);
      if (i == 3) {
#pragma unroll
        for (int t = 0; t < 2; ++t) { ce[t] = (TYPE == 0 ? -LOG2E : LOG2E) * rstd[t]; sd[t] = (TYPE == 0 ? 1.f : 1e-6f) * __builtin_amdgcn_rcpf(rstd[t]); }
      }
      if (i >= 3) pf2(96 + 8 * i);
      if (i == 5) { ld_cv(1); if (tid < 2) lds_st<u32x4>(L_CNT + tid * 16, u32x4{0u, 0u, 0u, 0u}); }
      SB0;
    }
    wait_lgkm0(); bar(); SB0;
    if (cgp) __builtin_amdgcn_s_setprio(1);
#pragma unroll
    for (int i = 0; i < 10; ++i) {
      mm(i < 2 ? 0 : 1, i < 2 ? 1 : (i - 2) >> 2, i < 2 ? 2 + i : (i - 2) & 3);
      epi(2 * i); epi(2 * i + 1);
      if (i < 4) pf2(64 + 8 * i);
      if (i == 7) ld_cv(2);
      SB0;
    }
    __builtin_amdgcn_s_setprio(0);
    if (TYPE == 0 || third) {
#pragma unroll
      for (int i = 0; i < 8; ++i) { mm(2, i >> 2, i & 3); epi(20 + 2 * i); epi(21 + 2 * i); SB0; }
      STAMP(3);
#pragma unroll
      for (int e = 36; e < 48; ++e) epi(e);
    } else {
      STAMP(3);
#pragma unroll
      for (int e = 20; e < 32; ++e) epi(e);
    }
    STAMP(4);
    publish<8, 2048>(ws, FH_OFF + rt * 256 + (TYPE == 0 ? j : 18 + j) * 4, tag);
    STAMP(5);
  } else {
    float cv[3];
    u32x4 xp[3][2][2];
    f32x4 sm[2], sr[2];
    float zs[2] = {0.f, 0.f};
    constexpr int EB[5] = {2, 0, 0, 1, 1}, ET[6] = {0, 0, 1, 0, 1, 0};
    auto ld_st = [&](int p) {
      const u32 a = L_SP + (64 * tg + 32 * (ET[p >> 2] ^ cgp) + 8 * (p & 3) + 4 * h) * 4;
      sm[p & 1] = lds_ld<f32x4>(a); sr[p & 1] = lds_ld<f32x4>(a + 1024);
    };
    auto epi2 = [&](int p) {
      const int b = EB[p >> 2], t = ET[p >> 2], r0 = 4 * (p & 3);
      const f32x4 m = sm[p & 1], r = sr[p & 1];
      float v[4];
#pragma unroll
      for (int e = 0; e < 4; ++e) {
        v[e] = (acc[b][t][r0 + e] - m[e] * cv[b]) * r[e];
        if (b == 0) v[e] = phi_f(v[e]);
        if (b == 2) { const float pv = phi_f(v[e]); v[e] = (n & 8) ? v[e] : pv; }
      }
      if (p + 2 < 20) ld_st(p + 2);
      if (b != 1) { zs[b >> 1] += (v[0] + v[1]) + (v[2] + v[3]); asm volatile("" : "+v"(zs[b >> 1])); }
      u32 o0 = pk_bf16(v[0], v[1]), o1 = pk_bf16(v[2], v[3]);
      asm volatile("" : "+v"(o0), "+v"(o1));
      xp[b][t][(p >> 1) & 1][2 * (p & 1)] = o0; xp[b][t][(p >> 1) & 1][2 * (p & 1) + 1] = o1;
    };
#pragma unroll
    for (int i = 0; i < 6; ++i) {
      mm(i < 2 || i >= 4 ? 2 : 0, 0, i < 4 ? i & 1 : i - 2);
      if (i < 5) fill_rd(fb, i == 0 ? 4 : i == 1 ? 0 : i == 2 ? 2 : i == 3 ? 1 : 3, 19);
      if (i == 3) {
#pragma unroll
        for (int b = 0; b < 3; ++b) cv[b] = lds_ld<float>(L_SP + 2048 + colid(b) * 4);
      }
      if (i >= 4) ld_st(i - 4);
      SB0;
    }
#pragma unroll
    for (int i = 0; i < 6; ++i) { mm(0, i >= 2, i < 2 ? 2 + i : i - 2); epi2(i); SB0; }
    if (n >= 16) {
#pragma unroll
      for (int k = 0; k < 2; ++k) lds_st<u32x4>(L_KX + ((w * 2 + k) * 32 + (n - 16) + 16 * h) * 16, xp[2][0][k]);
    }
    publish<1, 0, 7>(ws, FK_OFF + j * 256 + rt * 4, tag); SB0;
#pragma unroll
    for (int k = 0; k < 2; ++k) xp[2][1][k] = lds_ld<u32x4>(L_KX + (((w ^ 4) * 2 + k) * 32 + (n & 15) + 16 * h) * 16);
    f32x16 z16;
#pragma unroll
    for (int i = 0; i < 16; ++i) z16[i] = 0.f;
    f32x16 pmk = z16, pmm = z16, pvk = z16, pvm = z16;
    constexpr u32 IMG = L_KX + 8192, DUMP = IMG + 8 * 3840;
    const int lo8 = 8 * cgp, mix = 32 - 32 * cgp;
    u32 avk[4], amk, avm[2], amm;
    auto img_at = [&](int kc, int vc, bool on) { return (on && (kc >= 20) == (vc >= 20)) ? IMG + w * 3840 + kc * 96 + (vc >= 20 ? vc - 20 : vc) * 4 : DUMP + l * 16; };
#pragma unroll
    for (int i = 0; i < 8; ++i) {
      mm(1, i >> 2, i & 3); epi2(6 + i);
      if (i < 4) pf2(64 + 8 * i);
      if (i >= 2 && i < 6) pmm = mfma32(xp[2][(i - 2) >> 1][i & 1], xp[2][(i - 2) >> 1][i & 1], pmm);
      if (i == 6) { amm = img_at(mix + n, mix + 4 * h, n < 8); amk = img_at(lo8 + n, mix + 4 * h, true); asm volatile("" : "+v"(amm), "+v"(amk)); }
      if (i == 7) {
#pragma unroll
        for (int q = 0; q < 2; ++q) { avm[q] = img_at(mix + n, lo8 + 16 * (1 - cgp) + 8 * q + 4 * h, n < 8); asm volatile("" : "+v"(avm[q])); }
      }
      SB0;
    }
    STAMP(3);
#pragma unroll
    for (int i = 0; i < 6; ++i) {
      if (i < 4) pmk = mfma32(xp[2][i >> 1][i & 1], xp[0][i >> 1][i & 1], pmk);
      if (i >= 2 && i < 4) { pvk = mfma32(xp[1][0][i & 1], xp[0][0][i & 1], pvk); pvm = mfma32(xp[1][0][i & 1], xp[2][0][i & 1], pvm); }
      epi2(14 + i);
      if (i < 4) { avk[i] = img_at(lo8 + n, lo8 + 8 * i + 4 * h, true); asm volatile("" : "+v"(avk[i])); }
      if (i == 1) lds_st<f32x4>(amm, f32x4{pmm[4], pmm[5], pmm[6], pmm[7]});
      if (i == 4) {
        const float z0 = xsum32(zs[0]), z1 = xsum32(zs[1]);
        lds_st<float>(h == 0 ? IMG + w * 3840 + (lo8 + n) * 96 + 80 : (n & 8) ? DUMP + l * 16 : (n < 8 ? IMG + w * 3840 + (mix + n) * 96 + 80 : IMG + (w ^ 4) * 3840 + (32 - mix + n - 16) * 96 + 84), h == 0 ? z0 : z1);
      }
      if (i == 5) lds_st<f32x4>(amk, f32x4{pmk[4], pmk[5], pmk[6], pmk[7]});
      SB0;
    }
#pragma unroll
    for (int k = 0; k < 2; ++k) { pvk = mfma32(xp[1][1][k], xp[0][1][k], pvk); pvm = mfma32(xp[1][1][k], xp[2][1][k], pvm); }
    if (tid < 2) lds_st<u32x4>(L_CNT + tid * 16, u32x4{0u, 0u, 0u, 0u});
#pragma unroll
    for (int q = 0; q < 4; ++q) lds_st<f32x4>(avk[q], f32x4{pvk[4 * q], pvk[4 * q + 1], pvk[4 * q + 2], pvk[4 * q + 3]});
#pragma unroll
    for (int q = 0; q < 2; ++q) lds_st<f32x4>(avm[q], cgp ? f32x4{pvm[4 * q], pvm[4 * q + 1], pvm[4 * q + 2], pvm[4 * q + 3]} : f32x4{pvm[8 + 4 * q], pvm[9 + 4 * q], pvm[10 + 4 * q], pvm[11 + 4 * q]});
    __syncthreads();
    if (tid < 480) {
      const int cg = tid >= 240, o = (tid - 240 * cg) * 16;
      f32x4 sum = lds_ld<f32x4>(IMG + cg * 15360 + o);
#pragma unroll
      for (int k = 1; k < 4; ++k) sum += lds_ld<f32x4>(IMG + cg * 15360 + k * 3840 + o);
      bstore16(nosent4(__builtin_bit_cast(u32x4, sum)), rs, KVR_OFF + (u32)((2 * j + cg) * 8 + rt) * 3840 + o);
    }
    STAMP(4);
    wait_vm<1>(); wait_lgkm0(); bar();
    STAMP(5);
  }
}

DI void lds_ticket(u32 a) {
  asm volatile("s_mov_b64 exec, 1\n\tds_add_u32 %0, %1\n\ts_mov_b64 exec, -1" ::"v"(a), "v"(1u) : "memory");
}
DI void lds_wait(u32 a, u32 want) {
#pragma unroll 1
  for (int spin = 0; spin < SPIN_CAP * 16; ++spin) {
    if (__builtin_amdgcn_readfirstlane(__hip_atomic_load((LAS u32*)(size_t)a, __ATOMIC_RELAXED, __HIP_MEMORY_SCOPE_WORKGROUP)) >= want) break;
    __builtin_amdgcn_s_sleep(2);
  }
  asm volatile("" ::: "memory");
}

DI void lds_meet(u32 a, u32 want) { asm volatile("s_waitcnt lgkmcnt(0)" ::: "memory"); lds_ticket(a); lds_wait(a, want); }

DI void mjob(int pair, u32 tag, char* ws, rsrc_t rs, const u32x4 (&WO)[5][2]) {
  int tid = threadIdx.x;
  asm volatile("" : "+v"(tid));
  const int l = tid & 63, w = __builtin_amdgcn_readfirstlane(tid >> 6);
  constexpr u32 KVL = L_MJ;
  __builtin_amdgcn_s_setprio(3);
  STAMP(5);
  if (w < 4) {
    wait_flags<16>(ws, FK_OFF + (pair >> 1) * 256 + (l & 7) * 4, l < 8, tag);
    const int t4 = min(tid, 239);
    u32x4 pa[8];
#pragma unroll 1
    for (int spin = 0; spin < SPIN_CAP; ++spin) {
      u32 mx = 0;
#pragma unroll
      for (int k = 0; k < 8; ++k) pa[k] = bload(rs, KVR_OFF + (u32)pair * 30720 + t4 * 16, k * 3840);
#pragma unroll
      for (int k = 0; k < 8; ++k) mx = max4(mx, pa[k]);
      if (!__any(mx == SENT)) break;
      __builtin_amdgcn_s_sleep(4);
      asm volatile("" ::: "memory");
    }
    f32x4 sum = __builtin_bit_cast(f32x4, pa[0]);
#pragma unroll
    for (int k = 1; k < 8; ++k) sum += __builtin_bit_cast(f32x4, pa[k]);
    const bool zc = t4 % 6 == 5;
    lds_st<u32x2>(KVL + t4 * 8, zc ? u32x2{0u, 0u} : u32x2{pk_bf16(sum[0], sum[1]), pk_bf16(sum[2], sum[3])});
    if (zc) { const float z = sum[0] + (((pair & 1) ? t4 < 48 : t4 >= 192) ? sum[1] : 0.f); lds_st<u16>(KVL + 1920 + (t4 / 6) * 2, (u16)pk_bf16(z, z)); }
  }
  wait_lgkm0(); bar();
  asm volatile("" ::: "memory");
  if (w >= 5) { const int e = tid - 320; bstore16(nosent4(lds_ld<u32x4>(KVL + 1920 + (e % 5) * 16)), rs, e < 160 ? ZB_OFF + (e / 5) * 1280 + pair * 80 + (e % 5) * 16 : OOB); }
  STAMP(6);
  const int g = l >> 4, c = l & 15;
  const u32x4 zero4 = {0u, 0u, 0u, 0u};
  const int kx = 8 * (c >> 2) + (c & 3), ky = kx + 4, kz = 32 + (c & 7), go = min(g, 2) * 16;
  const u32x4 rx = lds_ld<u32x4>(KVL + kx * 48 + go), ry = lds_ld<u32x4>(KVL + ky * 48 + go), rz = lds_ld<u32x4>(KVL + kz * 48 + go);
  const u32x4 ax0 = (kx < 20 && g < 3) ? rx : zero4, ax1 = (kx >= 20 && g < 3) ? rx : zero4, ay0 = (ky < 20 && g < 3) ? ry : zero4, ay1 = (ky >= 20 && g < 3) ? ry : zero4, az = g < 3 ? rz : zero4;
  const u32 mo = MT_OFF + (u32)(100 * w + 5 * (pair >> 2)) * 1024 + (c + 16 * (pair & 3)) * 16;
  f32x4 dx[5], dy[5], dz[5];
#pragma unroll
  for (int cb = 0; cb < 5; ++cb) { const f32x4 z4 = {0.f, 0.f, 0.f, 0.f}; dx[cb] = mfma16(ax0, WO[cb][0], z4); dy[cb] = mfma16(ay0, WO[cb][0], z4); dz[cb] = mfma16(az, WO[cb][1], z4); }
#pragma unroll
  for (int cb = 0; cb < 5; ++cb) { dx[cb] = mfma16(ax1, WO[cb][1], dx[cb]); dy[cb] = mfma16(ay1, WO[cb][1], dy[cb]); }
  SB0;
#pragma unroll
  for (int cb = 0; cb < 5; ++cb) {
    bstore16(nosent4(u32x4{pk_bf16(dx[cb][0], dx[cb][1]), pk_bf16(dx[cb][2], dx[cb][3]), pk_bf16(dy[cb][0], dy[cb][1]), pk_bf16(dy[cb][2], dy[cb][3])}), rs, mo + g * 1024 + cb * 20480);
    bstore8(u32x2{nosent(pk_bf16(dz[cb][0], dz[cb][1])), nosent(pk_bf16(dz[cb][2], dz[cb][3]))}, rs, g < 2 ? mo + 4096 + 8 * g + cb * 20480 : OOB);
  }
  __builtin_amdgcn_s_setprio(0);
  STAMP(7);
}

DI int wt_step(int i) { const int q = i < 7 ? 0 : i < 14 ? 1 : i < 20 ? 2 : 3; return i + 7 * (q + 1); }

DI void spare(int sg, int c2, const u16* x, const u16* wo, const u16* wd, char* ws, u32& tag_out, u32x4 (&WO)[5][2]) {
  int tid = threadIdx.x;
  asm volatile("" : "+v"(tid));
  const int l = tid & 63, w = __builtin_amdgcn_readfirstlane(tid >> 6);
  u32x4 v[4][5];
  const int row0 = 128 * sg + 16 * w + (l >> 4);
#pragma unroll
  for (int a = 0; a < 4; ++a)
#pragma unroll
    for (int k = 0; k < 5; ++k) v[a][k] = *(const u32x4*)((const char*)x + (size_t)(row0 + 4 * a) * 1280 + k * 256 + (l & 15) * 16);
  u32 ep;
  asm volatile("s_load_dword %0, %1, 0x0\n\ts_waitcnt lgkmcnt(0)" : "=s"(ep) : "s"(ws + EP_OFF) : "memory");
  const u32 tag = ep + 1u;
  tag_out = tag;
  const int nst = sg < 10 ? 2 : 1;
#pragma unroll
  for (int a = 0; a < 4; ++a) {
    float s = 0.f, q = 0.f;
#pragma unroll
    for (int k = 0; k < 5; ++k)
#pragma unroll
      for (int e = 0; e < 4; ++e) { s = dot2(v[a][k][e], 0x3f803f80u, s); q = dot2(v[a][k][e], v[a][k][e], q); }
#pragma unroll
    for (int o = 1; o < 16; o <<= 1) { s += __shfl_xor(s, o); q += __shfl_xor(q, o); }
    const float mean = s * (1.f / 640.f);
    const float sd = __builtin_amdgcn_sqrtf(round_bf(round_bf(fmaxf(q * (1.f / 640.f) - mean * mean, 0.f)) + 1e-5f));
    if ((l & 15) == 0) __hip_atomic_store((GAS u64*)(ws + STG_OFF) + row0 + 4 * a, ((u64)tag << 32) | pk_bf16(mean, sd), RLX_AGENT);
  }
  STAMP(1);
#pragma unroll 1
  for (int a = 0; a < nst; ++a)
#pragma unroll
    for (int i = 0; i < 5; ++i) dma16((const char*)wd + (size_t)wt_step(sg + 16 * a) * 40960 + (w * 5 + i) * 1024, l * 16, a * 40960 + (w * 5 + i) * 1024);
#pragma unroll 1
  for (int pc = w; pc < 50; pc += 8) dma16((const char*)(wo + (size_t)sg * 40 * 640) + pc * 1024, l * 16, 81920 + pc * 1024);
  wait_vm<0>();
  __syncthreads();
  STAMP(2);
  const rsrc_t rs = __builtin_amdgcn_make_buffer_rsrc(ws, 0, WS_END, 0x00020000);
#pragma unroll 1
  for (int a = 0; a < nst; ++a)
#pragma unroll
    for (int i = 0; i < 5; ++i) {
      const int cb = w * 5 + i;
      const u32x4 f = tr_frag(a * 40960 + (8 * (l >> 4) + ((l & 15) >> 2)) * 1280 + cb * 32 + 8 * (l & 3), 5120);
      bstore16(f, rs, WT_OFF + (u32)(cb * 54 + wt_step(sg + 16 * a)) * 1024 + l * 16);
    }
  {
    const u32 ra = 81920 + min(8 * (l >> 4) + ((l & 15) >> 2), 19) * 1280 + w * 160 + 8 * (l & 3), rb = 81920 + min(8 * (l >> 4) + 4 + ((l & 15) >> 2), 19) * 1280 + w * 160 + 8 * (l & 3);
    const u32 mo = MT_OFF + (u32)(100 * w + 5 * (sg >> 2)) * 1024 + ((l & 15) + 16 * (sg & 3)) * 16;
#pragma unroll
    for (int cb = 0; cb < 5; ++cb) {
#pragma unroll
      for (int hh = 0; hh < 2; ++hh) { const u32x2 lo = tr_read(ra + cb * 32 + hh * 25600), hi = tr_read(rb + cb * 32 + hh * 25600); WO[cb][hh] = u32x4{lo[0], lo[1], hi[0], hi[1]}; }
      bstore16(u32x4{SENT, SENT, SENT, SENT}, rs, mo + (l >> 4) * 1024 + cb * 20480);
      bstore16(u32x4{SENT, SENT, SENT, SENT}, rs, l < 16 ? mo + 4096 + cb * 20480 : OOB);
    }
    if (w >= 5) { const int e = tid - 320; bstore16(u32x4{SENT, SENT, SENT, SENT}, rs, e < 160 ? ZB_OFF + (e / 5) * 1280 + sg * 80 + (e % 5) * 16 : OOB); }
  }
  STAMP(3);
  publish<8, 256>(ws, FW_OFF + sg * 4, tag);
  STAMP(4);
#pragma unroll 1
  for (int pc = w; pc < 140; pc += 8) p2_piece((const char*)(wd + 80 * c2), pc, l);
  if (tid < 2) lds_st<u32x4>(L_CNT + tid * 16, u32x4{0u, 0u, 0u, 0u});
  if (sg == 0 && w < 4) {
    const u32 off = tid < 240 ? CSG_OFF + tid * 1536 + 4 : STG_OFF + (tid - 240) * 1024 + 4;
    wait_flags(ws, off, true, tag);
  }
  wait_vm<0>();
  __syncthreads();
  if (sg == 0 && tid == 0) __hip_atomic_store((GAS u32*)(ws + EP_OFF), tag, RLX_AGENT);
}

DI u32 p2_attblk(int u, int j) { return j < 14 ? L_ATT + (u * 14 + j) * 1024 : u * 25600 + 20480; }
constexpr int h_step(int t) { return t < 6 ? 5 + t : t < 11 ? t - 6 : t; }
constexpr bool h_lds(int t) { return t < 2 || (t >= 6 && t < 11); }
constexpr int a_first(int t) { return t < 0 ? 0 : t < 2 ? 4 + t : t < 5 ? 2 + 2 * t : 12; }
constexpr int lds_seq(int t, int cb) { return (t < 2 ? t : t - 4) * 5 + cb; }
template <int T> DI void p2_hsteps(f32x4 (&acc)[5][4], u32x4 (&A)[12][4], u32x4 (&B)[4][5], u32x4 (&bl)[3], rsrc_t rs, u32 va, const u32 (&so)[4], u32 vb, u32 wl, u32 wl2, bool extra, Poll& pl, const u16* xp, u32x2 (&xr)[5]) {
  if constexpr (T < 12) {
#pragma unroll
    for (int cb = 0; cb < 5; ++cb) {
      const u32x4 bc = h_lds(T) ? bl[lds_seq(T, cb) % 3] : B[T < 6 ? T - 2 : T - 11][cb];
      const int q = h_lds(T) ? lds_seq(T, cb) + 2 : (T == 5 && cb >= 3) ? 7 + cb : -1;
      if (q >= 0 && q < 35 && !(T == 1 && q >= 10)) bl[q % 3] = tr_frag((q < 10 ? wl2 + (q / 5) * 5120 : wl + (q / 5 - 2) * 5120) + (q % 5) * 32, 1280);
#pragma unroll
      for (int t = 0; t < 4; ++t) acc[cb][t] = mfma16(bc, A[T][t], acc[cb][t]);
      SB0;
    }
    if constexpr (T == 0) poll_done(pl);
    if constexpr (T == 6) {
#pragma unroll
      for (int cb = 0; cb < 5; ++cb) xr[cb] = *(const u32x2*)(xp + 16 * cb);
    }
    if constexpr (T == 1) lds_ticket(L_CNT + 4);
    if constexpr (T == 10) lds_ticket(L_CNT);
#pragma unroll
    for (int n = a_first(T); n < a_first(T + 1); ++n)
#pragma unroll
      for (int t = 0; t < 4; ++t) A[n][t] = bload(rs, n == 11 ? (extra ? va : OOB) : va, so[t] + h_step(n) * 1024);
    if constexpr (T == 5) {
#pragma unroll
      for (int cb = 0; cb < 5; ++cb) B[0][cb] = bload(rs, extra ? vb : OOB, cb * 55296 + 11 * 1024);
    }
    SB0;
    p2_hsteps<T + 1>(acc, A, B, bl, rs, va, so, vb, wl, wl2, extra, pl, xp, xr);
  }
}

template <bool JOB> DI void phase2(u64 T0_, int r, int c, int job, bool late, u32 tag, const u16* x, const u16* wd, u16* out, char* ws, const u32x4 (&WO)[5][2]) {
  int tid = threadIdx.x;
  asm volatile("" : "+v"(tid), "+s"(ws), "+s"(tag));
  const int l = tid & 63, w = __builtin_amdgcn_readfirstlane(tid >> 6);
  const rsrc_t rs = __builtin_amdgcn_make_buffer_rsrc(ws, 0, WS_END, 0x00020000);
  const int g = l >> 4, n = l & 15;
  auto orow = [&]() { int t = threadIdx.x; asm volatile("" : "+v"(t)); return (u32)((64 * r + 16 * (w & 3) + (t & 15)) * 640 + 80 * c + ((t >> 2) & 12)); };
  const u32 fh = FH_OFF + c * 2048 + (r >> 2) * 256;
  STAMP(8);
  const int start = p2_start(w & 3);
  const u32 vb = WT_OFF + (u32)(5 * c * 54 + start) * 1024u + l * 16;
  u32x4 B[4][5];
  if (w < 4) {
    if (late) {
#pragma unroll 1
      for (int pc = max(100 + 10 * w, 116); pc < 110 + 10 * w; ++pc) p2_piece((const char*)(wd + 80 * c), pc, l);
    }
    wait_flags<24>(ws, FW_OFF + c * 256 + n * 4, l < 16, tag);
#pragma unroll
    for (int s = 0; s < 4; ++s) {
#pragma unroll
      for (int cb = 0; cb < 5; ++cb) B[s][cb] = bload(rs, vb, cb * 55296 + (7 + s) * 1024);
      SB0;
    }
  }
  if (JOB) mjob(job, tag, ws, rs, WO);
  if (w >= 4) {
    const int u = w - 4;
    u32x4 P[4][5];
    auto load_p = [&]() {
#pragma unroll
      for (int t = 0; t < 4; ++t) {
#pragma unroll
        for (int s = 0; s < 5; ++s) P[t][s] = bload(rs, ACT_OFF + (u32)(4 * r * NSTEP + 54 + 5 * u) * 1024u + l * 16, ((t + u) & 3) * (NSTEP * 1024) + s * 1024);
        SB0;
      }
    };
    __builtin_amdgcn_s_setprio(2);
    if (u == 0) { wait_flags<8>(ws, l < 16 ? FW_OFF + c * 256 + l * 4 : fh + (18 + (l & 3)) * 4, l < 20, tag); lds_ticket(L_CNT + 20); } else lds_wait(L_CNT + 20, 1);
    load_p();
    const int st2 = start + 11 + (u < 2);
    u32x4 B2[2][5];
#pragma unroll
    for (int s = 0; s < 2; ++s)
#pragma unroll
      for (int cb = 0; cb < 5; ++cb) B2[s][cb] = bload(rs, WT_OFF + (u32)(5 * c * 54 + st2) * 1024u + l * 16, cb * 55296 + s * 1024);
    SB0;
    __builtin_amdgcn_s_setprio(0);
    STAMP(9);
    f32x4 acc[5][4];
    const u32 mhint = MT_OFF + (u32)((5 * c + 4) * 20 + 5 * u + 4) * 1024u + g * 256 + 252;
    float rd[4][2];
    u32 mh, mh2;
    {
      u32x4 A2[2][4], Z[5];
      auto load_z = [&]() {
#pragma unroll
        for (int s = 0; s < 5; ++s) Z[s] = bload(rs, ZB_OFF + r * 1280 + 320 * u + 80 * g, s * 16);
        mh = __builtin_amdgcn_raw_buffer_load_b32(rs, mhint, 0, 16);
      };
      auto bad = [&]() {
        u32 mx = 0;
#pragma unroll
        for (int s = 0; s < 5; ++s) mx = max4(mx, Z[s]);
        return __any(mx == SENT);
      };
      lds_wait(L_CNT + 16, 1);
#pragma unroll
      for (int s = 0; s < 2; ++s)
#pragma unroll
        for (int t = 0; t < 4; ++t) A2[s][t] = bload(rs, ACT_OFF + (u32)(4 * r * NSTEP + st2) * 1024u + l * 16, ((t + u) & 3) * (NSTEP * 1024) + s * 1024);
      SB0;
      load_z();
      SB0;
#pragma unroll
      for (int cb = 0; cb < 5; ++cb) {
#pragma unroll
        for (int t = 0; t < 4; ++t) { acc[cb][t] = mfma16(B2[1][cb], A2[1][t], mfma16(B2[0][cb], A2[0][t], f32x4{0.f, 0.f, 0.f, 0.f})); asm volatile("" : "+v"(acc[cb][t])); }
        if (cb == 0) { SB0; asm volatile("" ::: "memory"); mh2 = __builtin_amdgcn_raw_buffer_load_b32(rs, mhint, 0, 16); SB0; }
      }
      STAMP(14);
      if (bad()) {
#pragma unroll 1
        for (int spin = 0; spin < SPIN_CAP; ++spin) { __builtin_amdgcn_s_sleep(8); asm volatile("" ::: "memory"); load_z(); if (!bad()) break; }
      }
      const bool m0 = n == 4 * g, m1 = n == 4 * g + 1;
#pragma unroll
      for (int s = 0; s < 5; ++s)
#pragma unroll
        for (int q = 0; q < 4; ++q) Z[s][q] = ((8 * s + 2 * q >= 20) ? m1 : m0) ? Z[s][q] : 0u;
#pragma unroll
      for (int t = 0; t < 4; ++t) {
        f32x4 den = {0.f, 0.f, 0.f, 0.f};
#pragma unroll
        for (int s = 0; s < 5; ++s) den = mfma16(Z[s], P[t][s], den);
        rd[t][0] = __builtin_amdgcn_rcpf(den[0] + 1e-6f); rd[t][1] = __builtin_amdgcn_rcpf(den[1] + 1e-6f);
      }
    }
    STAMP(11);
    if (__any(mh == SENT) && __any(mh2 == SENT)) wait_data<4>(rs, mhint);
    STAMP(10);
    u32x4 M[5][5];
    const u32 vm = MT_OFF + (u32)(5 * c * 20 + 5 * u) * 1024u + l * 16;
    auto load_m = [&](int c0, int c1) {
#pragma unroll
      for (int cb = 0; cb < 5; ++cb) {
        if (cb < c0 || cb >= c1) continue;
#pragma unroll
        for (int s = 0; s < 5; ++s) M[cb][s] = bload(rs, vm, (cb * 20 + s) * 1024);
      }
    };
    __builtin_amdgcn_s_setprio(2);
#pragma unroll
    for (int t = 0; t < 4; ++t)
#pragma unroll
      for (int s = 0; s < 5; ++s) {
        if (t < 3) M[t][s] = bload(rs, vm, (t * 20 + s) * 1024);
#pragma unroll
        for (int q = 0; q < 4; ++q) { const float rr = rd[t][8 * s + 2 * q >= 20]; P[t][s][q] = pk_bf16(bf_lo(P[t][s][q]) * rr, bf_hi(P[t][s][q]) * rr); }
        asm volatile("" : "+v"(P[t][s]));
        SB0;
      }
    __builtin_amdgcn_s_setprio(APRIO);
    lds_wait(L_CNT + 4, 4);
    STAMP(17);
#pragma unroll
    for (int cb = 0; cb < 5; ++cb) {
      auto bad = [&]() {
        u32 mx = 0;
#pragma unroll
        for (int s = 0; s < 5; ++s) mx = max4(mx, M[cb][s]);
        return __any(mx == SENT);
      };
      if (__builtin_expect(bad(), 0)) {
#pragma unroll 1
        for (int spin = 0; spin < SPIN_CAP; ++spin) { asm volatile("" ::: "memory"); load_m(cb, cb + 3); if (!bad()) break; }
      }
#pragma unroll
      for (int s = 0; s < 5; ++s)
#pragma unroll
        for (int t = 0; t < 4; ++t) acc[cb][t] = mfma16(M[cb][s], P[t][s], acc[cb][t]);
      if (cb < 2) { SB0; load_m(cb + 3, cb + 4); }
#pragma unroll
      for (int t = 1; t < 4; ++t) if (!(cb == 4 && t == 3)) lds_st<f32x4>(p2_attblk(u, cb * 3 + (t - 1)) + l * 16, acc[cb][t]);
    }
    STAMP(16);
    lds_wait(L_CNT, 4);
#pragma unroll
    for (int k = 0; k < 6; ++k) lds_st<f32x4>(u * 25600 + (15 + k) * 1024 + l * 16, k < 5 ? acc[k][0] : acc[4][3]);
    STAMP(15);
    __syncthreads();
    if constexpr (!JOB) asm volatile(".Lfk_end_a:");
    __builtin_amdgcn_endpgm();
  } else {
    const u32 va = ACT_OFF + (u32)(4 * r * NSTEP + start) * 1024u + l * 16;
    u32 so[4];
#pragma unroll
    for (int t = 0; t < 4; ++t) so[t] = ((t + w) & 3) * (NSTEP * 1024);
    const u32 wi = (16 * (g >> 1) + 4 * (g & 1) + (n >> 2)) * 160 + 8 * (l & 3), wl = w * 25600 + wi, wl2 = L_ATT + w * 10240 + wi;
    u32x4 A[12][4], bl[3];
    bl[0] = tr_frag(wl2, 1280); bl[1] = tr_frag(wl2 + 32, 1280);
    Poll pl = {};
    if (w == 0) { poll_flags(pl, ws, fh + min(l, 17) * 4, tag); lds_ticket(L_CNT + 16); } else lds_wait(L_CNT + 16, 1);
    STAMP(9);
    __builtin_amdgcn_s_setprio(HPRIO);
#pragma unroll
    for (int s = 0; s < 4; ++s) {
#pragma unroll
      for (int t = 0; t < 4; ++t) A[s][t] = bload(rs, va, so[t] + h_step(s) * 1024);
      SB0;
    }
    f32x4 acc[5][4];
#pragma unroll
    for (int cb = 0; cb < 5; ++cb)
#pragma unroll
      for (int t = 0; t < 4; ++t) acc[cb][t] = f32x4{0.f, 0.f, 0.f, 0.f};
    const u32 oo = (u32)((64 * r + 16 * w + n) * 640 + 80 * c + 4 * g);
    u32x2 xr[5];
    p2_hsteps<0>(acc, A, B, bl, rs, va, so, vb, wl, wl2, w < 2, pl, x + oo, xr);
    STAMP(10);
#pragma unroll
    for (int cb = 0; cb < 5; ++cb)
#pragma unroll
      for (int t = 1; t < 4; ++t) lds_st<f32x4>(w * 25600 + (cb * 3 + (t - 1)) * 1024 + l * 16, acc[cb][t]);
#pragma unroll
    for (int cb = 0; cb < 5; ++cb) { acc[cb][0] += f32x4{bf_lo(xr[cb][0]), bf_hi(xr[cb][0]), bf_lo(xr[cb][1]), bf_hi(xr[cb][1])}; asm volatile("" : "+v"(acc[cb][0])); }
    lds_meet(L_CNT + 8, 4);
#pragma unroll
    for (int cb = 0; cb < 5; ++cb) {
#pragma unroll
      for (int o = 1; o < 4; ++o) acc[cb][0] += lds_ld<f32x4>(((w + o) & 3) * 25600 + (cb * 3 + (3 - o)) * 1024 + l * 16);
      asm volatile("" : "+v"(acc[cb][0]));
    }
    STAMP(11);
    __syncthreads();
    STAMP(12);
#pragma unroll
    for (int cb = 0; cb < 5; ++cb) {
      f32x4 s = acc[cb][0];
      s += lds_ld<f32x4>(w * 25600 + (15 + cb) * 1024 + l * 16);
#pragma unroll
      for (int o = 1; o < 4; ++o) s += lds_ld<f32x4>(p2_attblk((w + o) & 3, cb * 3 + (3 - o)) + l * 16);
      *(u32x2*)(out + oo + 16 * cb) = u32x2{pk_bf16(s[0], s[1]), pk_bf16(s[2], s[3])};
    }
    STAMP(13);
    if constexpr (!JOB) asm volatile(".Lfk_end_h:");
    __builtin_amdgcn_endpgm();
  }
}

__host__ __device__ inline void decode(int bid, int& rt, int& ct) {
  const int xcd = bid & 7, idx = bid >> 3, cgrp = xcd >> 1, ci = idx >> 2;
  rt = 4 * (xcd & 1) + (idx & 3);
  ct = cgrp < 2 ? cgrp * 8 + ci : ci == 7 ? 30 : ci < 4 ? 18 + 4 * (cgrp - 2) + ci : ci < 6 ? 26 + 2 * (cgrp - 2) + ci - 4 : 16 + (cgrp - 2);
  if (ct == 30) rt += 8 * (cgrp - 2);
}

__global__ __launch_bounds__(512, 2) void fused_kernel(const u16* __restrict__ x, const u16* __restrict__ wqkv, const u16* __restrict__ wo,
                                                       const u16* __restrict__ wg, const u16* __restrict__ wu, const u16* __restrict__ wd,
                                                       u16* __restrict__ out, char* ws) {
  const u64 T0_ = __builtin_amdgcn_s_memrealtime();
  __builtin_amdgcn_s_waitcnt(0xC07F);
  const int bid = blockIdx.x, xcd = bid & 7, idx = bid >> 3;
  const int r2 = xcd * 4 + (idx >> 3), c2 = idx & 7;
  {
    const char* pc; u32 n0, n1;
    asm volatile("s_getpc_b64 %0\n.Lfk_beg:\n\ts_mov_b32 %1, .Lfk_end_h-.Lfk_beg\n\ts_mov_b32 %2, .Lfk_end_a-.Lfk_beg" : "=s"(pc), "=s"(n0), "=s"(n1));
    const int w = __builtin_amdgcn_readfirstlane(threadIdx.x >> 6);
    const u32 o = (u32)(idx * 4 + (w & 3)) * 1024u;
    if (w < 4 && o + 1024u <= max(n0, n1)) dma16((const char*)((size_t)pc & ~(size_t)15) + o, (threadIdx.x & 63) * 16, L_SP + (w & 3) * 1024);
  }
  int rt, ct;
  decode(bid, rt, ct);
  u32 tag;
  u32x4 WO[5][2];
  if (ct == 30) {
    spare(rt, c2, x, wo, wd, ws, tag, WO);
    phase2<true>(T0_, r2, c2, rt, false, tag, x, wd, out, ws, WO);
  } else {
    if (ct < 18) phase1<0>(T0_, rt, ct, ct, c2, x, wqkv, wo, wg, wu, wd, ws, tag);
    else if (ct < 26) phase1<2>(T0_, rt, ct, ct - 18, c2, x, wqkv, wo, wg, wu, wd, ws, tag);
    else phase1<1>(T0_, rt, ct, ct - 26, c2, x, wqkv, wo, wg, wu, wd, ws, tag);
    phase2<false>(T0_, r2, c2, -1, ct >= 18 && ct < 26, tag, x, wd, out, ws, WO);
  }
}

extern "C" void kernel_launch(void* const* d_in, const int* in_sizes, int n_in, void* d_out, int out_size, void* d_ws, size_t ws_size, hipStream_t stream) {
  (void)hipFuncSetAttribute((const void*)fused_kernel, hipFuncAttributeMaxDynamicSharedMemorySize, LDS_BYTES);
  fused_kernel<<<256, 512, LDS_BYTES, stream>>>((const u16*)d_in[0], (const u16*)d_in[1], (const u16*)d_in[2], (const u16*)d_in[4], (const u16*)d_in[5],
                                                (const u16*)d_in[6], (u16*)d_out, (char*)d_ws);
}
```

```cpp
#include <hip/hip_runtime.h>
#include <stdint.h>
#include <type_traits>

#define ABL_NOREAD 0
#define ABL_NODMA 0
#define APRIO 0
#define HPRIO 1
#define MAPPING 2
#define ABL_NOROT 0
typedef unsigned short u16;
typedef unsigned int u32;
typedef unsigned long long u64;
typedef __bf16 bf16x8 __attribute__((ext_vector_type(8)));
typedef __bf16 bf16x2 __attribute__((ext_vector_type(2)));
typedef short s16x4 __attribute__((ext_vector_type(4)));
typedef float f32x2 __attribute__((ext_vector_type(2)));
typedef float f32x4 __attribute__((ext_vector_type(4)));
typedef float f32x16 __attribute__((ext_vector_type(16)));
typedef u32 u32x2 __attribute__((ext_vector_type(2)));
typedef u32 u32x4 __attribute__((ext_vector_type(4)));
typedef __amdgpu_buffer_rsrc_t rsrc_t;
#define LAS __attribute__((address_space(3)))
#define GAS __attribute__((address_space(1)))
#define DI __device__ __forceinline__
#define RLX_AGENT __ATOMIC_RELAXED, __HIP_MEMORY_SCOPE_AGENT
#define SB0 __builtin_amdgcn_sched_barrier(0)

constexpr int NSTEP = 74;
constexpr u32 ACT_OFF = 0;
constexpr u32 MT_OFF  = ACT_OFF + 128u * NSTEP * 1024u;
constexpr u32 WT_OFF  = MT_OFF + 40u * 20u * 1024u;
constexpr u32 KVR_OFF = WT_OFF + 40u * 54u * 1024u;
constexpr u32 ZB_OFF  = KVR_OFF + 16u * 8u * 3840u;
constexpr u32 STG_OFF = ZB_OFF + 32u * 1280u;
constexpr u32 CSG_OFF = STG_OFF + 2048u * 8u;
constexpr u32 FH_OFF  = CSG_OFF + 30u * 8u * 192u * 8u;
constexpr u32 FK_OFF  = FH_OFF + 64u * 256u;
constexpr u32 FW_OFF  = FK_OFF + 8u * 256u;
constexpr u32 EP_OFF  = FW_OFF + 8u * 256u;
constexpr u32 WS_END  = EP_OFF + 256u;
constexpr u32 OOB = 0x7ffffff0u;
constexpr int SPIN_CAP = 1 << 15;

constexpr int L_W = 98304, L_SP = 159744, L_ATT = 102400, L_KX = 119808, L_MJ = 143360, L_CNT = 163808, LDS_BYTES = 163840;
constexpr int XB0 = 65536, XB1 = 0, XB2 = 32768;

#define STAMP(i) do {} while (0)
#define SSTAMP(i) do {} while (0)

DI float bf_lo(u32 v) { return __uint_as_float(v << 16); }
DI float bf_hi(u32 v) { return __uint_as_float(v & 0xffff0000u); }
DI u32 pk_bf16(float a, float b) { f32x2 v = {a, b}; return __builtin_bit_cast(u32, __builtin_convertvector(v, bf16x2)); }
DI float round_bf(float a) { return bf_lo(pk_bf16(a, a)); }
DI float dot2(u32 a, u32 b, float c) { return __builtin_amdgcn_fdot2_f32_bf16(__builtin_bit_cast(bf16x2, a), __builtin_bit_cast(bf16x2, b), c, false); }
DI float xsum32(float v) { auto r = __builtin_amdgcn_permlane32_swap(__float_as_uint(v), __float_as_uint(v), false, false); return __uint_as_float(r[0]) + __uint_as_float(r[1]); }
DI float xsum16(float v) { auto r = __builtin_amdgcn_permlane16_swap(__float_as_uint(v), __float_as_uint(v), false, false); return __uint_as_float(r[0]) + __uint_as_float(r[1]); }
DI f32x16 mfma32(u32x4 a, u32x4 b, f32x16 c) { return __builtin_amdgcn_mfma_f32_32x32x16_bf16(__builtin_bit_cast(bf16x8, a), __builtin_bit_cast(bf16x8, b), c, 0, 0, 0); }
DI f32x4 mfma16(u32x4 a, u32x4 b, f32x4 c) { return __builtin_amdgcn_mfma_f32_16x16x32_bf16(__builtin_bit_cast(bf16x8, a), __builtin_bit_cast(bf16x8, b), c, 0, 0, 0); }
DI u32x2 tr_read(u32 a) { return __builtin_bit_cast(u32x2, __builtin_amdgcn_ds_read_tr16_b64_v4i16((LAS s16x4*)(size_t)a)); }
DI u32x4 tr_frag(u32 a, int d) { const u32x2 lo = tr_read(a), hi = tr_read(a + d); return u32x4{lo[0], lo[1], hi[0], hi[1]}; }
template <class T> DI T lds_ld(u32 a) { return *(const LAS T*)(size_t)a; }
template <class T> DI void lds_st(u32 a, T v) { *(LAS T*)(size_t)a = v; }
DI void dma16(const char* sbase, u32 voff, u32 ldst) {
  asm volatile("s_mov_b32 m0, %2\n\ts_nop 0\n\tglobal_load_lds_dwordx4 %0, %1" ::"v"(voff), "s"(sbase), "s"(ldst) : "memory");
}
template <int N> DI void wait_vm() { asm volatile("s_waitcnt vmcnt(%0)" ::"n"(N) : "memory"); }
DI void wait_lgkm0() { __builtin_amdgcn_s_waitcnt(0xC07F); }
DI void bar() { __builtin_amdgcn_s_barrier(); }
DI u32x4 bload(rsrc_t r, u32 voff, u32 soff) { return __builtin_amdgcn_raw_buffer_load_b128(r, voff, soff, 16); }
DI void bstore16(u32x4 v, rsrc_t r, u32 voff) { __builtin_amdgcn_raw_buffer_store_b128(v, r, voff, 0, 16); }
DI void bstore8(u32x2 v, rsrc_t r, u32 voff) { __builtin_amdgcn_raw_buffer_store_b64(v, r, voff, 0, 16); }
constexpr u32 SENT = 0xffffffffu;
DI u32 nosent(u32 v) { return min(v, SENT - 1u); }
DI u32x4 nosent4(u32x4 v) { return u32x4{nosent(v[0]), nosent(v[1]), nosent(v[2]), nosent(v[3])}; }
DI u32 max4(u32 m, u32x4 v) { return max(max(m, max(v[0], v[1])), max(v[2], v[3])); }
constexpr float LOG2E = 1.4426950408889634f;
template <int SLEEP = 4> DI void wait_flags(const char* ws, u32 off, bool on, u32 tag) {
  GAS u32* p = (GAS u32*)(ws + off);
#pragma unroll 1
  for (int spin = 0; spin < SPIN_CAP; ++spin) {
    const u32 v = on ? __hip_atomic_load(p, RLX_AGENT) : tag;
    if (__all(v == tag)) break;
    __builtin_amdgcn_s_sleep(SLEEP);
  }
  __builtin_amdgcn_fence(__ATOMIC_ACQUIRE, "wavefront");
  asm volatile("" ::: "memory");
}
template <int SLEEP> DI void wait_data(rsrc_t rs, u32 off) {
#pragma unroll 1
  for (int spin = 0; spin < SPIN_CAP; ++spin) {
    if (!__any(__builtin_amdgcn_raw_buffer_load_b32(rs, off, 0, 16) == SENT)) break;
    __builtin_amdgcn_s_sleep(SLEEP);
    asm volatile("" ::: "memory");
  }
  asm volatile("" ::: "memory");
}
struct Poll { u32 v[2]; };
DI void poll_flags(Poll& p, const char* ws, u32 off, u32 tag) {
  u32 cnt = SPIN_CAP;
#define POLL_STEP(LD, CK) "s_sleep 4\n\tglobal_load_dword %" #LD ", %3, %4 sc1\n\ts_waitcnt vmcnt(1)\n\tv_cmp_eq_u32 vcc, %5, %" #CK "\n\ts_nop 0\n\ts_cmp_eq_u64 vcc, -1\n\ts_cbranch_scc1 .Lpd%=\n\t"
  asm volatile("global_load_dword %0, %3, %4 sc1\n"
               ".Lpl%=:\n\t" POLL_STEP(1, 0) POLL_STEP(0, 1)
               "s_sub_u32 %2, %2, 1\n\ts_cmp_lg_u32 %2, 0\n\ts_cbranch_scc1 .Lpl%=\n"
               ".Lpd%=:"
               : "=&v"(p.v[0]), "=&v"(p.v[1]), "+s"(cnt) : "v"(off), "s"(ws), "s"(tag) : "vcc", "scc", "memory");
#undef POLL_STEP
}
DI void poll_done(Poll& p) { asm volatile("" ::"v"(p.v[0]), "v"(p.v[1])); }
template <int REP> DI void raise_flag(char* ws, u32 off, u32 tag) {
  asm volatile("s_mov_b64 exec, %3\n\tglobal_store_dword %0, %1, %2 sc1\n\ts_mov_b64 exec, -1" ::"v"(off), "v"(tag), "s"(ws), "s"((1ull << REP) - 1) : "memory");
}
template <int REP = 1, int STRIDE = 0, int KEEP = 0> DI void publish(char* ws, u32 off, u32 tag) {
  wait_vm<KEEP>();
  wait_lgkm0(); bar();
  asm volatile("" ::: "memory");
  if (__builtin_amdgcn_readfirstlane(threadIdx.x) == 0) raise_flag<REP>(ws, off + threadIdx.x * STRIDE, tag);
}

struct Sched { int op[96]; int first[20]; };
constexpr Sched make_sched(int n16) {
  Sched r{}; int n = 0;
  for (int k = 0; k < 4; ++k) r.op[n++] = 100 + k;
  r.op[n++] = 0; r.op[n++] = 1; r.op[n++] = 2;
  for (int k = 0; k < 4; ++k) r.op[n++] = 104 + k;
  r.op[n++] = 3; r.op[n++] = 4;
  for (int s = 0; s < 18; ++s) {
    r.first[s + 1] = n;
    const int it = s / 2;
    if (s == 13) { r.op[n++] = 1000; r.op[n++] = 1000; }
    if (s == 0) { r.op[n++] = 5; r.op[n++] = 108; r.op[n++] = 109; r.op[n++] = 6; }
    if (s <= 14) {
      if ((s & 1) == 0) { r.op[n++] = 7 + 3 * it; r.op[n++] = 8 + 3 * it; r.op[n++] = 100 + 4 * (it + 2) + 2; r.op[n++] = 100 + 4 * (it + 2) + 3; }
      else { r.op[n++] = 9 + 3 * it; r.op[n++] = 100 + 4 * (it + 3); r.op[n++] = 100 + 4 * (it + 3) + 1; }
    } else for (int k = 0; k < (s == 15 ? n16 / 100 : s == 16 ? n16 / 10 % 10 : n16 % 10); ++k) r.op[n++] = 1000;
  }
  r.first[19] = n;
  return r;
}
constexpr int vm_need(int n16, int t, int b) {
  const Sched sch = make_sched(n16);
  const int issued = b < 0 ? 7 : sch.first[b + 1];
  int last = -1;
  for (int k = 0; k < issued; ++k) {
    const int op = sch.op[k];
    if (op < 100 ? (8 * op <= 12 * t + 11) : (op < 1000 && (op - 100) / 4 <= t / 2)) last = k;
  }
  return issued - 1 - last;
}

struct Frag { u32x4 x[2][2]; u32x4 w[3][2]; };

DI int p2_start(int i) { return i * 13 + min(i, 2); }
DI int p2_blkstep(int blk) { const int i = blk < 20 ? blk / 5 : (blk - 20) >> 1; return p2_start(i) + (blk < 20 ? blk - 5 * i : 5 + (blk & 1)); }
DI u32 p2_voff(int p5, int l) { const int n = p5 * 64 + l, r = n / 10, c = n - 10 * r; return (u32)((r & 19) | ((r & 4) << 1) | ((r & 8) >> 1)) * 1280u + c * 16; }
DI void p2_piece(const char* wdc, int piece, int l) {
  const int blk = piece / 5;
  dma16(wdc + (size_t)p2_blkstep(blk) * 40960, p2_voff(piece - 5 * blk, l), piece * 1024);
}

template <int TYPE> DI f32x16 p1_mfma(u32x4 w, u32x4 x, f32x16 c) { return TYPE == 2 ? mfma32(x, w, c) : mfma32(w, x, c); }

template <int TYPE, class F> DI void p1_step(f32x16 (&acc)[3][2], const Frag& cur, bool third, F&& fill) {
#pragma unroll
  for (int i = 0; i < 8; ++i) {
    const int b = (i >> 1) & 1, k = i >> 2, t = i & 1;
    acc[b][t] = p1_mfma<TYPE>(cur.w[b][k], cur.x[t][k], acc[b][t]);
    fill(i);
    SB0;
  }
  if (TYPE != 1 || third) {
#pragma unroll
    for (int i = 8; i < 12; ++i) {
      const int k = (i >> 1) & 1, t = i & 1;
      if (TYPE == 2 && t == 1) continue;
      acc[2][t] = p1_mfma<TYPE>(cur.w[2][k], cur.x[t][k], acc[2][t]);
      SB0;
    }
  }
}
template <int I, int N, class F> DI void static_for(F&& f) { if constexpr (I < N) { f(std::integral_constant<int, I>{}); static_for<I + 1, N>(f); } }
constexpr int xslot(int i) { return i % 3 == 0 ? XB0 : (i % 3 == 1 ? XB1 : XB2); }

DI float exp2_le1(float t) { return __builtin_amdgcn_fmed3f(__builtin_amdgcn_exp2f(t), 0.f, 1.f); }
DI float phi_f(float t) { return fmaxf(t, 1e-6f) + exp2_le1(t * LOG2E); }

struct P1Src { const char* xg; const char* wb[3]; u32 xvo, wvo[3]; int k0; };
DI void phase1_start(P1Src& p, int rt, int ct, const u16* x, const u16* wqkv, const u16* wg, const u16* wu) {
  const int l = threadIdx.x & 63, w = __builtin_amdgcn_readfirstlane(threadIdx.x >> 6);
  p.k0 = ABL_NOROT ? 0 : (rt * 10) >> 3;
  p.xg = (const char*)x + (size_t)rt * 256 * 1280;
  p.xvo = (u32)(8 * w + (l >> 3)) * 1280 + (((l & 7) ^ ((4 * (w & 1) + (l >> 4)) & 7)) * 16);
#pragma unroll
  for (int xi = 0; xi < 4; ++xi) dma16(p.xg + (xi * 64 * 1280 + p.k0 * 128), p.xvo, XB0 + xi * 8192 + w * 1024);
  const bool gu = ct < 18, kv = ct < 26;
  const u32 stride = gu ? 3456 : 3840, lim0 = (gu || !kv) ? 12 : 10, lim1 = gu ? 12 : kv ? 10 : 8;
  const char* b0 = (gu ? (const char*)wg : (const char*)wqkv) + (gu ? 192 * ct : kv ? 160 * ct - 1600 : 320 * ct - 8320);
  const char* b1 = gu ? (const char*)wu + 192 * ct : b0 + (kv ? 1280 : 192);
#pragma unroll
  for (int i = 0; i < 3; ++i) {
    const int pp = 8 * i + w, st = pp >= 12, pi = pp - 12 * st, part = pi >= 6;
    const int m = (pi - 6 * part) * 64 + l, row = m / 12, cp = m - 12 * row;
    const u32 ch = part ? cp ^ 2 : cp;
    p.wb[i] = part ? b1 : b0;
    p.wvo[i] = (u32)(st * 32 + row) * stride + (ch >= (part ? lim1 : lim0) ? ch - 4 : ch) * 16;
    dma16(p.wb[i] + (size_t)((u32)p.k0 * 64u * stride), p.wvo[i], L_W + (8 * i + w) * 1024);
  }
}

template <int TYPE>
DI void phase1(const P1Src& src, int rt, int ct, int j, int c2, const u16* wd, char* ws, u32& tag_out) {
  const int tid = threadIdx.x, l = tid & 63, w = __builtin_amdgcn_readfirstlane(tid >> 6);
  const int tg = w & 3, cgp = w >> 2, h = l >> 5, n = l & 31;
  auto dma_x = [&](int kk, int xi, u32 slot) { dma16(src.xg + (xi * 64 * 1280 + kk * 128), src.xvo, slot + xi * 8192 + w * 1024); };
  constexpr u32 WPER = 64 * (TYPE == 0 ? 3456 : 3840);
  const u32 wl0 = L_W + w * 1024, wl1 = w >= 4 ? wl0 - 61440 : wl0;
  auto dma_w = [&](int kk, int grp) { const int pos = (8 * grp) % 60; dma16(src.wb[grp % 3] + (size_t)((u32)kk * WPER), src.wvo[grp % 3], (pos == 56 ? wl1 : wl0) + pos * 1024); };
  auto rot = [&](int kk) { return kk + 1 >= 10 ? kk - 9 : kk + 1; };
  const int k1 = rot(src.k0), k2 = rot(k1);
  u32 ep;
  asm volatile("s_load_dword %0, %1, 0x0" : "=s"(ep) : "s"(ws + EP_OFF) : "memory");

  auto bphys = [&](int b) { int v = b + tg; v = v >= 3 ? v - 3 : v; return v >= 3 ? v - 3 : v; };
  u32 xa[2][4], wa[3][2];
  {
    const int ksw = TYPE == 2 ? (tg & 1) : 0;
#pragma unroll
    for (int t = 0; t < 2; ++t) {
      const int row = 64 * tg + 32 * (TYPE == 2 ? t ^ cgp : t) + n;
#pragma unroll
      for (int k = 0; k < 4; ++k) xa[t][k] = row * 128 + 16 * ((2 * (k ^ ksw) + h) ^ ((row >> 1) & 7));
    }
    const int blk = (l >> 4) & 1, q = (l & 15) >> 2, p = l & 3, rowb = L_W + (8 * h + q) * 192;
    if (TYPE == 2) {
      const int a = cgp ? 3 : 0;
      const u32 b0 = rowb + (a + blk) * 32 + 8 * p, b1 = 6144 + rowb + ((a + blk) ^ 1) * 32 + 8 * p;
      const u32 b2 = rowb + (p >> 1) * 6144 + ((4 + (cgp ^ blk)) ^ (p & 2)) * 16 + 8 * (p & 1);
#pragma unroll
      for (int k = 0; k < 2; ++k) { const u32 o = (k ^ ksw) * 3072; wa[0][k] = b0 + o; wa[1][k] = b1 + o; wa[2][k] = b2 + o; }
    } else {
#pragma unroll
      for (int b = 0; b < 3; ++b) {
        const u32 in32 = 16 * (p & 1) + 8 * (p >> 1);
        if (TYPE == 0) wa[b][0] = rowb + blk * 6144 + ((3 * cgp + bphys(b)) ^ blk) * 32 + in32;
        else wa[b][0] = rowb + cgp * 6144 + ((2 * (cgp ? (b == 2 ? 2 : b ^ (tg & 1)) : bphys(b)) + blk) ^ cgp) * 32 + in32;
        wa[b][1] = 0;
      }
    }
  }
  auto rd_w = [&](int b, int k, int off) { return tr_frag((TYPE == 2 ? wa[b][k] : wa[b][0] + k * 3072) + off, 768); };
  auto rd_x = [&](int t, int k4, int off) {
    return TYPE == 2 ? lds_ld<u32x4>(xa[t][k4] + off) : lds_ld<u32x4>(xa[0][k4] + off + t * 4096);
  };
  auto fill_rd = [&](Frag& f, int i, int st) {
    const int wo_ = (st % 5) * 12288, xo = xslot(st / 2), k4 = 2 * (st & 1);
    if (i == 0) { f.w[0][0] = rd_w(0, 0, wo_); f.x[0][0] = rd_x(0, k4, xo); }
    if (i == 1) { f.x[1][0] = rd_x(1, k4, xo); f.w[1][0] = rd_w(1, 0, wo_); }
    if (i == 2) { f.w[0][1] = rd_w(0, 1, wo_); f.x[0][1] = rd_x(0, k4 + 1, xo); }
    if (i == 3) { f.x[1][1] = rd_x(1, k4 + 1, xo); f.w[1][1] = rd_w(1, 1, wo_); }
    if (i == 4) { f.w[2][0] = rd_w(2, 0, wo_); f.w[2][1] = rd_w(2, 1, wo_); }
  };
  f32x16 acc[3][2];
#pragma unroll
  for (int b = 0; b < 3; ++b)
#pragma unroll
    for (int t = 0; t < 2; ++t)
#pragma unroll
      for (int i = 0; i < 16; ++i) acc[b][t][i] = 0.f;
  Frag fa, fb;
  STAMP(1);
  asm volatile("s_waitcnt vmcnt(%1) lgkmcnt(0)" : "+s"(ep) : "n"(vm_need(0, 0, -1) + 1) : "memory");
  bar(); SB0;
  const u32 tag = ep + 1u;
  tag_out = tag;
#pragma unroll
  for (int i = 0; i < 5; ++i) fill_rd(fa, i, 0);
  SB0;
#pragma unroll
  for (int xi = 0; xi < 4; ++xi) dma_x(k1, xi, XB1);
  dma_w(k1, 3); dma_w(k1, 4);
  SB0;
  STAMP(2);
  const bool third = cgp == 0;
  float cs[3] = {0.f, 0.f, 0.f};
  auto colsum = [&](const Frag& f, int i) {
    constexpr u32 ones = 0x3f803f80u;
    if (TYPE == 2) {
      if (i < 6) { const int b = i >> 1; cs[b] = dot2(f.w[b][0][2 * (i & 1)], ones, cs[b]); cs[b] = dot2(f.w[b][0][2 * (i & 1) + 1], ones, cs[b]); asm volatile("" : "+v"(cs[b])); }
    } else { cs[0] = dot2(f.w[0][i >> 2][i & 3], ones, cs[0]); asm volatile("" : "+v"(cs[0])); }
  };
  auto colid = [&](int b) {
    if (TYPE == 2) return b == 2 ? ((n >> 3) & 1) * 96 + 32 + 8 * (cgp ^ (n >> 4)) + (n & 7) : b * 96 + (cgp ? 48 : 0) + n;
    const int p = (n & 15) >> 2, c16 = 8 * (p & 1) + 4 * (p >> 1) + (n & 3);
    if (TYPE == 0) return (n >> 4) * 96 + (3 * cgp + bphys(b)) * 16 + c16;
    return cgp * 96 + (cgp ? (b == 2 ? 2 : b ^ (tg & 1)) : bphys(b)) * 32 + (n & 16) + c16;
  };
  GAS u64* csg = (GAS u64*)(ws + CSG_OFF) + ct * 1536 + rt * 192;
  const rsrc_t rs = __builtin_amdgcn_make_buffer_rsrc(ws, 0, WS_END, 0x00020000);
  int ka = k2, kb = rot(k2);
  const char* wdc = (const char*)(wd + 80 * c2);
  u32x4 gr[2];
  u32 goff[2], pvo[5];
  f32x2 gs[2];
  auto gload = [&]() {
#pragma unroll
    for (int k = 0; k < 2; ++k) asm volatile("global_load_dwordx4 %0, %1, %2 sc1" : "=v"(gr[k]) : "v"(goff[k]), "s"(ws) : "memory");
  };
  auto gcheck = [&]() {
    asm volatile("" : "+v"(gr[0]), "+v"(gr[1]));
    return __all(gr[0][1] == tag && gr[0][3] == tag && gr[1][1] == tag && gr[1][3] == tag);
  };
  auto oct_sum = [&](u32 u) {
    float v = __uint_as_float(u);
    v += __uint_as_float(__builtin_amdgcn_mov_dpp(__float_as_uint(v), 0xB1, 0xf, 0xf, true));
    v += __uint_as_float(__builtin_amdgcn_mov_dpp(__float_as_uint(v), 0x4E, 0xf, 0xf, true));
    return v + __uint_as_float(__builtin_amdgcn_mov_dpp(__float_as_uint(v), 0x141, 0xf, 0xf, true));
  };
  auto pf2 = [&](int base) {
    constexpr int NP = TYPE == 2 ? 116 : 140;
    const bool clip = base + 8 > NP;
    const int piece = base + (clip ? w & 3 : w), blk = piece / 5;
    u32 vo = pvo[base % 5];
    if (clip) { u32 v1 = pvo[(base + 1) % 5]; asm volatile("" : "+v"(v1)); vo = w < 4 ? vo : v1; }
    const int lo = blk / 5, hi = (blk - 20) >> 1;
    const int st = base + 8 <= 100 || clip && base < 100 ? p2_start(lo) + blk - 5 * lo : base >= 100 ? p2_start(hi) + 5 + (blk & 1) : (w < 4 ? 45 : 5);
    dma16(wdc + (u32)st * 40960u, vo, piece * 1024);
  };
  constexpr int N16 = 3, NX = TYPE == 2 ? 534 : 434;
  auto bp_of = [&](int b) { return TYPE == 0 ? bphys(b) : (cgp ? (b == 2 ? 2 : b ^ (tg & 1)) : bphys(b)); };
  auto oaddr = [&](int k) {
    const u32 dst = ACT_OFF + (u32)(16 * rt + 4 * tg + (n >> 4)) * (NSTEP * 1024) + (n & 15) * 16;
    if (TYPE == 0) { const int id = 12 * j + 6 * cgp + 2 * bp_of(k >> 1) + h; return dst + (k & 1) * 2 * NSTEP * 1024 + (id >> 2) * 1024 + (id & 3) * 256; }
    const int ch = 4 * (3 * cgp + bp_of(k >> 2)) + 2 * (k & 1) + h, g4 = ch / 5;
    return (k >= 8 && cgp) ? OOB : dst + ((k >> 1) & 1) * 2 * NSTEP * 1024 + (54 + 5 * j + ch - 5 * g4) * 1024 + g4 * 256;
  };
  static_for<0, 18>([&](auto SC) {
    constexpr int S = decltype(SC)::value, IT = S / 2;
    Frag& cur = (S & 1) ? fb : fa; Frag& nxt = (S & 1) ? fa : fb;
    wait_lgkm0(); wait_vm<vm_need(NX, S + 1, S)>(); bar(); SSTAMP(16 + S); SB0;
    p1_step<TYPE>(acc, cur, third, [&](int i) {
      if (!ABL_NOREAD) fill_rd(nxt, i, S + 1);
      if (!ABL_NODMA) {
        if (S == 0) { if (i == 0) dma_w(k1, 5); if (i == 1 || i == 2) dma_x(k2, i - 1, XB2); if (i == 3) dma_w(k2, 6); }
        if (S > 14) { if (S == 16 ? (i >= 5 && i < 5 + N16) : i >= 4) pf2(S == 15 ? 8 * (i - 4) : S == 16 ? 96 + 8 * (i - 5) : 8 * i); }
        else if ((S & 1) == 0) { if (i == 4 || i == 5) dma_w(ka, 3 + 3 * IT + i); if (i >= 6) dma_x(ka, i - 4, xslot(IT + 2)); }
        else { if (i == 5) dma_w(kb, 9 + 3 * IT); if (i >= 6) dma_x(kb, i - 6, xslot(IT)); if (i == 7) { ka = kb; kb = rot(kb); } }
      }
      if (S < 2) colsum(cur, i);
      if ((S == 2 || S == 3) && i == 5 && (rt & 3) == 3) {
#pragma unroll
        for (int e = 0; e < 8; ++e) colsum(cur, e);
      }
      if (S == 4 && i == 2) {
        if (TYPE == 2) {
#pragma unroll
          for (int b = 0; b < 3; ++b) { const float v = xsum32(cs[b]); if (l < 32 && tg < 2) lds_st<float>(L_SP + (tg * 192 + colid(b)) * 4, v); }
        } else {
          const float v = xsum32(cs[0]);
          if (l < 32) __hip_atomic_store(csg + colid(0), ((u64)tag << 32) | __float_as_uint(v), RLX_AGENT);
        }
      }
      if (TYPE == 2 && S == 5 && i == 2 && tid < 192)
        __hip_atomic_store(csg + tid, ((u64)tag << 32) | __float_as_uint(lds_ld<float>(L_SP + tid * 4) + lds_ld<float>(L_SP + 768 + tid * 4)), RLX_AGENT);
      if (S == 12 && i == 1) {
        const int hi = (TYPE == 1 && tid >= 128) ? tid - 128 : tid;
        goff[0] = CSG_OFF + ct * 12288 + (tid & 7) * 1536 + (tid >> 3) * 16;
        goff[1] = w < 4 ? CSG_OFF + ct * 12288 + (tid & 7) * 1536 + 1024 + (hi >> 3) * 16 : STG_OFF + 2048 * rt + (tid & 127) * 16;
      }
      if (S == 13 && i == 0) gload();
      if ((S == 13 || S == 14) && i >= 1 && i < (S == 13 ? 4 : 3)) {
        int p5 = w + (S - 13) * 3 + i - 1; p5 = p5 >= 10 ? p5 - 10 : p5 >= 5 ? p5 - 5 : p5; p5 = p5 >= 5 ? p5 - 5 : p5;
        pvo[(S - 13) * 3 + i - 1] = p2_voff(p5, l);
      }
      if (TYPE == 2 && S == 15 && i == 1 && tid < 480) bstore16(u32x4{SENT, SENT, SENT, SENT}, rs, KVR_OFF + (u32)((2 * j + (tid >= 240)) * 8 + rt) * 3840 + (tid >= 240 ? tid - 240 : tid) * 16);
      if (S == 16) {
        if (i == 1) {
          wait_vm<TYPE == 2 ? 12 : 11>();
          if (!gcheck()) {
#pragma unroll 1
            for (int spin = 0; spin < SPIN_CAP; ++spin) { __builtin_amdgcn_s_sleep(2); gload(); wait_vm<0>(); if (gcheck()) break; }
          }
        }
        if (i == 2 || i == 3) gs[i - 2] = f32x2{oct_sum(gr[i - 2][0]), oct_sum(gr[i - 2][2])};
        if (i == 4) lds_st<f32x2>(L_SP + 2048 + (tid >> 3) * 8, gs[0]);
        if (i == 5) {
          if (w < 4) lds_st<f32x2>(L_SP + 2560 + (tid >> 3) * 8, gs[1]);
          else {
            lds_st<f32x2>(L_SP + (tid & 127) * 8, f32x2{bf_lo(gr[1][0]), bf_lo(gr[1][2])});
            lds_st<f32x2>(L_SP + 1024 + (tid & 127) * 8, f32x2{__builtin_amdgcn_rcpf(bf_hi(gr[1][0])), __builtin_amdgcn_rcpf(bf_hi(gr[1][2]))});
          }
        }
      }
    });
  });
  auto mm = [&](int b, int t, int q) { const Frag& f = q < 2 ? fa : fb; acc[b][t] = p1_mfma<TYPE>(f.w[b][q & 1], f.x[t][q & 1], acc[b][t]); };
  wait_lgkm0(); wait_vm<vm_need(NX, 19, 18)>(); bar(); SSTAMP(34); SB0;
  if constexpr (TYPE != 2) {
    float mean[2], rstd[2], ce[2], sd[2];
    f32x4 cv[2][4];
    u32 ob[8];
    auto ld_cv = [&](int b) {
      const u32 a = L_SP + 2048 + (TYPE == 0 ? (3 * cgp + bp_of(b)) * 16 : cgp * 96 + bp_of(b) * 32) * 4 + 32 * h;
#pragma unroll
      for (int u = 0; u < 4; ++u) cv[b & 1][u] = lds_ld<f32x4>(a + (u >> 1) * (TYPE == 0 ? 384 : 64) + (u & 1) * 16);
    };
    auto epi = [&](int e) {
      const int b = e >> 4, t = (e >> 3) & 1, i = e & 7;
      const f32x4 (&c)[4] = cv[b & 1];
      if (TYPE == 0) {
        const float ex = __builtin_amdgcn_exp2f((acc[b][t][i] - mean[t] * c[i >> 2][i & 3]) * ce[t]);
        const float hv = (acc[b][t][8 + i] - mean[t] * c[2 + (i >> 2)][i & 3]) * __builtin_amdgcn_rcpf(ex * sd[t] + sd[t]);
        if (i & 1) ob[i >> 1] = pk_bf16(__uint_as_float(ob[i >> 1]), hv); else ob[i >> 1] = __float_as_uint(hv);
        asm volatile("" : "+v"(ob[i >> 1]));
        if (i == 7) bstore16(u32x4{ob[0], ob[1], ob[2], ob[3]}, rs, oaddr(2 * b + t));
      } else {
        const float u0 = acc[b][t][2 * i] - mean[t] * c[i >> 1][(2 * i) & 3], u1 = acc[b][t][2 * i + 1] - mean[t] * c[i >> 1][(2 * i + 1) & 3];
        ob[i] = pk_bf16(fmaxf(u0, sd[t]) * rstd[t] + exp2_le1(u0 * ce[t]), fmaxf(u1, sd[t]) * rstd[t] + exp2_le1(u1 * ce[t]));
        asm volatile("" : "+v"(ob[i]));
        if ((i & 3) == 3) bstore16(u32x4{ob[i - 3], ob[i - 2], ob[i - 1], ob[i]}, rs, oaddr(4 * b + 2 * t + (i >> 2)));
      }
    };
#pragma unroll
    for (int i = 0; i < 6; ++i) {
      mm(0, i < 4 ? i >> 1 : 0, i < 4 ? i & 1 : i - 2);
      if (i < 5) fill_rd(fb, i == 1 ? 2 : i == 2 ? 1 : i, 19);
      if (i == 0) {
#pragma unroll
        for (int t = 0; t < 2; ++t) { mean[t] = lds_ld<float>(L_SP + (64 * tg + 32 * t + n) * 4); rstd[t] = lds_ld<float>(L_SP + 1024 + (64 * tg + 32 * t + n) * 4); }
      }
      if (i == 2) ld_cv(0);
      if (i == 3) {
#pragma unroll
        for (int t = 0; t < 2; ++t) { ce[t] = (TYPE == 0 ? -LOG2E : LOG2E) * rstd[t]; sd[t] = (TYPE == 0 ? 1.f : 1e-6f) * __builtin_amdgcn_rcpf(rstd[t]); }
      }
      if (i >= 3) pf2(96 + 8 * i);
      if (i == 5) { ld_cv(1); if (tid < 2) lds_st<u32x4>(L_CNT + tid * 16, u32x4{0u, 0u, 0u, 0u}); }
      SB0;
    }
    wait_lgkm0(); bar(); SB0;
    if (cgp) __builtin_amdgcn_s_setprio(1);
#pragma unroll
    for (int i = 0; i < 10; ++i) {
      mm(i < 2 ? 0 : 1, i < 2 ? 1 : (i - 2) >> 2, i < 2 ? 2 + i : (i - 2) & 3);
      epi(2 * i); epi(2 * i + 1);
      if (i < 4) pf2(64 + 8 * i);
      if (i == 7) ld_cv(2);
      SB0;
    }
    __builtin_amdgcn_s_setprio(0);
    if (TYPE == 0 || third) {
#pragma unroll
      for (int i = 0; i < 8; ++i) { mm(2, i >> 2, i & 3); epi(20 + 2 * i); epi(21 + 2 * i); SB0; }
      STAMP(3);
#pragma unroll
      for (int e = 36; e < 48; ++e) epi(e);
    } else {
      STAMP(3);
#pragma unroll
      for (int e = 20; e < 32; ++e) epi(e);
    }
    STAMP(4);
    publish<8, 2048>(ws, FH_OFF + rt * 256 + (TYPE == 0 ? j : 18 + j) * 4, tag);
    STAMP(5);
  } else {
    float cv[3];
    u32x4 xp[3][2][2];
    f32x4 sm[2], sr[2];
    float zs[2] = {0.f, 0.f};
    constexpr int EB[5] = {2, 0, 0, 1, 1}, ET[6] = {0, 0, 1, 0, 1, 0};
    auto ld_st = [&](int p) {
      const u32 a = L_SP + (64 * tg + 32 * (ET[p >> 2] ^ cgp) + 8 * (p & 3) + 4 * h) * 4;
      sm[p & 1] = lds_ld<f32x4>(a); sr[p & 1] = lds_ld<f32x4>(a + 1024);
    };
    auto epi2 = [&](int p) {
      const int b = EB[p >> 2], t = ET[p >> 2], r0 = 4 * (p & 3);
      const f32x4 m = sm[p & 1], r = sr[p & 1];
      float v[4];
#pragma unroll
      for (int e = 0; e < 4; ++e) {
        v[e] = (acc[b][t][r0 + e] - m[e] * cv[b]) * r[e];
        if (b == 0) v[e] = phi_f(v[e]);
        if (b == 2) { const float pv = phi_f(v[e]); v[e] = (n & 8) ? v[e] : pv; }
      }
      if (p + 2 < 20) ld_st(p + 2);
      if (b != 1) { zs[b >> 1] += (v[0] + v[1]) + (v[2] + v[3]); asm volatile("" : "+v"(zs[b >> 1])); }
      u32 o0 = pk_bf16(v[0], v[1]), o1 = pk_bf16(v[2], v[3]);
      asm volatile("" : "+v"(o0), "+v"(o1));
      xp[b][t][(p >> 1) & 1][2 * (p & 1)] = o0; xp[b][t][(p >> 1) & 1][2 * (p & 1) + 1] = o1;
    };
#pragma unroll
    for (int i = 0; i < 6; ++i) {
      mm(i < 2 || i >= 4 ? 2 : 0, 0, i < 4 ? i & 1 : i - 2);
      if (i < 5) fill_rd(fb, i == 0 ? 4 : i == 1 ? 0 : i == 2 ? 2 : i == 3 ? 1 : 3, 19);
      if (i == 3) {
#pragma unroll
        for (int b = 0; b < 3; ++b) cv[b] = lds_ld<float>(L_SP + 2048 + colid(b) * 4);
      }
      if (i >= 4) ld_st(i - 4);
      SB0;
    }
#pragma unroll
    for (int i = 0; i < 6; ++i) { mm(0, i >= 2, i < 2 ? 2 + i : i - 2); epi2(i); SB0; }
    if (n >= 16) {
#pragma unroll
      for (int k = 0; k < 2; ++k) lds_st<u32x4>(L_KX + ((w * 2 + k) * 32 + (n - 16) + 16 * h) * 16, xp[2][0][k]);
    }
    publish<1, 0, 7>(ws, FK_OFF + j * 256 + rt * 4, tag); SB0;
#pragma unroll
    for (int k = 0; k < 2; ++k) xp[2][1][k] = lds_ld<u32x4>(L_KX + (((w ^ 4) * 2 + k) * 32 + (n & 15) + 16 * h) * 16);
    f32x16 z16;
#pragma unroll
    for (int i = 0; i < 16; ++i) z16[i] = 0.f;
    f32x16 pmk = z16, pmm = z16, pvk = z16, pvm = z16;
    constexpr u32 IMG = L_KX + 8192, DUMP = IMG + 8 * 3840;
    const int lo8 = 8 * cgp, mix = 32 - 32 * cgp;
    u32 avk[4], amk, avm[2], amm;
    auto img_at = [&](int kc, int vc, bool on) { return (on && (kc >= 20) == (vc >= 20)) ? IMG + w * 3840 + kc * 96 + (vc >= 20 ? vc - 20 : vc) * 4 : DUMP + l * 16; };
#pragma unroll
    for (int i = 0; i < 8; ++i) {
      mm(1, i >> 2, i & 3); epi2(6 + i);
      if (i < 4) pf2(64 + 8 * i);
      if (i >= 2 && i < 6) pmm = mfma32(xp[2][(i - 2) >> 1][i & 1], xp[2][(i - 2) >> 1][i & 1], pmm);
      if (i == 6) { amm = img_at(mix + n, mix + 4 * h, n < 8); amk = img_at(lo8 + n, mix + 4 * h, true); asm volatile("" : "+v"(amm), "+v"(amk)); }
      if (i == 7) {
#pragma unroll
        for (int q = 0; q < 2; ++q) { avm[q] = img_at(mix + n, lo8 + 16 * (1 - cgp) + 8 * q + 4 * h, n < 8); asm volatile("" : "+v"(avm[q])); }
      }
      SB0;
    }
    STAMP(3);
#pragma unroll
    for (int i = 0; i < 6; ++i) {
      if (i < 4) pmk = mfma32(xp[2][i >> 1][i & 1], xp[0][i >> 1][i & 1], pmk);
      if (i >= 2 && i < 4) { pvk = mfma32(xp[1][0][i & 1], xp[0][0][i & 1], pvk); pvm = mfma32(xp[1][0][i & 1], xp[2][0][i & 1], pvm); }
      epi2(14 + i);
      if (i < 4) { avk[i] = img_at(lo8 + n, lo8 + 8 * i + 4 * h, true); asm volatile("" : "+v"(avk[i])); }
      if (i == 1) lds_st<f32x4>(amm, f32x4{pmm[4], pmm[5], pmm[6], pmm[7]});
      if (i == 4) {
        const float z0 = xsum32(zs[0]), z1 = xsum32(zs[1]);
        lds_st<float>(h == 0 ? IMG + w * 3840 + (lo8 + n) * 96 + 80 : (n & 8) ? DUMP + l * 16 : (n < 8 ? IMG + w * 3840 + (mix + n) * 96 + 80 : IMG + (w ^ 4) * 3840 + (32 - mix + n - 16) * 96 + 84), h == 0 ? z0 : z1);
      }
      if (i == 5) lds_st<f32x4>(amk, f32x4{pmk[4], pmk[5], pmk[6], pmk[7]});
      SB0;
    }
#pragma unroll
    for (int k = 0; k < 2; ++k) { pvk = mfma32(xp[1][1][k], xp[0][1][k], pvk); pvm = mfma32(xp[1][1][k], xp[2][1][k], pvm); }
    if (tid < 2) lds_st<u32x4>(L_CNT + tid * 16, u32x4{0u, 0u, 0u, 0u});
#pragma unroll
    for (int q = 0; q < 4; ++q) lds_st<f32x4>(avk[q], f32x4{pvk[4 * q], pvk[4 * q + 1], pvk[4 * q + 2], pvk[4 * q + 3]});
#pragma unroll
    for (int q = 0; q < 2; ++q) lds_st<f32x4>(avm[q], cgp ? f32x4{pvm[4 * q], pvm[4 * q + 1], pvm[4 * q + 2], pvm[4 * q + 3]} : f32x4{pvm[8 + 4 * q], pvm[9 + 4 * q], pvm[10 + 4 * q], pvm[11 + 4 * q]});
    __syncthreads();
    if (tid < 480) {
      const int cg = tid >= 240, o = (tid - 240 * cg) * 16;
      f32x4 sum = lds_ld<f32x4>(IMG + cg * 15360 + o);
#pragma unroll
      for (int k = 1; k < 4; ++k) sum += lds_ld<f32x4>(IMG + cg * 15360 + k * 3840 + o);
      bstore16(nosent4(__builtin_bit_cast(u32x4, sum)), rs, KVR_OFF + (u32)((2 * j + cg) * 8 + rt) * 3840 + o);
    }
    STAMP(4);
    wait_vm<1>(); wait_lgkm0(); bar();
    STAMP(5);
  }
}

DI void lds_ticket(u32 a) {
  asm volatile("s_mov_b64 exec, 1\n\tds_add_u32 %0, %1\n\ts_mov_b64 exec, -1" ::"v"(a), "v"(1u) : "memory");
}
DI void lds_wait(u32 a, u32 want) {
#pragma unroll 1
  for (int spin = 0; spin < SPIN_CAP * 16; ++spin) {
    if (__builtin_amdgcn_readfirstlane(__hip_atomic_load((LAS u32*)(size_t)a, __ATOMIC_RELAXED, __HIP_MEMORY_SCOPE_WORKGROUP)) >= want) break;
    __builtin_amdgcn_s_sleep(2);
  }
  asm volatile("" ::: "memory");
}

DI void lds_meet(u32 a, u32 want) { asm volatile("s_waitcnt lgkmcnt(0)" ::: "memory"); lds_ticket(a); lds_wait(a, want); }

DI void mjob(int pair, u32 tag, char* ws, rsrc_t rs, const u32x4 (&WO)[5][2]) {
  int tid = threadIdx.x;
  asm volatile("" : "+v"(tid));
  const int l = tid & 63, w = __builtin_amdgcn_readfirstlane(tid >> 6);
  constexpr u32 KVL = L_MJ;
  __builtin_amdgcn_s_setprio(3);
  STAMP(5);
  if (w < 4) {
    wait_flags<16>(ws, FK_OFF + (pair >> 1) * 256 + (l & 7) * 4, l < 8, tag);
    const int t4 = min(tid, 239);
    u32x4 pa[8];
#pragma unroll 1
    for (int spin = 0; spin < SPIN_CAP; ++spin) {
      u32 mx = 0;
#pragma unroll
      for (int k = 0; k < 8; ++k) pa[k] = bload(rs, KVR_OFF + (u32)pair * 30720 + t4 * 16, k * 3840);
#pragma unroll
      for (int k = 0; k < 8; ++k) mx = max4(mx, pa[k]);
      if (!__any(mx == SENT)) break;
      __builtin_amdgcn_s_sleep(4);
      asm volatile("" ::: "memory");
    }
    f32x4 sum = __builtin_bit_cast(f32x4, pa[0]);
#pragma unroll
    for (int k = 1; k < 8; ++k) sum += __builtin_bit_cast(f32x4, pa[k]);
    const bool zc = t4 % 6 == 5;
    lds_st<u32x2>(KVL + t4 * 8, zc ? u32x2{0u, 0u} : u32x2{pk_bf16(sum[0], sum[1]), pk_bf16(sum[2], sum[3])});
    if (zc) { const float z = sum[0] + (((pair & 1) ? t4 < 48 : t4 >= 192) ? sum[1] : 0.f); lds_st<u16>(KVL + 1920 + (t4 / 6) * 2, (u16)pk_bf16(z, z)); }
  }
  wait_lgkm0(); bar();
  asm volatile("" ::: "memory");
  if (w >= 5) { const int e = tid - 320; bstore16(nosent4(lds_ld<u32x4>(KVL + 1920 + (e % 5) * 16)), rs, e < 160 ? ZB_OFF + (e / 5) * 1280 + pair * 80 + (e % 5) * 16 : OOB); }
  STAMP(6);
  const int g = l >> 4, c = l & 15;
  const u32x4 zero4 = {0u, 0u, 0u, 0u};
  const int kx = 8 * (c >> 2) + (c & 3), ky = kx + 4, kz = 32 + (c & 7), go = min(g, 2) * 16;
  const u32x4 rx = lds_ld<u32x4>(KVL + kx * 48 + go), ry = lds_ld<u32x4>(KVL + ky * 48 + go), rz = lds_ld<u32x4>(KVL + kz * 48 + go);
  const u32x4 ax0 = (kx < 20 && g < 3) ? rx : zero4, ax1 = (kx >= 20 && g < 3) ? rx : zero4, ay0 = (ky < 20 && g < 3) ? ry : zero4, ay1 = (ky >= 20 && g < 3) ? ry : zero4, az = g < 3 ? rz : zero4;
  const u32 mo = MT_OFF + (u32)(100 * w + 5 * (pair >> 2)) * 1024 + (c + 16 * (pair & 3)) * 16;
  f32x4 dx[5], dy[5], dz[5];
#pragma unroll
  for (int cb = 0; cb < 5; ++cb) { const f32x4 z4 = {0.f, 0.f, 0.f, 0.f}; dx[cb] = mfma16(ax0, WO[cb][0], z4); dy[cb] = mfma16(ay0, WO[cb][0], z4); dz[cb] = mfma16(az, WO[cb][1], z4); }
#pragma unroll
  for (int cb = 0; cb < 5; ++cb) { dx[cb] = mfma16(ax1, WO[cb][1], dx[cb]); dy[cb] = mfma16(ay1, WO[cb][1], dy[cb]); }
  SB0;
#pragma unroll
  for (int cb = 0; cb < 5; ++cb) {
    bstore16(nosent4(u32x4{pk_bf16(dx[cb][0], dx[cb][1]), pk_bf16(dx[cb][2], dx[cb][3]), pk_bf16(dy[cb][0], dy[cb][1]), pk_bf16(dy[cb][2], dy[cb][3])}), rs, mo + g * 1024 + cb * 20480);
    bstore8(u32x2{nosent(pk_bf16(dz[cb][0], dz[cb][1])), nosent(pk_bf16(dz[cb][2], dz[cb][3]))}, rs, g < 2 ? mo + 4096 + 8 * g + cb * 20480 : OOB);
  }
  __builtin_amdgcn_s_setprio(0);
  STAMP(7);
}

DI int wt_step(int i) { const int q = i < 7 ? 0 : i < 14 ? 1 : i < 20 ? 2 : 3; return i + 7 * (q + 1); }

DI void spare(int sg, int c2, const u16* x, const u16* wo, const u16* wd, char* ws, u32& tag_out, u32x4 (&WO)[5][2]) {
  int tid = threadIdx.x;
  asm volatile("" : "+v"(tid));
  const int l = tid & 63, w = __builtin_amdgcn_readfirstlane(tid >> 6);
  u32x4 v[4][5];
  const int row0 = 128 * sg + 16 * w + (l >> 4);
#pragma unroll
  for (int a = 0; a < 4; ++a)
#pragma unroll
    for (int k = 0; k < 5; ++k) v[a][k] = *(const u32x4*)((const char*)x + (size_t)(row0 + 4 * a) * 1280 + k * 256 + (l & 15) * 16);
  u32 ep;
  asm volatile("s_load_dword %0, %1, 0x0\n\ts_waitcnt lgkmcnt(0)" : "=s"(ep) : "s"(ws + EP_OFF) : "memory");
  const u32 tag = ep + 1u;
  tag_out = tag;
  const int nst = sg < 10 ? 2 : 1;
#pragma unroll
  for (int a = 0; a < 4; ++a) {
    float s = 0.f, q = 0.f;
#pragma unroll
    for (int k = 0; k < 5; ++k)
#pragma unroll
      for (int e = 0; e < 4; ++e) { s = dot2(v[a][k][e], 0x3f803f80u, s); q = dot2(v[a][k][e], v[a][k][e], q); }
#pragma unroll
    for (int o = 1; o < 16; o <<= 1) { s += __shfl_xor(s, o); q += __shfl_xor(q, o); }
    const float mean = s * (1.f / 640.f);
    const float sd = __builtin_amdgcn_sqrtf(round_bf(round_bf(fmaxf(q * (1.f / 640.f) - mean * mean, 0.f)) + 1e-5f));
    if ((l & 15) == 0) __hip_atomic_store((GAS u64*)(ws + STG_OFF) + row0 + 4 * a, ((u64)tag << 32) | pk_bf16(mean, sd), RLX_AGENT);
  }
  STAMP(1);
#pragma unroll 1
  for (int a = 0; a < nst; ++a)
#pragma unroll
    for (int i = 0; i < 5; ++i) dma16((const char*)wd + (size_t)wt_step(sg + 16 * a) * 40960 + (w * 5 + i) * 1024, l * 16, a * 40960 + (w * 5 + i) * 1024);
#pragma unroll 1
  for (int pc = w; pc < 50; pc += 8) dma16((const char*)(wo + (size_t)sg * 40 * 640) + pc * 1024, l * 16, 81920 + pc * 1024);
  wait_vm<0>();
  __syncthreads();
  STAMP(2);
  const rsrc_t rs = __builtin_amdgcn_make_buffer_rsrc(ws, 0, WS_END, 0x00020000);
#pragma unroll 1
  for (int a = 0; a < nst; ++a)
#pragma unroll
    for (int i = 0; i < 5; ++i) {
      const int cb = w * 5 + i;
      const u32x4 f = tr_frag(a * 40960 + (8 * (l >> 4) + ((l & 15) >> 2)) * 1280 + cb * 32 + 8 * (l & 3), 5120);
      bstore16(f, rs, WT_OFF + (u32)(cb * 54 + wt_step(sg + 16 * a)) * 1024 + l * 16);
    }
  {
    const u32 ra = 81920 + min(8 * (l >> 4) + ((l & 15) >> 2), 19) * 1280 + w * 160 + 8 * (l & 3), rb = 81920 + min(8 * (l >> 4) + 4 + ((l & 15) >> 2), 19) * 1280 + w * 160 + 8 * (l & 3);
    const u32 mo = MT_OFF + (u32)(100 * w + 5 * (sg >> 2)) * 1024 + ((l & 15) + 16 * (sg & 3)) * 16;
#pragma unroll
    for (int cb = 0; cb < 5; ++cb) {
#pragma unroll
      for (int hh = 0; hh < 2; ++hh) { const u32x2 lo = tr_read(ra + cb * 32 + hh * 25600), hi = tr_read(rb + cb * 32 + hh * 25600); WO[cb][hh] = u32x4{lo[0], lo[1], hi[0], hi[1]}; }
      bstore16(u32x4{SENT, SENT, SENT, SENT}, rs, mo + (l >> 4) * 1024 + cb * 20480);
      bstore16(u32x4{SENT, SENT, SENT, SENT}, rs, l < 16 ? mo + 4096 + cb * 20480 : OOB);
    }
    if (w >= 5) { const int e = tid - 320; bstore16(u32x4{SENT, SENT, SENT, SENT}, rs, e < 160 ? ZB_OFF + (e / 5) * 1280 + sg * 80 + (e % 5) * 16 : OOB); }
  }
  STAMP(3);
  publish<8, 256>(ws, FW_OFF + sg * 4, tag);
  STAMP(4);
#pragma unroll 1
  for (int pc = w; pc < 140; pc += 8) p2_piece((const char*)(wd + 80 * c2), pc, l);
  if (tid < 2) lds_st<u32x4>(L_CNT + tid * 16, u32x4{0u, 0u, 0u, 0u});
  if (sg == 0 && w < 4) {
    const u32 off = tid < 240 ? CSG_OFF + tid * 1536 + 4 : STG_OFF + (tid - 240) * 1024 + 4;
    wait_flags(ws, off, true, tag);
  }
  wait_vm<0>();
  __syncthreads();
  if (sg == 0 && tid == 0) __hip_atomic_store((GAS u32*)(ws + EP_OFF), tag, RLX_AGENT);
}

DI u32 p2_attblk(int u, int j) { return j < 14 ? L_ATT + (u * 14 + j) * 1024 : u * 25600 + 20480; }
constexpr int h_step(int t) { return t < 6 ? 5 + t : t < 11 ? t - 6 : t; }
constexpr bool h_lds(int t) { return t < 2 || (t >= 6 && t < 11); }
constexpr int a_first(int t) { return t < 0 ? 0 : t < 2 ? 4 + t : t < 5 ? 2 + 2 * t : 12; }
constexpr int lds_seq(int t, int cb) { return (t < 2 ? t : t - 4) * 5 + cb; }
template <int T> DI void p2_hsteps(f32x4 (&acc)[5][4], u32x4 (&A)[12][4], u32x4 (&B)[4][5], u32x4 (&bl)[3], rsrc_t rs, u32 va, const u32 (&so)[4], u32 vb, u32 wl, u32 wl2, bool extra, Poll& pl, const u16* xp, u32x2 (&xr)[5]) {
  if constexpr (T < 12) {
#pragma unroll
    for (int cb = 0; cb < 5; ++cb) {
      const u32x4 bc = h_lds(T) ? bl[lds_seq(T, cb) % 3] : B[T < 6 ? T - 2 : T - 11][cb];
      const int q = h_lds(T) ? lds_seq(T, cb) + 2 : (T == 5 && cb >= 3) ? 7 + cb : -1;
      if (q >= 0 && q < 35 && !(T == 1 && q >= 10)) bl[q % 3] = tr_frag((q < 10 ? wl2 + (q / 5) * 5120 : wl + (q / 5 - 2) * 5120) + (q % 5) * 32, 1280);
#pragma unroll
      for (int t = 0; t < 4; ++t) acc[cb][t] = mfma16(bc, A[T][t], acc[cb][t]);
      SB0;
    }
    if constexpr (T == 0) poll_done(pl);
    if constexpr (T == 6) {
#pragma unroll
      for (int cb = 0; cb < 5; ++cb) xr[cb] = *(const u32x2*)(xp + 16 * cb);
    }
    if constexpr (T == 1) lds_ticket(L_CNT + 4);
    if constexpr (T == 10) lds_ticket(L_CNT);
#pragma unroll
    for (int n = a_first(T); n < a_first(T + 1); ++n)
#pragma unroll
      for (int t = 0; t < 4; ++t) A[n][t] = bload(rs, n == 11 ? (extra ? va : OOB) : va, so[t] + h_step(n) * 1024);
    if constexpr (T == 5) {
#pragma unroll
      for (int cb = 0; cb < 5; ++cb) B[0][cb] = bload(rs, extra ? vb : OOB, cb * 55296 + 11 * 1024);
    }
    SB0;
    p2_hsteps<T + 1>(acc, A, B, bl, rs, va, so, vb, wl, wl2, extra, pl, xp, xr);
  }
}

template <bool JOB> DI void phase2(int r, int c, int job, bool late, u32 tag, const u16* x, const u16* wd, u16* out, char* ws, const u32x4 (&WO)[5][2]) {
  int tid = threadIdx.x;
  asm volatile("" : "+v"(tid), "+s"(ws), "+s"(tag));
  const int l = tid & 63, w = __builtin_amdgcn_readfirstlane(tid >> 6);
  const rsrc_t rs = __builtin_amdgcn_make_buffer_rsrc(ws, 0, WS_END, 0x00020000);
  const int g = l >> 4, n = l & 15;
  auto orow = [&]() { int t = threadIdx.x; asm volatile("" : "+v"(t)); return (u32)((64 * r + 16 * (w & 3) + (t & 15)) * 640 + 80 * c + ((t >> 2) & 12)); };
  const u32 fh = FH_OFF + c * 2048 + (r >> 2) * 256;
  STAMP(8);
  const int start = p2_start(w & 3);
  const u32 vb = WT_OFF + (u32)(5 * c * 54 + start) * 1024u + l * 16;
  u32x4 B[4][5];
  if (w < 4) {
    if (late) {
#pragma unroll 1
      for (int pc = max(100 + 10 * w, 116); pc < 110 + 10 * w; ++pc) p2_piece((const char*)(wd + 80 * c), pc, l);
    }
    wait_flags<24>(ws, FW_OFF + c * 256 + n * 4, l < 16, tag);
#pragma unroll
    for (int s = 0; s < 4; ++s) {
#pragma unroll
      for (int cb = 0; cb < 5; ++cb) B[s][cb] = bload(rs, vb, cb * 55296 + (7 + s) * 1024);
      SB0;
    }
  }
  if (JOB) mjob(job, tag, ws, rs, WO);
  if (w >= 4) {
    const int u = w - 4;
    u32x4 P[4][5];
    auto load_p = [&]() {
#pragma unroll
      for (int t = 0; t < 4; ++t) {
#pragma unroll
        for (int s = 0; s < 5; ++s) P[t][s] = bload(rs, ACT_OFF + (u32)(4 * r * NSTEP + 54 + 5 * u) * 1024u + l * 16, ((t + u) & 3) * (NSTEP * 1024) + s * 1024);
        SB0;
      }
    };
    __builtin_amdgcn_s_setprio(2);
    if (u == 0) { wait_flags<8>(ws, l < 16 ? FW_OFF + c * 256 + l * 4 : fh + (18 + (l & 3)) * 4, l < 20, tag); lds_ticket(L_CNT + 20); } else lds_wait(L_CNT + 20, 1);
    load_p();
    const int st2 = start + 11 + (u < 2);
    u32x4 B2[2][5];
#pragma unroll
    for (int s = 0; s < 2; ++s)
#pragma unroll
      for (int cb = 0; cb < 5; ++cb) B2[s][cb] = bload(rs, WT_OFF + (u32)(5 * c * 54 + st2) * 1024u + l * 16, cb * 55296 + s * 1024);
    SB0;
    __builtin_amdgcn_s_setprio(0);
    STAMP(9);
    f32x4 acc[5][4];
    const u32 mhint = MT_OFF + (u32)((5 * c + 4) * 20 + 5 * u + 4) * 1024u + g * 256 + 252;
    float rd[4][2];
    u32 mh, mh2;
    {
      u32x4 A2[2][4], Z[5];
      auto load_z = [&]() {
#pragma unroll
        for (int s = 0; s < 5; ++s) Z[s] = bload(rs, ZB_OFF + r * 1280 + 320 * u + 80 * g, s * 16);
        mh = __builtin_amdgcn_raw_buffer_load_b32(rs, mhint, 0, 16);
      };
      auto bad = [&]() {
        u32 mx = 0;
#pragma unroll
        for (int s = 0; s < 5; ++s) mx = max4(mx, Z[s]);
        return __any(mx == SENT);
      };
      lds_wait(L_CNT + 16, 1);
#pragma unroll
      for (int s = 0; s < 2; ++s)
#pragma unroll
        for (int t = 0; t < 4; ++t) A2[s][t] = bload(rs, ACT_OFF + (u32)(4 * r * NSTEP + st2) * 1024u + l * 16, ((t + u) & 3) * (NSTEP * 1024) + s * 1024);
      SB0;
      load_z();
      SB0;
#pragma unroll
      for (int cb = 0; cb < 5; ++cb) {
#pragma unroll
        for (int t = 0; t < 4; ++t) { acc[cb][t] = mfma16(B2[1][cb], A2[1][t], mfma16(B2[0][cb], A2[0][t], f32x4{0.f, 0.f, 0.f, 0.f})); asm volatile("" : "+v"(acc[cb][t])); }
        if (cb == 0) { SB0; asm volatile("" ::: "memory"); mh2 = __builtin_amdgcn_raw_buffer_load_b32(rs, mhint, 0, 16); SB0; }
      }
      STAMP(14);
      if (bad()) {
#pragma unroll 1
        for (int spin = 0; spin < SPIN_CAP; ++spin) { __builtin_amdgcn_s_sleep(8); asm volatile("" ::: "memory"); load_z(); if (!bad()) break; }
      }
      const bool m0 = n == 4 * g, m1 = n == 4 * g + 1;
#pragma unroll
      for (int s = 0; s < 5; ++s)
#pragma unroll
        for (int q = 0; q < 4; ++q) Z[s][q] = ((8 * s + 2 * q >= 20) ? m1 : m0) ? Z[s][q] : 0u;
#pragma unroll
      for (int t = 0; t < 4; ++t) {
        f32x4 den = {0.f, 0.f, 0.f, 0.f};
#pragma unroll
        for (int s = 0; s < 5; ++s) den = mfma16(Z[s], P[t][s], den);
        rd[t][0] = __builtin_amdgcn_rcpf(den[0] + 1e-6f); rd[t][1] = __builtin_amdgcn_rcpf(den[1] + 1e-6f);
      }
    }
    STAMP(11);
    if (__any(mh == SENT) && __any(mh2 == SENT)) wait_data<4>(rs, mhint);
    STAMP(10);
    u32x4 M[5][5];
    const u32 vm = MT_OFF + (u32)(5 * c * 20 + 5 * u) * 1024u + l * 16;
    auto load_m = [&](int c0, int c1) {
#pragma unroll
      for (int cb = 0; cb < 5; ++cb) {
        if (cb < c0 || cb >= c1) continue;
#pragma unroll
        for (int s = 0; s < 5; ++s) M[cb][s] = bload(rs, vm, (cb * 20 + s) * 1024);
      }
    };
    __builtin_amdgcn_s_setprio(2);
#pragma unroll
    for (int t = 0; t < 4; ++t)
#pragma unroll
      for (int s = 0; s < 5; ++s) {
        if (t < 3) M[t][s] = bload(rs, vm, (t * 20 + s) * 1024);
#pragma unroll
        for (int q = 0; q < 4; ++q) { const float rr = rd[t][8 * s + 2 * q >= 20]; P[t][s][q] = pk_bf16(bf_lo(P[t][s][q]) * rr, bf_hi(P[t][s][q]) * rr); }
        asm volatile("" : "+v"(P[t][s]));
        SB0;
      }
    __builtin_amdgcn_s_setprio(APRIO);
    lds_wait(L_CNT + 4, 4);
    STAMP(17);
#pragma unroll
    for (int cb = 0; cb < 5; ++cb) {
      auto bad = [&]() {
        u32 mx = 0;
#pragma unroll
        for (int s = 0; s < 5; ++s) mx = max4(mx, M[cb][s]);
        return __any(mx == SENT);
      };
      if (__builtin_expect(bad(), 0)) {
#pragma unroll 1
        for (int spin = 0; spin < SPIN_CAP; ++spin) { asm volatile("" ::: "memory"); load_m(cb, cb + 3); if (!bad()) break; }
      }
#pragma unroll
      for (int s = 0; s < 5; ++s)
#pragma unroll
        for (int t = 0; t < 4; ++t) acc[cb][t] = mfma16(M[cb][s], P[t][s], acc[cb][t]);
      if (cb < 2) { SB0; load_m(cb + 3, cb + 4); }
#pragma unroll
      for (int t = 1; t < 4; ++t) if (!(cb == 4 && t == 3)) lds_st<f32x4>(p2_attblk(u, cb * 3 + (t - 1)) + l * 16, acc[cb][t]);
    }
    STAMP(16);
    lds_wait(L_CNT, 4);
#pragma unroll
    for (int k = 0; k < 6; ++k) lds_st<f32x4>(u * 25600 + (15 + k) * 1024 + l * 16, k < 5 ? acc[k][0] : acc[4][3]);
    STAMP(15);
    __syncthreads();
    if constexpr (!JOB) asm volatile(".Lfk_end_a:");
    __builtin_amdgcn_endpgm();
  } else {
    const u32 va = ACT_OFF + (u32)(4 * r * NSTEP + start) * 1024u + l * 16;
    u32 so[4];
#pragma unroll
    for (int t = 0; t < 4; ++t) so[t] = ((t + w) & 3) * (NSTEP * 1024);
    const u32 wi = (16 * (g >> 1) + 4 * (g & 1) + (n >> 2)) * 160 + 8 * (l & 3), wl = w * 25600 + wi, wl2 = L_ATT + w * 10240 + wi;
    u32x4 A[12][4], bl[3];
    bl[0] = tr_frag(wl2, 1280); bl[1] = tr_frag(wl2 + 32, 1280);
    Poll pl = {};
    if (w == 0) { poll_flags(pl, ws, fh + min(l, 17) * 4, tag); lds_ticket(L_CNT + 16); } else lds_wait(L_CNT + 16, 1);
    STAMP(9);
    __builtin_amdgcn_s_setprio(HPRIO);
#pragma unroll
    for (int s = 0; s < 4; ++s) {
#pragma unroll
      for (int t = 0; t < 4; ++t) A[s][t] = bload(rs, va, so[t] + h_step(s) * 1024);
      SB0;
    }
    f32x4 acc[5][4];
#pragma unroll
    for (int cb = 0; cb < 5; ++cb)
#pragma unroll
      for (int t = 0; t < 4; ++t) acc[cb][t] = f32x4{0.f, 0.f, 0.f, 0.f};
    const u32 oo = (u32)((64 * r + 16 * w + n) * 640 + 80 * c + 4 * g);
    u32x2 xr[5];
    p2_hsteps<0>(acc, A, B, bl, rs, va, so, vb, wl, wl2, w < 2, pl, x + oo, xr);
    STAMP(10);
#pragma unroll
    for (int cb = 0; cb < 5; ++cb)
#pragma unroll
      for (int t = 1; t < 4; ++t) lds_st<f32x4>(w * 25600 + (cb * 3 + (t - 1)) * 1024 + l * 16, acc[cb][t]);
#pragma unroll
    for (int cb = 0; cb < 5; ++cb) { acc[cb][0] += f32x4{bf_lo(xr[cb][0]), bf_hi(xr[cb][0]), bf_lo(xr[cb][1]), bf_hi(xr[cb][1])}; asm volatile("" : "+v"(acc[cb][0])); }
    lds_meet(L_CNT + 8, 4);
#pragma unroll
    for (int cb = 0; cb < 5; ++cb) {
#pragma unroll
      for (int o = 1; o < 4; ++o) acc[cb][0] += lds_ld<f32x4>(((w + o) & 3) * 25600 + (cb * 3 + (3 - o)) * 1024 + l * 16);
      asm volatile("" : "+v"(acc[cb][0]));
    }
    STAMP(11);
    __syncthreads();
    STAMP(12);
#pragma unroll
    for (int cb = 0; cb < 5; ++cb) {
      f32x4 s = acc[cb][0];
      s += lds_ld<f32x4>(w * 25600 + (15 + cb) * 1024 + l * 16);
#pragma unroll
      for (int o = 1; o < 4; ++o) s += lds_ld<f32x4>(p2_attblk((w + o) & 3, cb * 3 + (3 - o)) + l * 16);
      *(u32x2*)(out + oo + 16 * cb) = u32x2{pk_bf16(s[0], s[1]), pk_bf16(s[2], s[3])};
    }
    STAMP(13);
    if constexpr (!JOB) asm volatile(".Lfk_end_h:");
    __builtin_amdgcn_endpgm();
  }
}

__host__ __device__ inline void decode(int bid, int& rt, int& ct) {
  const int xcd = bid & 7, idx = bid >> 3, cgrp = xcd >> 1, ci = idx >> 2, c2 = cgrp - 2;
  const int a = 18 + 4 * c2 + ci, b = 22 + 2 * c2 + ci, c = ci == 7 ? 30 : 16 + c2;
  const int m = ci < 4 ? a : (ci < 6 ? b : c);
  ct = cgrp < 2 ? cgrp * 8 + ci : m;
  rt = 4 * (xcd & 1) + (idx & 3) + (ct == 30 ? 8 * c2 : 0);
}

__global__ __launch_bounds__(512, 2) void fused_kernel(const u16* __restrict__ x, const u16* __restrict__ wqkv, const u16* __restrict__ wo,
                                                       const u16* __restrict__ wg, const u16* __restrict__ wu, const u16* __restrict__ wd,
                                                       u16* __restrict__ out, char* ws) {
  asm volatile("" ::"s"(x), "s"(wqkv), "s"(wo), "s"(wg), "s"(wu), "s"(wd), "s"(out), "s"(ws));
  STAMP(0);
  const int bid = blockIdx.x, xcd = bid & 7, idx = bid >> 3;
  const int r2 = xcd * 4 + (idx >> 3), c2 = idx & 7;
  int rt, ct;
  decode(bid, rt, ct);
  asm volatile("" : "+s"(rt), "+s"(ct));
  P1Src src;
  if (__builtin_expect(ct != 30, 1)) phase1_start(src, rt, ct, x, wqkv, wg, wu);
  {
    const char* pc; u32 n0, n1;
    asm volatile("s_getpc_b64 %0\n.Lfk_beg:\n\ts_mov_b32 %1, .Lfk_end_h-.Lfk_beg\n\ts_mov_b32 %2, .Lfk_end_a-.Lfk_beg" : "=s"(pc), "=s"(n0), "=s"(n1));
    const int w = __builtin_amdgcn_readfirstlane(threadIdx.x >> 6);
    dma16((const char*)((size_t)pc & ~(size_t)15) + min((u32)(idx * 8 + w) * 512u, max(n0, n1) - 1024u), (threadIdx.x & 31) * 16, L_SP + (w & 3) * 1024);
  }
  u32 tag;
  u32x4 WO[5][2];
  if (ct == 30) {
    spare(rt, c2, x, wo, wd, ws, tag, WO);
    phase2<true>(r2, c2, rt, false, tag, x, wd, out, ws, WO);
  } else {
    if (ct < 18) phase1<0>(src, rt, ct, ct, c2, wd, ws, tag);
    else if (ct < 26) phase1<2>(src, rt, ct, ct - 18, c2, wd, ws, tag);
    else phase1<1>(src, rt, ct, ct - 26, c2, wd, ws, tag);
    phase2<false>(r2, c2, -1, ct >= 18 && ct < 26, tag, x, wd, out, ws, WO);
  }
}

extern "C" void kernel_launch(void* const* d_in, const int* in_sizes, int n_in, void* d_out, int out_size, void* d_ws, size_t ws_size, hipStream_t stream) {
  (void)hipFuncSetAttribute((const void*)fused_kernel, hipFuncAttributeMaxDynamicSharedMemorySize, LDS_BYTES);
  fused_kernel<<<256, 512, LDS_BYTES, stream>>>((const u16*)d_in[0], (const u16*)d_in[1], (const u16*)d_in[2], (const u16*)d_in[4], (const u16*)d_in[5],
                                                (const u16*)d_in[6], (u16*)d_out, (char*)d_ws);
}
```

```cpp
#include <hip/hip_runtime.h>
#include <stdint.h>
#include <type_traits>

#define ABL_NOREAD 0
#define ABL_NODMA 0
#define MRATE 3
#define APRIO 0
#define HPRIO 1
#define MAPPING 2
#define ABL_NOROT 0
typedef unsigned short u16;
typedef unsigned int u32;
typedef unsigned long long u64;
typedef __bf16 bf16x8 __attribute__((ext_vector_type(8)));
typedef __bf16 bf16x2 __attribute__((ext_vector_type(2)));
typedef short s16x4 __attribute__((ext_vector_type(4)));
typedef float f32x2 __attribute__((ext_vector_type(2)));
typedef float f32x4 __attribute__((ext_vector_type(4)));
typedef float f32x16 __attribute__((ext_vector_type(16)));
typedef u32 u32x2 __attribute__((ext_vector_type(2)));
typedef u32 u32x4 __attribute__((ext_vector_type(4)));
typedef __amdgpu_buffer_rsrc_t rsrc_t;
#define LAS __attribute__((address_space(3)))
#define GAS __attribute__((address_space(1)))
#define DI __device__ __forceinline__
#define RLX_AGENT __ATOMIC_RELAXED, __HIP_MEMORY_SCOPE_AGENT
#define SB0 __builtin_amdgcn_sched_barrier(0)

constexpr int NSTEP = 74;
constexpr u32 ACT_OFF = 0;
constexpr u32 MT_OFF  = ACT_OFF + 128u * NSTEP * 1024u;
constexpr u32 WT_OFF  = MT_OFF + 40u * 20u * 1024u;
constexpr u32 KVR_OFF = WT_OFF + 40u * 54u * 1024u;
constexpr u32 ZB_OFF  = KVR_OFF + 16u * 8u * 3840u;
constexpr u32 STG_OFF = ZB_OFF + 32u * 1280u;
constexpr u32 CSG_OFF = STG_OFF + 2048u * 8u;
constexpr u32 FH_OFF  = CSG_OFF + 30u * 8u * 192u * 8u;
constexpr u32 FK_OFF  = FH_OFF + 64u * 256u;
constexpr u32 FW_OFF  = FK_OFF + 8u * 256u;
constexpr u32 EP_OFF  = FW_OFF + 8u * 256u;
constexpr u32 WS_END  = EP_OFF + 256u;
constexpr u32 OOB = 0x7ffffff0u;
constexpr int SPIN_CAP = 1 << 15;

constexpr int L_W = 98304, L_SP = 159744, L_ATT = 102400, L_KX = 119808, L_MJ = 143360, L_CNT = 163808, LDS_BYTES = 163840;
constexpr int XB0 = 65536, XB1 = 0, XB2 = 32768;

#define STAMP(i) do {} while (0)
#define SSTAMP(i) do {} while (0)

DI float bf_lo(u32 v) { return __uint_as_float(v << 16); }
DI float bf_hi(u32 v) { return __uint_as_float(v & 0xffff0000u); }
DI u32 pk_bf16(float a, float b) { f32x2 v = {a, b}; return __builtin_bit_cast(u32, __builtin_convertvector(v, bf16x2)); }
DI float round_bf(float a) { return bf_lo(pk_bf16(a, a)); }
DI float dot2(u32 a, u32 b, float c) { return __builtin_amdgcn_fdot2_f32_bf16(__builtin_bit_cast(bf16x2, a), __builtin_bit_cast(bf16x2, b), c, false); }
DI float xsum32(float v) { auto r = __builtin_amdgcn_permlane32_swap(__float_as_uint(v), __float_as_uint(v), false, false); return __uint_as_float(r[0]) + __uint_as_float(r[1]); }
DI float xsum16(float v) { auto r = __builtin_amdgcn_permlane16_swap(__float_as_uint(v), __float_as_uint(v), false, false); return __uint_as_float(r[0]) + __uint_as_float(r[1]); }
DI f32x16 mfma32(u32x4 a, u32x4 b, f32x16 c) { return __builtin_amdgcn_mfma_f32_32x32x16_bf16(__builtin_bit_cast(bf16x8, a), __builtin_bit_cast(bf16x8, b), c, 0, 0, 0); }
DI f32x4 mfma16(u32x4 a, u32x4 b, f32x4 c) { return __builtin_amdgcn_mfma_f32_16x16x32_bf16(__builtin_bit_cast(bf16x8, a), __builtin_bit_cast(bf16x8, b), c, 0, 0, 0); }
DI u32x2 tr_read(u32 a) { return __builtin_bit_cast(u32x2, __builtin_amdgcn_ds_read_tr16_b64_v4i16((LAS s16x4*)(size_t)a)); }
DI u32x4 tr_frag(u32 a, int d) { const u32x2 lo = tr_read(a), hi = tr_read(a + d); return u32x4{lo[0], lo[1], hi[0], hi[1]}; }
template <class T> DI T lds_ld(u32 a) { return *(const LAS T*)(size_t)a; }
template <class T> DI void lds_st(u32 a, T v) { *(LAS T*)(size_t)a = v; }
DI void dma16(const char* sbase, u32 voff, u32 ldst) {
  asm volatile("s_mov_b32 m0, %2\n\ts_nop 0\n\tglobal_load_lds_dwordx4 %0, %1" ::"v"(voff), "s"(sbase), "s"(ldst) : "memory");
}
template <int N> DI void wait_vm() { asm volatile("s_waitcnt vmcnt(%0)" ::"n"(N) : "memory"); }
DI void wait_lgkm0() { __builtin_amdgcn_s_waitcnt(0xC07F); }
DI void bar() { __builtin_amdgcn_s_barrier(); }
DI u32x4 bload(rsrc_t r, u32 voff, u32 soff) { return __builtin_amdgcn_raw_buffer_load_b128(r, voff, soff, 16); }
DI void bstore16(u32x4 v, rsrc_t r, u32 voff) { __builtin_amdgcn_raw_buffer_store_b128(v, r, voff, 0, 16); }
DI void bstore8(u32x2 v, rsrc_t r, u32 voff) { __builtin_amdgcn_raw_buffer_store_b64(v, r, voff, 0, 16); }
constexpr u32 SENT = 0xffffffffu;
DI u32 nosent(u32 v) { return min(v, SENT - 1u); }
DI u32x4 nosent4(u32x4 v) { return u32x4{nosent(v[0]), nosent(v[1]), nosent(v[2]), nosent(v[3])}; }
DI u32 max4(u32 m, u32x4 v) { return max(max(m, max(v[0], v[1])), max(v[2], v[3])); }
constexpr float LOG2E = 1.4426950408889634f;
template <int SLEEP = 4> DI void wait_flags(const char* ws, u32 off, bool on, u32 tag) {
  GAS u32* p = (GAS u32*)(ws + off);
#pragma unroll 1
  for (int spin = 0; spin < SPIN_CAP; ++spin) {
    const u32 v = on ? __hip_atomic_load(p, RLX_AGENT) : tag;
    if (__all(v == tag)) break;
    __builtin_amdgcn_s_sleep(SLEEP);
  }
  __builtin_amdgcn_fence(__ATOMIC_ACQUIRE, "wavefront");
  asm volatile("" ::: "memory");
}
template <int SLEEP> DI void wait_data(rsrc_t rs, u32 off) {
#pragma unroll 1
  for (int spin = 0; spin < SPIN_CAP; ++spin) {
    if (!__any(__builtin_amdgcn_raw_buffer_load_b32(rs, off, 0, 16) == SENT)) break;
    __builtin_amdgcn_s_sleep(SLEEP);
    asm volatile("" ::: "memory");
  }
  asm volatile("" ::: "memory");
}
struct Poll { u32 v[2]; };
DI void poll_flags(Poll& p, const char* ws, u32 off, u32 tag) {
  u32 cnt = SPIN_CAP;
#define POLL_STEP(LD, CK) "s_sleep 4\n\tglobal_load_dword %" #LD ", %3, %4 sc1\n\ts_waitcnt vmcnt(1)\n\tv_cmp_eq_u32 vcc, %5, %" #CK "\n\ts_nop 0\n\ts_cmp_eq_u64 vcc, -1\n\ts_cbranch_scc1 .Lpd%=\n\t"
  asm volatile("global_load_dword %0, %3, %4 sc1\n"
               ".Lpl%=:\n\t" POLL_STEP(1, 0) POLL_STEP(0, 1)
               "s_sub_u32 %2, %2, 1\n\ts_cmp_lg_u32 %2, 0\n\ts_cbranch_scc1 .Lpl%=\n"
               ".Lpd%=:"
               : "=&v"(p.v[0]), "=&v"(p.v[1]), "+s"(cnt) : "v"(off), "s"(ws), "s"(tag) : "vcc", "scc", "memory");
#undef POLL_STEP
}
DI void poll_done(Poll& p) { asm volatile("" ::"v"(p.v[0]), "v"(p.v[1])); }
template <int REP> DI void raise_flag(char* ws, u32 off, u32 tag) {
  asm volatile("s_mov_b64 exec, %3\n\tglobal_store_dword %0, %1, %2 sc1\n\ts_mov_b64 exec, -1" ::"v"(off), "v"(tag), "s"(ws), "s"((1ull << REP) - 1) : "memory");
}
template <int REP = 1, int STRIDE = 0, int KEEP = 0> DI void publish(char* ws, u32 off, u32 tag) {
  wait_vm<KEEP>();
  wait_lgkm0(); bar();
  asm volatile("" ::: "memory");
  if (__builtin_amdgcn_readfirstlane(threadIdx.x) == 0) raise_flag<REP>(ws, off + threadIdx.x * STRIDE, tag);
}

struct Sched { int op[96]; int first[20]; };
constexpr Sched make_sched(int n16) {
  Sched r{}; int n = 0;
  for (int k = 0; k < 4; ++k) r.op[n++] = 100 + k;
  r.op[n++] = 0; r.op[n++] = 1; r.op[n++] = 2;
  for (int k = 0; k < 4; ++k) r.op[n++] = 104 + k;
  r.op[n++] = 3; r.op[n++] = 4;
  for (int s = 0; s < 18; ++s) {
    r.first[s + 1] = n;
    const int it = s / 2;
    if (s == 13) { r.op[n++] = 1000; r.op[n++] = 1000; }
    if (s == 0) { r.op[n++] = 5; r.op[n++] = 108; r.op[n++] = 109; r.op[n++] = 6; }
    if (s <= 14) {
      if ((s & 1) == 0) { r.op[n++] = 7 + 3 * it; r.op[n++] = 8 + 3 * it; r.op[n++] = 100 + 4 * (it + 2) + 2; r.op[n++] = 100 + 4 * (it + 2) + 3; }
      else { r.op[n++] = 9 + 3 * it; r.op[n++] = 100 + 4 * (it + 3); r.op[n++] = 100 + 4 * (it + 3) + 1; }
    } else for (int k = 0; k < (s == 15 ? n16 / 100 : s == 16 ? n16 / 10 % 10 : n16 % 10); ++k) r.op[n++] = 1000;
  }
  r.first[19] = n;
  return r;
}
constexpr int vm_need(int n16, int t, int b) {
  const Sched sch = make_sched(n16);
  const int issued = b < 0 ? 7 : sch.first[b + 1];
  int last = -1;
  for (int k = 0; k < issued; ++k) {
    const int op = sch.op[k];
    if (op < 100 ? (8 * op <= 12 * t + 11) : (op < 1000 && (op - 100) / 4 <= t / 2)) last = k;
  }
  return issued - 1 - last;
}

struct Frag { u32x4 x[2][2]; u32x4 w[3][2]; };

DI int p2_start(int i) { return i * 13 + min(i, 2); }
DI int p2_blkstep(int blk) { const int i = blk < 20 ? blk / 5 : (blk - 20) >> 1; return p2_start(i) + (blk < 20 ? blk - 5 * i : 5 + (blk & 1)); }
DI u32 p2_voff(int p5, int l) { const int n = p5 * 64 + l, r = n / 10, c = n - 10 * r; return (u32)((r & 19) | ((r & 4) << 1) | ((r & 8) >> 1)) * 1280u + c * 16; }
DI void p2_piece(const char* wdc, int piece, int l) {
  const int blk = piece / 5;
  dma16(wdc + (size_t)p2_blkstep(blk) * 40960, p2_voff(piece - 5 * blk, l), piece * 1024);
}

template <int TYPE> DI f32x16 p1_mfma(u32x4 w, u32x4 x, f32x16 c) { return TYPE == 2 ? mfma32(x, w, c) : mfma32(w, x, c); }

template <int TYPE, class F> DI void p1_step(f32x16 (&acc)[3][2], const Frag& cur, bool third, F&& fill) {
#pragma unroll
  for (int i = 0; i < 8; ++i) {
    const int b = (i >> 1) & 1, k = i >> 2, t = i & 1;
    acc[b][t] = p1_mfma<TYPE>(cur.w[b][k], cur.x[t][k], acc[b][t]);
    fill(i);
    SB0;
  }
  if (TYPE != 1 || third) {
#pragma unroll
    for (int i = 8; i < 12; ++i) {
      const int k = (i >> 1) & 1, t = i & 1;
      if (TYPE == 2 && t == 1) continue;
      acc[2][t] = p1_mfma<TYPE>(cur.w[2][k], cur.x[t][k], acc[2][t]);
      SB0;
    }
  }
}
template <int I, int N, class F> DI void static_for(F&& f) { if constexpr (I < N) { f(std::integral_constant<int, I>{}); static_for<I + 1, N>(f); } }
constexpr int xslot(int i) { return i % 3 == 0 ? XB0 : (i % 3 == 1 ? XB1 : XB2); }

DI float exp2_le1(float t) { return __builtin_amdgcn_fmed3f(__builtin_amdgcn_exp2f(t), 0.f, 1.f); }
DI float phi_f(float t) { return fmaxf(t, 1e-6f) + exp2_le1(t * LOG2E); }

struct P1Src { const char* xg; const char* wb[3]; u32 xvo, wvo[3]; int k0; };
DI void phase1_start(P1Src& p, int rt, int ct, const u16* x, const u16* wqkv, const u16* wg, const u16* wu) {
  const int l = threadIdx.x & 63, w = __builtin_amdgcn_readfirstlane(threadIdx.x >> 6);
  p.k0 = ABL_NOROT ? 0 : (rt * 10) >> 3;
  p.xg = (const char*)x + (size_t)rt * 256 * 1280;
  p.xvo = (u32)(8 * w + (l >> 3)) * 1280 + (((l & 7) ^ ((4 * (w & 1) + (l >> 4)) & 7)) * 16);
#pragma unroll
  for (int xi = 0; xi < 4; ++xi) dma16(p.xg + (xi * 64 * 1280 + p.k0 * 128), p.xvo, XB0 + xi * 8192 + w * 1024);
  const bool gu = ct < 18, kv = ct < 26;
  const u32 stride = gu ? 3456 : 3840, lim0 = (gu || !kv) ? 12 : 10, lim1 = gu ? 12 : kv ? 10 : 8;
  const char* b0 = (gu ? (const char*)wg : (const char*)wqkv) + (gu ? 192 * ct : kv ? 160 * ct - 1600 : 320 * ct - 8320);
  const char* b1 = gu ? (const char*)wu + 192 * ct : b0 + (kv ? 1280 : 192);
#pragma unroll
  for (int i = 0; i < 3; ++i) {
    const int pp = 8 * i + w, st = pp >= 12, pi = pp - 12 * st, part = pi >= 6;
    const int m = (pi - 6 * part) * 64 + l, row = m / 12, cp = m - 12 * row;
    const u32 ch = part ? cp ^ 2 : cp;
    p.wb[i] = part ? b1 : b0;
    p.wvo[i] = (u32)(st * 32 + row) * stride + (ch >= (part ? lim1 : lim0) ? ch - 4 : ch) * 16;
    dma16(p.wb[i] + (size_t)((u32)p.k0 * 64u * stride), p.wvo[i], L_W + (8 * i + w) * 1024);
  }
}

template <int TYPE>
DI void phase1(const P1Src& src, int rt, int ct, int j, int c2, const u16* wd, char* ws, u32& tag_out) {
  const int tid = threadIdx.x, l = tid & 63, w = __builtin_amdgcn_readfirstlane(tid >> 6);
  const int tg = w & 3, cgp = w >> 2, h = l >> 5, n = l & 31;
  auto dma_x = [&](int kk, int xi, u32 slot) { dma16(src.xg + (xi * 64 * 1280 + kk * 128), src.xvo, slot + xi * 8192 + w * 1024); };
  constexpr u32 WPER = 64 * (TYPE == 0 ? 3456 : 3840);
  const u32 wl0 = L_W + w * 1024, wl1 = w >= 4 ? wl0 - 61440 : wl0;
  auto dma_w = [&](int kk, int grp) { const int pos = (8 * grp) % 60; dma16(src.wb[grp % 3] + (size_t)((u32)kk * WPER), src.wvo[grp % 3], (pos == 56 ? wl1 : wl0) + pos * 1024); };
  auto rot = [&](int kk) { return kk + 1 >= 10 ? kk - 9 : kk + 1; };
  const int k1 = rot(src.k0), k2 = rot(k1);
  u32 ep;
  asm volatile("s_load_dword %0, %1, 0x0" : "=s"(ep) : "s"(ws + EP_OFF) : "memory");

  auto bphys = [&](int b) { int v = b + tg; v = v >= 3 ? v - 3 : v; return v >= 3 ? v - 3 : v; };
  u32 xa[2][4], wa[3][2];
  {
    const int ksw = TYPE == 2 ? (tg & 1) : 0;
#pragma unroll
    for (int t = 0; t < 2; ++t) {
      const int row = 64 * tg + 32 * (TYPE == 2 ? t ^ cgp : t) + n;
#pragma unroll
      for (int k = 0; k < 4; ++k) xa[t][k] = row * 128 + 16 * ((2 * (k ^ ksw) + h) ^ ((row >> 1) & 7));
    }
    const int blk = (l >> 4) & 1, q = (l & 15) >> 2, p = l & 3, rowb = L_W + (8 * h + q) * 192;
    if (TYPE == 2) {
      const int a = cgp ? 3 : 0;
      const u32 b0 = rowb + (a + blk) * 32 + 8 * p, b1 = 6144 + rowb + ((a + blk) ^ 1) * 32 + 8 * p;
      const u32 b2 = rowb + (p >> 1) * 6144 + ((4 + (cgp ^ blk)) ^ (p & 2)) * 16 + 8 * (p & 1);
#pragma unroll
      for (int k = 0; k < 2; ++k) { const u32 o = (k ^ ksw) * 3072; wa[0][k] = b0 + o; wa[1][k] = b1 + o; wa[2][k] = b2 + o; }
    } else {
#pragma unroll
      for (int b = 0; b < 3; ++b) {
        const u32 in32 = 16 * (p & 1) + 8 * (p >> 1);
        if (TYPE == 0) wa[b][0] = rowb + blk * 6144 + ((3 * cgp + bphys(b)) ^ blk) * 32 + in32;
        else wa[b][0] = rowb + cgp * 6144 + ((2 * (cgp ? (b == 2 ? 2 : b ^ (tg & 1)) : bphys(b)) + blk) ^ cgp) * 32 + in32;
        wa[b][1] = 0;
      }
    }
  }
  auto rd_w = [&](int b, int k, int off) { return tr_frag((TYPE == 2 ? wa[b][k] : wa[b][0] + k * 3072) + off, 768); };
  auto rd_x = [&](int t, int k4, int off) {
    return TYPE == 2 ? lds_ld<u32x4>(xa[t][k4] + off) : lds_ld<u32x4>(xa[0][k4] + off + t * 4096);
  };
  auto fill_rd = [&](Frag& f, int i, int st) {
    const int wo_ = (st % 5) * 12288, xo = xslot(st / 2), k4 = 2 * (st & 1);
    if (i == 0) { f.w[0][0] = rd_w(0, 0, wo_); f.x[0][0] = rd_x(0, k4, xo); }
    if (i == 1) { f.x[1][0] = rd_x(1, k4, xo); f.w[1][0] = rd_w(1, 0, wo_); }
    if (i == 2) { f.w[0][1] = rd_w(0, 1, wo_); f.x[0][1] = rd_x(0, k4 + 1, xo); }
    if (i == 3) { f.x[1][1] = rd_x(1, k4 + 1, xo); f.w[1][1] = rd_w(1, 1, wo_); }
    if (i == 4) { f.w[2][0] = rd_w(2, 0, wo_); f.w[2][1] = rd_w(2, 1, wo_); }
  };
  f32x16 acc[3][2];
#pragma unroll
  for (int b = 0; b < 3; ++b)
#pragma unroll
    for (int t = 0; t < 2; ++t)
#pragma unroll
      for (int i = 0; i < 16; ++i) acc[b][t][i] = 0.f;
  Frag fa, fb;
  STAMP(1);
  asm volatile("s_waitcnt vmcnt(%1) lgkmcnt(0)" : "+s"(ep) : "n"(vm_need(0, 0, -1) + 1) : "memory");
  bar(); SB0;
  const u32 tag = ep + 1u;
  tag_out = tag;
#pragma unroll
  for (int i = 0; i < 5; ++i) fill_rd(fa, i, 0);
  SB0;
#pragma unroll
  for (int xi = 0; xi < 4; ++xi) dma_x(k1, xi, XB1);
  dma_w(k1, 3); dma_w(k1, 4);
  SB0;
  STAMP(2);
  const bool third = cgp == 0;
  float cs[3] = {0.f, 0.f, 0.f};
  auto colsum = [&](const Frag& f, int i) {
    constexpr u32 ones = 0x3f803f80u;
    if (TYPE == 2) {
      if (i < 6) { const int b = i >> 1; cs[b] = dot2(f.w[b][0][2 * (i & 1)], ones, cs[b]); cs[b] = dot2(f.w[b][0][2 * (i & 1) + 1], ones, cs[b]); asm volatile("" : "+v"(cs[b])); }
    } else { cs[0] = dot2(f.w[0][i >> 2][i & 3], ones, cs[0]); asm volatile("" : "+v"(cs[0])); }
  };
  auto colid = [&](int b) {
    if (TYPE == 2) return b == 2 ? ((n >> 3) & 1) * 96 + 32 + 8 * (cgp ^ (n >> 4)) + (n & 7) : b * 96 + (cgp ? 48 : 0) + n;
    const int p = (n & 15) >> 2, c16 = 8 * (p & 1) + 4 * (p >> 1) + (n & 3);
    if (TYPE == 0) return (n >> 4) * 96 + (3 * cgp + bphys(b)) * 16 + c16;
    return cgp * 96 + (cgp ? (b == 2 ? 2 : b ^ (tg & 1)) : bphys(b)) * 32 + (n & 16) + c16;
  };
  GAS u64* csg = (GAS u64*)(ws + CSG_OFF) + ct * 1536 + rt * 192;
  const rsrc_t rs = __builtin_amdgcn_make_buffer_rsrc(ws, 0, WS_END, 0x00020000);
  int ka = k2, kb = rot(k2);
  const char* wdc = (const char*)(wd + 80 * c2);
  u32x4 gr[2];
  u32 goff[2], pvo[5];
  f32x2 gs[2];
  auto gload = [&]() {
#pragma unroll
    for (int k = 0; k < 2; ++k) asm volatile("global_load_dwordx4 %0, %1, %2 sc1" : "=v"(gr[k]) : "v"(goff[k]), "s"(ws) : "memory");
  };
  auto gcheck = [&]() {
    asm volatile("" : "+v"(gr[0]), "+v"(gr[1]));
    return __all(gr[0][1] == tag && gr[0][3] == tag && gr[1][1] == tag && gr[1][3] == tag);
  };
  auto oct_sum = [&](u32 u) {
    float v = __uint_as_float(u);
    v += __uint_as_float(__builtin_amdgcn_mov_dpp(__float_as_uint(v), 0xB1, 0xf, 0xf, true));
    v += __uint_as_float(__builtin_amdgcn_mov_dpp(__float_as_uint(v), 0x4E, 0xf, 0xf, true));
    return v + __uint_as_float(__builtin_amdgcn_mov_dpp(__float_as_uint(v), 0x141, 0xf, 0xf, true));
  };
  auto pf2 = [&](int base) {
    constexpr int NP = TYPE == 2 ? 116 : 140;
    const bool clip = base + 8 > NP;
    const int piece = base + (clip ? w & 3 : w), blk = piece / 5;
    u32 vo = pvo[base % 5];
    if (clip) { u32 v1 = pvo[(base + 1) % 5]; asm volatile("" : "+v"(v1)); vo = w < 4 ? vo : v1; }
    const int lo = blk / 5, hi = (blk - 20) >> 1;
    const int st = base + 8 <= 100 || clip && base < 100 ? p2_start(lo) + blk - 5 * lo : base >= 100 ? p2_start(hi) + 5 + (blk & 1) : (w < 4 ? 45 : 5);
    dma16(wdc + (u32)st * 40960u, vo, piece * 1024);
  };
  constexpr int N16 = 3, NX = TYPE == 2 ? 534 : 434;
  auto bp_of = [&](int b) { return TYPE == 0 ? bphys(b) : (cgp ? (b == 2 ? 2 : b ^ (tg & 1)) : bphys(b)); };
  auto oaddr = [&](int k) {
    const u32 dst = ACT_OFF + (u32)(16 * rt + 4 * tg + (n >> 4)) * (NSTEP * 1024) + (n & 15) * 16;
    if (TYPE == 0) { const int id = 12 * j + 6 * cgp + 2 * bp_of(k >> 1) + h; return dst + (k & 1) * 2 * NSTEP * 1024 + (id >> 2) * 1024 + (id & 3) * 256; }
    const int ch = 4 * (3 * cgp + bp_of(k >> 2)) + 2 * (k & 1) + h, g4 = ch / 5;
    return (k >= 8 && cgp) ? OOB : dst + ((k >> 1) & 1) * 2 * NSTEP * 1024 + (54 + 5 * j + ch - 5 * g4) * 1024 + g4 * 256;
  };
  static_for<0, 18>([&](auto SC) {
    constexpr int S = decltype(SC)::value, IT = S / 2;
    Frag& cur = (S & 1) ? fb : fa; Frag& nxt = (S & 1) ? fa : fb;
    wait_lgkm0(); wait_vm<vm_need(NX, S + 1, S)>(); bar(); SSTAMP(16 + S); SB0;
    p1_step<TYPE>(acc, cur, third, [&](int i) {
      if (!ABL_NOREAD) fill_rd(nxt, i, S + 1);
      if (!ABL_NODMA) {
        if (S == 0) { if (i == 0) dma_w(k1, 5); if (i == 1 || i == 2) dma_x(k2, i - 1, XB2); if (i == 3) dma_w(k2, 6); }
        if (S > 14) { if (S == 16 ? (i >= 5 && i < 5 + N16) : i >= 4) pf2(S == 15 ? 8 * (i - 4) : S == 16 ? 96 + 8 * (i - 5) : 8 * i); }
        else if ((S & 1) == 0) { if (i == 4 || i == 5) dma_w(ka, 3 + 3 * IT + i); if (i >= 6) dma_x(ka, i - 4, xslot(IT + 2)); }
        else { if (i == 5) dma_w(kb, 9 + 3 * IT); if (i >= 6) dma_x(kb, i - 6, xslot(IT)); if (i == 7) { ka = kb; kb = rot(kb); } }
      }
      if (S < 2) colsum(cur, i);
      if ((S == 2 || S == 3) && i == 5 && (rt & 3) == 3) {
#pragma unroll
        for (int e = 0; e < 8; ++e) colsum(cur, e);
      }
      if (S == 4 && i == 2) {
        if (TYPE == 2) {
#pragma unroll
          for (int b = 0; b < 3; ++b) { const float v = xsum32(cs[b]); if (l < 32 && tg < 2) lds_st<float>(L_SP + (tg * 192 + colid(b)) * 4, v); }
        } else {
          const float v = xsum32(cs[0]);
          if (l < 32) __hip_atomic_store(csg + colid(0), ((u64)tag << 32) | __float_as_uint(v), RLX_AGENT);
        }
      }
      if (TYPE == 2 && S == 5 && i == 2 && tid < 192)
        __hip_atomic_store(csg + tid, ((u64)tag << 32) | __float_as_uint(lds_ld<float>(L_SP + tid * 4) + lds_ld<float>(L_SP + 768 + tid * 4)), RLX_AGENT);
      if (S == 12 && i == 1) {
        const int hi = (TYPE == 1 && tid >= 128) ? tid - 128 : tid;
        goff[0] = CSG_OFF + ct * 12288 + (tid & 7) * 1536 + (tid >> 3) * 16;
        goff[1] = w < 4 ? CSG_OFF + ct * 12288 + (tid & 7) * 1536 + 1024 + (hi >> 3) * 16 : STG_OFF + 2048 * rt + (tid & 127) * 16;
      }
      if (S == 13 && i == 0) gload();
      if ((S == 13 || S == 14) && i >= 1 && i < (S == 13 ? 4 : 3)) {
        int p5 = w + (S - 13) * 3 + i - 1; p5 = p5 >= 10 ? p5 - 10 : p5 >= 5 ? p5 - 5 : p5; p5 = p5 >= 5 ? p5 - 5 : p5;
        pvo[(S - 13) * 3 + i - 1] = p2_voff(p5, l);
      }
      if (TYPE == 2 && S == 15 && i == 1 && tid < 480) bstore16(u32x4{SENT, SENT, SENT, SENT}, rs, KVR_OFF + (u32)((2 * j + (tid >= 240)) * 8 + rt) * 3840 + (tid >= 240 ? tid - 240 : tid) * 16);
      if (S == 16) {
        if (i == 1) {
          wait_vm<TYPE == 2 ? 12 : 11>();
          if (!gcheck()) {
#pragma unroll 1
            for (int spin = 0; spin < SPIN_CAP; ++spin) { __builtin_amdgcn_s_sleep(2); gload(); wait_vm<0>(); if (gcheck()) break; }
          }
        }
        if (i == 2 || i == 3) gs[i - 2] = f32x2{oct_sum(gr[i - 2][0]), oct_sum(gr[i - 2][2])};
        if (i == 4) lds_st<f32x2>(L_SP + 2048 + (tid >> 3) * 8, gs[0]);
        if (i == 5) {
          if (w < 4) lds_st<f32x2>(L_SP + 2560 + (tid >> 3) * 8, gs[1]);
          else {
            lds_st<f32x2>(L_SP + (tid & 127) * 8, f32x2{bf_lo(gr[1][0]), bf_lo(gr[1][2])});
            lds_st<f32x2>(L_SP + 1024 + (tid & 127) * 8, f32x2{__builtin_amdgcn_rcpf(bf_hi(gr[1][0])), __builtin_amdgcn_rcpf(bf_hi(gr[1][2]))});
          }
        }
      }
    });
  });
  auto mm = [&](int b, int t, int q) { const Frag& f = q < 2 ? fa : fb; acc[b][t] = p1_mfma<TYPE>(f.w[b][q & 1], f.x[t][q & 1], acc[b][t]); };
  wait_lgkm0(); wait_vm<vm_need(NX, 19, 18)>(); bar(); SSTAMP(34); SB0;
  if constexpr (TYPE != 2) {
    float mean[2], rstd[2], ce[2], sd[2];
    f32x4 cv[2][4];
    u32 ob[8];
    auto ld_cv = [&](int b) {
      const u32 a = L_SP + 2048 + (TYPE == 0 ? (3 * cgp + bp_of(b)) * 16 : cgp * 96 + bp_of(b) * 32) * 4 + 32 * h;
#pragma unroll
      for (int u = 0; u < 4; ++u) cv[b & 1][u] = lds_ld<f32x4>(a + (u >> 1) * (TYPE == 0 ? 384 : 64) + (u & 1) * 16);
    };
    auto epi = [&](int e) {
      const int b = e >> 4, t = (e >> 3) & 1, i = e & 7;
      const f32x4 (&c)[4] = cv[b & 1];
      if (TYPE == 0) {
        const float ex = __builtin_amdgcn_exp2f((acc[b][t][i] - mean[t] * c[i >> 2][i & 3]) * ce[t]);
        const float hv = (acc[b][t][8 + i] - mean[t] * c[2 + (i >> 2)][i & 3]) * __builtin_amdgcn_rcpf(ex * sd[t] + sd[t]);
        if (i & 1) ob[i >> 1] = pk_bf16(__uint_as_float(ob[i >> 1]), hv); else ob[i >> 1] = __float_as_uint(hv);
        asm volatile("" : "+v"(ob[i >> 1]));
        if (i == 7) bstore16(u32x4{ob[0], ob[1], ob[2], ob[3]}, rs, oaddr(2 * b + t));
      } else {
        const float u0 = acc[b][t][2 * i] - mean[t] * c[i >> 1][(2 * i) & 3], u1 = acc[b][t][2 * i + 1] - mean[t] * c[i >> 1][(2 * i + 1) & 3];
        ob[i] = pk_bf16(fmaxf(u0, sd[t]) * rstd[t] + exp2_le1(u0 * ce[t]), fmaxf(u1, sd[t]) * rstd[t] + exp2_le1(u1 * ce[t]));
        asm volatile("" : "+v"(ob[i]));
        if ((i & 3) == 3) bstore16(u32x4{ob[i - 3], ob[i - 2], ob[i - 1], ob[i]}, rs, oaddr(4 * b + 2 * t + (i >> 2)));
      }
    };
#pragma unroll
    for (int i = 0; i < 6; ++i) {
      mm(0, i < 4 ? i >> 1 : 0, i < 4 ? i & 1 : i - 2);
      if (i < 5) fill_rd(fb, i == 1 ? 2 : i == 2 ? 1 : i, 19);
      if (i == 0) {
#pragma unroll
        for (int t = 0; t < 2; ++t) { mean[t] = lds_ld<float>(L_SP + (64 * tg + 32 * t + n) * 4); rstd[t] = lds_ld<float>(L_SP + 1024 + (64 * tg + 32 * t + n) * 4); }
      }
      if (i == 2) ld_cv(0);
      if (i == 3) {
#pragma unroll
        for (int t = 0; t < 2; ++t) { ce[t] = (TYPE == 0 ? -LOG2E : LOG2E) * rstd[t]; sd[t] = (TYPE == 0 ? 1.f : 1e-6f) * __builtin_amdgcn_rcpf(rstd[t]); }
      }
      if (i >= 3) pf2(96 + 8 * i);
      if (i == 5) { ld_cv(1); if (tid < 2) lds_st<u32x4>(L_CNT + tid * 16, u32x4{0u, 0u, 0u, 0u}); }
      SB0;
    }
    wait_lgkm0(); bar(); SB0;
    if (cgp) __builtin_amdgcn_s_setprio(1);
#pragma unroll
    for (int i = 0; i < 10; ++i) {
      mm(i < 2 ? 0 : 1, i < 2 ? 1 : (i - 2) >> 2, i < 2 ? 2 + i : (i - 2) & 3);
      epi(2 * i); epi(2 * i + 1);
      if (i < 4) pf2(64 + 8 * i);
      if (i == 7) ld_cv(2);
      SB0;
    }
    __builtin_amdgcn_s_setprio(0);
    if (TYPE == 0 || third) {
#pragma unroll
      for (int i = 0; i < 8; ++i) { mm(2, i >> 2, i & 3); epi(20 + 2 * i); epi(21 + 2 * i); SB0; }
      STAMP(3);
#pragma unroll
      for (int e = 36; e < 48; ++e) epi(e);
    } else {
      STAMP(3);
#pragma unroll
      for (int e = 20; e < 32; ++e) epi(e);
    }
    STAMP(4);
    publish<8, 2048>(ws, FH_OFF + rt * 256 + (TYPE == 0 ? j : 18 + j) * 4, tag);
    STAMP(5);
  } else {
    float cv[3];
    u32x4 xp[3][2][2];
    f32x4 sm[2], sr[2];
    float zs[2] = {0.f, 0.f};
    constexpr int EB[5] = {2, 0, 0, 1, 1}, ET[6] = {0, 0, 1, 0, 1, 0};
    auto ld_st = [&](int p) {
      const u32 a = L_SP + (64 * tg + 32 * (ET[p >> 2] ^ cgp) + 8 * (p & 3) + 4 * h) * 4;
      sm[p & 1] = lds_ld<f32x4>(a); sr[p & 1] = lds_ld<f32x4>(a + 1024);
    };
    auto epi2 = [&](int p) {
      const int b = EB[p >> 2], t = ET[p >> 2], r0 = 4 * (p & 3);
      const f32x4 m = sm[p & 1], r = sr[p & 1];
      float v[4];
#pragma unroll
      for (int e = 0; e < 4; ++e) {
        v[e] = (acc[b][t][r0 + e] - m[e] * cv[b]) * r[e];
        if (b == 0) v[e] = phi_f(v[e]);
        if (b == 2) { const float pv = phi_f(v[e]); v[e] = (n & 8) ? v[e] : pv; }
      }
      if (p + 2 < 20) ld_st(p + 2);
      if (b != 1) { zs[b >> 1] += (v[0] + v[1]) + (v[2] + v[3]); asm volatile("" : "+v"(zs[b >> 1])); }
      u32 o0 = pk_bf16(v[0], v[1]), o1 = pk_bf16(v[2], v[3]);
      asm volatile("" : "+v"(o0), "+v"(o1));
      xp[b][t][(p >> 1) & 1][2 * (p & 1)] = o0; xp[b][t][(p >> 1) & 1][2 * (p & 1) + 1] = o1;
    };
#pragma unroll
    for (int i = 0; i < 6; ++i) {
      mm(i < 2 || i >= 4 ? 2 : 0, 0, i < 4 ? i & 1 : i - 2);
      if (i < 5) fill_rd(fb, i == 0 ? 4 : i == 1 ? 0 : i == 2 ? 2 : i == 3 ? 1 : 3, 19);
      if (i == 3) {
#pragma unroll
        for (int b = 0; b < 3; ++b) cv[b] = lds_ld<float>(L_SP + 2048 + colid(b) * 4);
      }
      if (i >= 4) ld_st(i - 4);
      SB0;
    }
#pragma unroll
    for (int i = 0; i < 6; ++i) { mm(0, i >= 2, i < 2 ? 2 + i : i - 2); epi2(i); SB0; }
    if (n >= 16) {
#pragma unroll
      for (int k = 0; k < 2; ++k) lds_st<u32x4>(L_KX + ((w * 2 + k) * 32 + (n - 16) + 16 * h) * 16, xp[2][0][k]);
    }
    publish<1, 0, 7>(ws, FK_OFF + j * 256 + rt * 4, tag); SB0;
#pragma unroll
    for (int k = 0; k < 2; ++k) xp[2][1][k] = lds_ld<u32x4>(L_KX + (((w ^ 4) * 2 + k) * 32 + (n & 15) + 16 * h) * 16);
    f32x16 z16;
#pragma unroll
    for (int i = 0; i < 16; ++i) z16[i] = 0.f;
    f32x16 pmk = z16, pmm = z16, pvk = z16, pvm = z16;
    constexpr u32 IMG = L_KX + 8192, DUMP = IMG + 8 * 3840;
    const int lo8 = 8 * cgp, mix = 32 - 32 * cgp;
    u32 avk[4], amk, avm[2], amm;
    auto img_at = [&](int kc, int vc, bool on) { return (on && (kc >= 20) == (vc >= 20)) ? IMG + w * 3840 + kc * 96 + (vc >= 20 ? vc - 20 : vc) * 4 : DUMP + l * 16; };
#pragma unroll
    for (int i = 0; i < 8; ++i) {
      mm(1, i >> 2, i & 3); epi2(6 + i);
      if (i < 4) pf2(64 + 8 * i);
      if (i >= 2 && i < 6) pmm = mfma32(xp[2][(i - 2) >> 1][i & 1], xp[2][(i - 2) >> 1][i & 1], pmm);
      if (i == 6) { amm = img_at(mix + n, mix + 4 * h, n < 8); amk = img_at(lo8 + n, mix + 4 * h, true); asm volatile("" : "+v"(amm), "+v"(amk)); }
      if (i == 7) {
#pragma unroll
        for (int q = 0; q < 2; ++q) { avm[q] = img_at(mix + n, lo8 + 16 * (1 - cgp) + 8 * q + 4 * h, n < 8); asm volatile("" : "+v"(avm[q])); }
      }
      SB0;
    }
    STAMP(3);
#pragma unroll
    for (int i = 0; i < 6; ++i) {
      if (i < 4) pmk = mfma32(xp[2][i >> 1][i & 1], xp[0][i >> 1][i & 1], pmk);
      if (i >= 2 && i < 4) { pvk = mfma32(xp[1][0][i & 1], xp[0][0][i & 1], pvk); pvm = mfma32(xp[1][0][i & 1], xp[2][0][i & 1], pvm); }
      epi2(14 + i);
      if (i < 4) { avk[i] = img_at(lo8 + n, lo8 + 8 * i + 4 * h, true); asm volatile("" : "+v"(avk[i])); }
      if (i == 1) lds_st<f32x4>(amm, f32x4{pmm[4], pmm[5], pmm[6], pmm[7]});
      if (i == 4) {
        const float z0 = xsum32(zs[0]), z1 = xsum32(zs[1]);
        lds_st<float>(h == 0 ? IMG + w * 3840 + (lo8 + n) * 96 + 80 : (n & 8) ? DUMP + l * 16 : (n < 8 ? IMG + w * 3840 + (mix + n) * 96 + 80 : IMG + (w ^ 4) * 3840 + (32 - mix + n - 16) * 96 + 84), h == 0 ? z0 : z1);
      }
      if (i == 5) lds_st<f32x4>(amk, f32x4{pmk[4], pmk[5], pmk[6], pmk[7]});
      SB0;
    }
#pragma unroll
    for (int k = 0; k < 2; ++k) { pvk = mfma32(xp[1][1][k], xp[0][1][k], pvk); pvm = mfma32(xp[1][1][k], xp[2][1][k], pvm); }
    if (tid < 2) lds_st<u32x4>(L_CNT + tid * 16, u32x4{0u, 0u, 0u, 0u});
#pragma unroll
    for (int q = 0; q < 4; ++q) lds_st<f32x4>(avk[q], f32x4{pvk[4 * q], pvk[4 * q + 1], pvk[4 * q + 2], pvk[4 * q + 3]});
#pragma unroll
    for (int q = 0; q < 2; ++q) lds_st<f32x4>(avm[q], cgp ? f32x4{pvm[4 * q], pvm[4 * q + 1], pvm[4 * q + 2], pvm[4 * q + 3]} : f32x4{pvm[8 + 4 * q], pvm[9 + 4 * q], pvm[10 + 4 * q], pvm[11 + 4 * q]});
    __syncthreads();
    if (tid < 480) {
      const int cg = tid >= 240, o = (tid - 240 * cg) * 16;
      f32x4 sum = lds_ld<f32x4>(IMG + cg * 15360 + o);
#pragma unroll
      for (int k = 1; k < 4; ++k) sum += lds_ld<f32x4>(IMG + cg * 15360 + k * 3840 + o);
      bstore16(nosent4(__builtin_bit_cast(u32x4, sum)), rs, KVR_OFF + (u32)((2 * j + cg) * 8 + rt) * 3840 + o);
    }
    STAMP(4);
    wait_vm<1>(); wait_lgkm0(); bar();
    STAMP(5);
  }
}

DI void lds_ticket(u32 a) {
  asm volatile("s_mov_b64 exec, 1\n\tds_add_u32 %0, %1\n\ts_mov_b64 exec, -1" ::"v"(a), "v"(1u) : "memory");
}
DI void lds_wait(u32 a, u32 want) {
#pragma unroll 1
  for (int spin = 0; spin < SPIN_CAP * 16; ++spin) {
    if (__builtin_amdgcn_readfirstlane(__hip_atomic_load((LAS u32*)(size_t)a, __ATOMIC_RELAXED, __HIP_MEMORY_SCOPE_WORKGROUP)) >= want) break;
    __builtin_amdgcn_s_sleep(2);
  }
  asm volatile("" ::: "memory");
}

DI void lds_meet(u32 a, u32 want) { asm volatile("s_waitcnt lgkmcnt(0)" ::: "memory"); lds_ticket(a); lds_wait(a, want); }

DI void mjob(int pair, u32 tag, char* ws, rsrc_t rs, const u32x4 (&WO)[5][2]) {
  int tid = threadIdx.x;
  asm volatile("" : "+v"(tid));
  const int l = tid & 63, w = __builtin_amdgcn_readfirstlane(tid >> 6);
  constexpr u32 KVL = L_MJ;
  __builtin_amdgcn_s_setprio(3);
  STAMP(5);
  if (w < 4) {
    wait_flags<16>(ws, FK_OFF + (pair >> 1) * 256 + (l & 7) * 4, l < 8, tag);
    const int t4 = min(tid, 239);
    u32x4 pa[8];
#pragma unroll 1
    for (int spin = 0; spin < SPIN_CAP; ++spin) {
      u32 mx = 0;
#pragma unroll
      for (int k = 0; k < 8; ++k) pa[k] = bload(rs, KVR_OFF + (u32)pair * 30720 + t4 * 16, k * 3840);
#pragma unroll
      for (int k = 0; k < 8; ++k) mx = max4(mx, pa[k]);
      if (!__any(mx == SENT)) break;
      __builtin_amdgcn_s_sleep(4);
      asm volatile("" ::: "memory");
    }
    f32x4 sum = __builtin_bit_cast(f32x4, pa[0]);
#pragma unroll
    for (int k = 1; k < 8; ++k) sum += __builtin_bit_cast(f32x4, pa[k]);
    const bool zc = t4 % 6 == 5;
    lds_st<u32x2>(KVL + t4 * 8, zc ? u32x2{0u, 0u} : u32x2{pk_bf16(sum[0], sum[1]), pk_bf16(sum[2], sum[3])});
    if (zc) { const float z = sum[0] + (((pair & 1) ? t4 < 48 : t4 >= 192) ? sum[1] : 0.f); lds_st<u16>(KVL + 1920 + (t4 / 6) * 2, (u16)pk_bf16(z, z)); }
  }
  wait_lgkm0(); bar();
  asm volatile("" ::: "memory");
  if (w >= 5) { const int e = tid - 320; bstore16(nosent4(lds_ld<u32x4>(KVL + 1920 + (e % 5) * 16)), rs, e < 160 ? ZB_OFF + (e / 5) * 1280 + pair * 80 + (e % 5) * 16 : OOB); }
  STAMP(6);
  const int g = l >> 4, c = l & 15;
  const u32x4 zero4 = {0u, 0u, 0u, 0u};
  const int kx = 8 * (c >> 2) + (c & 3), ky = kx + 4, kz = 32 + (c & 7), go = min(g, 2) * 16;
  const u32x4 rx = lds_ld<u32x4>(KVL + kx * 48 + go), ry = lds_ld<u32x4>(KVL + ky * 48 + go), rz = lds_ld<u32x4>(KVL + kz * 48 + go);
  const u32x4 ax0 = (kx < 20 && g < 3) ? rx : zero4, ax1 = (kx >= 20 && g < 3) ? rx : zero4, ay0 = (ky < 20 && g < 3) ? ry : zero4, ay1 = (ky >= 20 && g < 3) ? ry : zero4, az = g < 3 ? rz : zero4;
  const u32 mo = MT_OFF + (u32)(100 * w + 5 * (pair >> 2)) * 1024 + (c + 16 * (pair & 3)) * 16;
  f32x4 dx[5], dy[5], dz[5];
#pragma unroll
  for (int cb = 0; cb < 5; ++cb) { const f32x4 z4 = {0.f, 0.f, 0.f, 0.f}; dx[cb] = mfma16(ax0, WO[cb][0], z4); dy[cb] = mfma16(ay0, WO[cb][0], z4); dz[cb] = mfma16(az, WO[cb][1], z4); }
#pragma unroll
  for (int cb = 0; cb < 5; ++cb) { dx[cb] = mfma16(ax1, WO[cb][1], dx[cb]); dy[cb] = mfma16(ay1, WO[cb][1], dy[cb]); }
  SB0;
#pragma unroll
  for (int cb = 0; cb < 5; ++cb) {
    bstore16(nosent4(u32x4{pk_bf16(dx[cb][0], dx[cb][1]), pk_bf16(dx[cb][2], dx[cb][3]), pk_bf16(dy[cb][0], dy[cb][1]), pk_bf16(dy[cb][2], dy[cb][3])}), rs, mo + g * 1024 + cb * 20480);
    bstore8(u32x2{nosent(pk_bf16(dz[cb][0], dz[cb][1])), nosent(pk_bf16(dz[cb][2], dz[cb][3]))}, rs, g < 2 ? mo + 4096 + 8 * g + cb * 20480 : OOB);
  }
  __builtin_amdgcn_s_setprio(0);
  STAMP(7);
}

DI int wt_step(int i) { const int q = i < 7 ? 0 : i < 14 ? 1 : i < 20 ? 2 : 3; return i + 7 * (q + 1); }

DI void spare(int sg, int c2, const u16* x, const u16* wo, const u16* wd, char* ws, u32& tag_out, u32x4 (&WO)[5][2]) {
  int tid = threadIdx.x;
  asm volatile("" : "+v"(tid));
  const int l = tid & 63, w = __builtin_amdgcn_readfirstlane(tid >> 6);
  u32x4 v[4][5];
  const int row0 = 128 * sg + 16 * w + (l >> 4);
#pragma unroll
  for (int a = 0; a < 4; ++a)
#pragma unroll
    for (int k = 0; k < 5; ++k) v[a][k] = *(const u32x4*)((const char*)x + (size_t)(row0 + 4 * a) * 1280 + k * 256 + (l & 15) * 16);
  u32 ep;
  asm volatile("s_load_dword %0, %1, 0x0\n\ts_waitcnt lgkmcnt(0)" : "=s"(ep) : "s"(ws + EP_OFF) : "memory");
  const u32 tag = ep + 1u;
  tag_out = tag;
  const int nst = sg < 10 ? 2 : 1;
#pragma unroll
  for (int a = 0; a < 4; ++a) {
    float s = 0.f, q = 0.f;
#pragma unroll
    for (int k = 0; k < 5; ++k)
#pragma unroll
      for (int e = 0; e < 4; ++e) { s = dot2(v[a][k][e], 0x3f803f80u, s); q = dot2(v[a][k][e], v[a][k][e], q); }
#pragma unroll
    for (int o = 1; o < 16; o <<= 1) { s += __shfl_xor(s, o); q += __shfl_xor(q, o); }
    const float mean = s * (1.f / 640.f);
    const float sd = __builtin_amdgcn_sqrtf(round_bf(round_bf(fmaxf(q * (1.f / 640.f) - mean * mean, 0.f)) + 1e-5f));
    if ((l & 15) == 0) __hip_atomic_store((GAS u64*)(ws + STG_OFF) + row0 + 4 * a, ((u64)tag << 32) | pk_bf16(mean, sd), RLX_AGENT);
  }
  STAMP(1);
#pragma unroll 1
  for (int a = 0; a < nst; ++a)
#pragma unroll
    for (int i = 0; i < 5; ++i) dma16((const char*)wd + (size_t)wt_step(sg + 16 * a) * 40960 + (w * 5 + i) * 1024, l * 16, a * 40960 + (w * 5 + i) * 1024);
#pragma unroll 1
  for (int pc = w; pc < 50; pc += 8) dma16((const char*)(wo + (size_t)sg * 40 * 640) + pc * 1024, l * 16, 81920 + pc * 1024);
  wait_vm<0>();
  __syncthreads();
  STAMP(2);
  const rsrc_t rs = __builtin_amdgcn_make_buffer_rsrc(ws, 0, WS_END, 0x00020000);
#pragma unroll 1
  for (int a = 0; a < nst; ++a)
#pragma unroll
    for (int i = 0; i < 5; ++i) {
      const int cb = w * 5 + i;
      const u32x4 f = tr_frag(a * 40960 + (8 * (l >> 4) + ((l & 15) >> 2)) * 1280 + cb * 32 + 8 * (l & 3), 5120);
      bstore16(f, rs, WT_OFF + (u32)(cb * 54 + wt_step(sg + 16 * a)) * 1024 + l * 16);
    }
  {
    const u32 ra = 81920 + min(8 * (l >> 4) + ((l & 15) >> 2), 19) * 1280 + w * 160 + 8 * (l & 3), rb = 81920 + min(8 * (l >> 4) + 4 + ((l & 15) >> 2), 19) * 1280 + w * 160 + 8 * (l & 3);
    const u32 mo = MT_OFF + (u32)(100 * w + 5 * (sg >> 2)) * 1024 + ((l & 15) + 16 * (sg & 3)) * 16;
#pragma unroll
    for (int cb = 0; cb < 5; ++cb) {
#pragma unroll
      for (int hh = 0; hh < 2; ++hh) { const u32x2 lo = tr_read(ra + cb * 32 + hh * 25600), hi = tr_read(rb + cb * 32 + hh * 25600); WO[cb][hh] = u32x4{lo[0], lo[1], hi[0], hi[1]}; }
      bstore16(u32x4{SENT, SENT, SENT, SENT}, rs, mo + (l >> 4) * 1024 + cb * 20480);
      bstore16(u32x4{SENT, SENT, SENT, SENT}, rs, l < 16 ? mo + 4096 + cb * 20480 : OOB);
    }
    if (w >= 5) { const int e = tid - 320; bstore16(u32x4{SENT, SENT, SENT, SENT}, rs, e < 160 ? ZB_OFF + (e / 5) * 1280 + sg * 80 + (e % 5) * 16 : OOB); }
  }
  STAMP(3);
  publish<8, 256>(ws, FW_OFF + sg * 4, tag);
  STAMP(4);
#pragma unroll 1
  for (int pc = w; pc < 140; pc += 8) p2_piece((const char*)(wd + 80 * c2), pc, l);
  if (tid < 2) lds_st<u32x4>(L_CNT + tid * 16, u32x4{0u, 0u, 0u, 0u});
  if (sg == 0 && w < 4) {
    const u32 off = tid < 240 ? CSG_OFF + tid * 1536 + 4 : STG_OFF + (tid - 240) * 1024 + 4;
    wait_flags(ws, off, true, tag);
  }
  wait_vm<0>();
  __syncthreads();
  if (sg == 0 && tid == 0) __hip_atomic_store((GAS u32*)(ws + EP_OFF), tag, RLX_AGENT);
}

DI u32 p2_attblk(int u, int j) { return j < 14 ? L_ATT + (u * 14 + j) * 1024 : u * 25600 + 20480; }
constexpr int h_step(int t) { return t < 6 ? 5 + t : t < 11 ? t - 6 : t; }
constexpr bool h_lds(int t) { return t < 2 || (t >= 6 && t < 11); }
constexpr int a_first(int t) { return t < 0 ? 0 : t < 2 ? 4 + t : t < 6 ? 2 + 2 * t : 14; }
constexpr int lds_seq(int t, int cb) { return (t < 2 ? t : t - 4) * 5 + cb; }
template <int T> DI void p2_hsteps(f32x4 (&acc)[5][4], u32x4 (&A)[14][4], u32x4 (&B)[4][5], u32x4 (&bl)[3], rsrc_t rs, u32 va, const u32 (&so)[4], u32 vb, u32 wl, u32 wl2, bool extra, Poll& pl, const u16* xp, u32x2 (&xr)[5]) {
  if constexpr (T < 14) {
#pragma unroll
    for (int cb = 0; cb < 5; ++cb) {
      const u32x4 bc = h_lds(T) ? bl[lds_seq(T, cb) % 3] : B[T < 6 ? T - 2 : T - 11][cb];
      const int q = h_lds(T) ? lds_seq(T, cb) + 2 : (T == 5 && cb >= 3) ? 7 + cb : -1;
      if (q >= 0 && q < 35 && !(T == 1 && q >= 10)) bl[q % 3] = tr_frag((q < 10 ? wl2 + (q / 5) * 5120 : wl + (q / 5 - 2) * 5120) + (q % 5) * 32, 1280);
#pragma unroll
      for (int t = 0; t < 4; ++t) acc[cb][t] = mfma16(bc, A[T][t], acc[cb][t]);
      SB0;
    }
    if constexpr (T == 0) poll_done(pl);
    if constexpr (T == 9) {
#pragma unroll
      for (int cb = 0; cb < 5; ++cb) xr[cb] = *(const u32x2*)(xp + 16 * cb);
    }
    if constexpr (T == 1) lds_ticket(L_CNT + 4);
    if constexpr (T == 10) lds_ticket(L_CNT);
#pragma unroll
    for (int n = a_first(T); n < a_first(T + 1); ++n)
#pragma unroll
      for (int t = 0; t < 4; ++t) A[n][t] = bload(rs, n == 13 ? (extra ? va : OOB) : va, so[t] + h_step(n) * 1024);
    if constexpr (T >= 6 && T < 9) {
#pragma unroll
      for (int cb = 0; cb < 5; ++cb) B[T - 6][cb] = bload(rs, T == 8 ? (extra ? vb : OOB) : vb, cb * 55296 + (T + 5) * 1024);
    }
    SB0;
    p2_hsteps<T + 1>(acc, A, B, bl, rs, va, so, vb, wl, wl2, extra, pl, xp, xr);
  }
}

template <bool JOB> DI void phase2(int r, int c, int job, bool late, u32 tag, const u16* x, const u16* wd, u16* out, char* ws, const u32x4 (&WO)[5][2]) {
  int tid = threadIdx.x;
  asm volatile("" : "+v"(tid), "+s"(ws), "+s"(tag));
  const int l = tid & 63, w = __builtin_amdgcn_readfirstlane(tid >> 6);
  const rsrc_t rs = __builtin_amdgcn_make_buffer_rsrc(ws, 0, WS_END, 0x00020000);
  const int g = l >> 4, n = l & 15;
  auto orow = [&]() { int t = threadIdx.x; asm volatile("" : "+v"(t)); return (u32)((64 * r + 16 * (w & 3) + (t & 15)) * 640 + 80 * c + ((t >> 2) & 12)); };
  const u32 fh = FH_OFF + c * 2048 + (r >> 2) * 256;
  STAMP(8);
  const int start = p2_start(w & 3);
  const u32 vb = WT_OFF + (u32)(5 * c * 54 + start) * 1024u + l * 16;
  u32x4 B[4][5];
  asm volatile("s_cmp_lg_u32 %0, 0\n\ts_cbranch_scc1 .Lnp%=\n\ts_setprio 3\n.Lnp%=:" ::"s"(w) : "scc");
  if (w < 4) {
    if (late) {
#pragma unroll 1
      for (int pc = max(100 + 10 * w, 116); pc < 110 + 10 * w; ++pc) p2_piece((const char*)(wd + 80 * c), pc, l);
    }
    wait_flags<24>(ws, FW_OFF + c * 256 + n * 4, l < 16, tag);
#pragma unroll
    for (int s = 0; s < 4; ++s) {
#pragma unroll
      for (int cb = 0; cb < 5; ++cb) B[s][cb] = bload(rs, vb, cb * 55296 + (7 + s) * 1024);
      SB0;
    }
  }
  if (JOB) mjob(job, tag, ws, rs, WO);
  if (w >= 4) {
    const int u = w - 4;
    int ta = threadIdx.x;
    asm volatile("" : "+v"(ta));
    const int l = ta & 63, g = l >> 4, n = l & 15;
    u32x4 P[4][5];
    auto load_p = [&]() {
#pragma unroll
      for (int t = 0; t < 4; ++t) {
#pragma unroll
        for (int s = 0; s < 5; ++s) P[t][s] = bload(rs, ACT_OFF + (u32)(4 * r * NSTEP + 54 + 5 * u) * 1024u + l * 16, ((t + u) & 3) * (NSTEP * 1024) + s * 1024);
        SB0;
      }
    };
    __builtin_amdgcn_s_setprio(2);
    if (u == 0) { wait_flags<8>(ws, l < 16 ? FW_OFF + c * 256 + l * 4 : fh + (18 + (l & 3)) * 4, l < 20, tag); lds_ticket(L_CNT + 20); } else lds_wait(L_CNT + 20, 1);
    load_p();
    __builtin_amdgcn_s_setprio(0);
    STAMP(9);
    f32x4 acc[5][4];
    const u32 mhint = MT_OFF + (u32)((5 * c + 4) * 20 + 5 * u + 4) * 1024u + g * 256 + 252;
    float rd[4][2];
    u32 mh;
    {
      u32x4 Z[5];
#pragma unroll 1
      for (int spin = 0; spin < SPIN_CAP; ++spin) {
        u32 mz = 0;
#pragma unroll
        for (int s = 0; s < 5; ++s) Z[s] = bload(rs, ZB_OFF + r * 1280 + 320 * u + 80 * g, s * 16);
        mh = __builtin_amdgcn_raw_buffer_load_b32(rs, mhint, 0, 16);
#pragma unroll
        for (int s = 0; s < 5; ++s) mz = max4(mz, Z[s]);
        if (!__any(mz == SENT)) break;
        __builtin_amdgcn_s_sleep(10);
        asm volatile("" ::: "memory");
      }
      STAMP(14);
      const bool m0 = n == 4 * g, m1 = n == 4 * g + 1;
#pragma unroll
      for (int s = 0; s < 5; ++s)
#pragma unroll
        for (int q = 0; q < 4; ++q) Z[s][q] = ((8 * s + 2 * q >= 20) ? m1 : m0) ? Z[s][q] : 0u;
#pragma unroll
      for (int t = 0; t < 4; ++t) {
        f32x4 den = {0.f, 0.f, 0.f, 0.f};
#pragma unroll
        for (int s = 0; s < 5; ++s) den = mfma16(Z[s], P[t][s], den);
        rd[t][0] = __builtin_amdgcn_rcpf(den[0] + 1e-6f); rd[t][1] = __builtin_amdgcn_rcpf(den[1] + 1e-6f);
      }
    }
    STAMP(11);
    if (__any(mh == SENT)) wait_data<4>(rs, mhint);
    STAMP(10);
    u32x4 M[5][5];
    const u32 vm = MT_OFF + (u32)(5 * c * 20 + 5 * u) * 1024u + l * 16;
    auto load_m = [&](int c0, int c1) {
#pragma unroll
      for (int cb = 0; cb < 5; ++cb) {
        if (cb < c0 || cb >= c1) continue;
#pragma unroll
        for (int s = 0; s < 5; ++s) M[cb][s] = bload(rs, vm, (cb * 20 + s) * 1024);
      }
    };
    __builtin_amdgcn_s_setprio(2);
#pragma unroll
    for (int t = 0; t < 4; ++t)
#pragma unroll
      for (int s = 0; s < 5; ++s) {
#pragma unroll
        for (int k = MRATE * (5 * t + s); k < MRATE * (5 * t + s + 1); ++k) if (k < 25) M[k / 5][k % 5] = bload(rs, vm, ((k / 5) * 20 + k % 5) * 1024);
#pragma unroll
        for (int q = 0; q < 4; ++q) { const float rr = rd[t][8 * s + 2 * q >= 20]; P[t][s][q] = pk_bf16(bf_lo(P[t][s][q]) * rr, bf_hi(P[t][s][q]) * rr); }
        asm volatile("" : "+v"(P[t][s]));
        SB0;
      }
    __builtin_amdgcn_s_setprio(APRIO);
    lds_wait(L_CNT + 4, 4);
    STAMP(17);
    u32 mx = 0;
#pragma unroll
    for (int s = 0; s < 5; ++s) mx = max4(mx, M[0][s]);
#pragma unroll
    for (int cb = 0; cb < 6; ++cb) {
      if (cb < 5 && __builtin_expect(__any(mx == SENT), 0)) {
#pragma unroll 1
        for (int spin = 0; spin < SPIN_CAP; ++spin) {
          asm volatile("" ::: "memory");
          load_m(cb, 5);
          mx = 0;
#pragma unroll
          for (int s = 0; s < 5; ++s) mx = max4(mx, M[cb][s]);
          if (!__any(mx == SENT)) break;
        }
      }
      mx = 0;
#pragma unroll
      for (int s = 0; s < 5; ++s) {
        if (cb < 5) {
#pragma unroll
          for (int t = 0; t < 4; ++t) acc[cb][t] = mfma16(M[cb][s], P[t][s], s == 0 ? f32x4{0.f, 0.f, 0.f, 0.f} : acc[cb][t]);
        }
        if (cb < 4 && s >= 2) { mx = max4(mx, M[cb + 1][2 * s - 4]); if (s < 4) mx = max4(mx, M[cb + 1][2 * s - 3]); }
        if (cb > 0 && s >= 1 && s < 4 && !(cb == 5 && s == 3)) lds_st<f32x4>(p2_attblk(u, cb * 3 - 4 + s) + l * 16, acc[cb - 1][s]);
        SB0;
      }
    }
    STAMP(16);
    lds_wait(L_CNT, 4);
#pragma unroll
    for (int k = 0; k < 6; ++k) lds_st<f32x4>(u * 25600 + (15 + k) * 1024 + l * 16, k < 5 ? acc[k][0] : acc[4][3]);
    STAMP(15);
    __syncthreads();
    if constexpr (!JOB) asm volatile(".Lfk_end_a:");
    __builtin_amdgcn_endpgm();
  } else {
    const u32 va = ACT_OFF + (u32)(4 * r * NSTEP + start) * 1024u + l * 16;
    u32 so[4];
#pragma unroll
    for (int t = 0; t < 4; ++t) so[t] = ((t + w) & 3) * (NSTEP * 1024);
    const u32 wi = (16 * (g >> 1) + 4 * (g & 1) + (n >> 2)) * 160 + 8 * (l & 3), wl = w * 25600 + wi, wl2 = L_ATT + w * 10240 + wi;
    u32x4 A[14][4], bl[3];
    bl[0] = tr_frag(wl2, 1280); bl[1] = tr_frag(wl2 + 32, 1280);
    Poll pl = {};
    if (w == 0) { poll_flags(pl, ws, fh + min(l, 17) * 4, tag); lds_ticket(L_CNT + 16); } else lds_wait(L_CNT + 16, 1);
    STAMP(9);
    __builtin_amdgcn_s_setprio(HPRIO);
#pragma unroll
    for (int s = 0; s < 4; ++s) {
#pragma unroll
      for (int t = 0; t < 4; ++t) A[s][t] = bload(rs, va, so[t] + h_step(s) * 1024);
      SB0;
    }
    f32x4 acc[5][4];
#pragma unroll
    for (int cb = 0; cb < 5; ++cb)
#pragma unroll
      for (int t = 0; t < 4; ++t) acc[cb][t] = f32x4{0.f, 0.f, 0.f, 0.f};
    const u32 oo = (u32)((64 * r + 16 * w + n) * 640 + 80 * c + 4 * g);
    u32x2 xr[5];
    p2_hsteps<0>(acc, A, B, bl, rs, va, so, vb, wl, wl2, w < 2, pl, x + oo, xr);
    STAMP(10);
#pragma unroll
    for (int cb = 0; cb < 5; ++cb)
#pragma unroll
      for (int t = 1; t < 4; ++t) lds_st<f32x4>(w * 25600 + (cb * 3 + (t - 1)) * 1024 + l * 16, acc[cb][t]);
#pragma unroll
    for (int cb = 0; cb < 5; ++cb) { acc[cb][0] += f32x4{bf_lo(xr[cb][0]), bf_hi(xr[cb][0]), bf_lo(xr[cb][1]), bf_hi(xr[cb][1])}; asm volatile("" : "+v"(acc[cb][0])); }
    lds_meet(L_CNT + 8, 4);
#pragma unroll
    for (int cb = 0; cb < 5; ++cb) {
#pragma unroll
      for (int o = 1; o < 4; ++o) acc[cb][0] += lds_ld<f32x4>(((w + o) & 3) * 25600 + (cb * 3 + (3 - o)) * 1024 + l * 16);
      asm volatile("" : "+v"(acc[cb][0]));
    }
    STAMP(11);
    __syncthreads();
    STAMP(12);
#pragma unroll
    for (int cb = 0; cb < 5; ++cb) {
      f32x4 s = acc[cb][0];
      s += lds_ld<f32x4>(w * 25600 + (15 + cb) * 1024 + l * 16);
#pragma unroll
      for (int o = 1; o < 4; ++o) s += lds_ld<f32x4>(p2_attblk((w + o) & 3, cb * 3 + (3 - o)) + l * 16);
      *(u32x2*)(out + oo + 16 * cb) = u32x2{pk_bf16(s[0], s[1]), pk_bf16(s[2], s[3])};
    }
    STAMP(13);
    if constexpr (!JOB) asm volatile(".Lfk_end_h:");
    __builtin_amdgcn_endpgm();
  }
}

__host__ __device__ inline void decode(int bid, int& rt, int& ct) {
  const int xcd = bid & 7, idx = bid >> 3, cgrp = xcd >> 1, ci = idx >> 2, c2 = cgrp - 2;
  const int a = 18 + 4 * c2 + ci, b = 22 + 2 * c2 + ci, c = ci == 7 ? 30 : 16 + c2;
  const int m = ci < 4 ? a : (ci < 6 ? b : c);
  ct = cgrp < 2 ? cgrp * 8 + ci : m;
  rt = 4 * (xcd & 1) + (idx & 3) + (ct == 30 ? 8 * c2 : 0);
}

__global__ __launch_bounds__(512, 2) void fused_kernel(const u16* __restrict__ x, const u16* __restrict__ wqkv, const u16* __restrict__ wo,
                                                       const u16* __restrict__ wg, const u16* __restrict__ wu, const u16* __restrict__ wd,
                                                       u16* __restrict__ out, char* ws) {
  asm volatile("" ::"s"(x), "s"(wqkv), "s"(wo), "s"(wg), "s"(wu), "s"(wd), "s"(out), "s"(ws));
  STAMP(0);
  const int bid = blockIdx.x, xcd = bid & 7, idx = bid >> 3;
  const int r2 = xcd * 4 + (idx >> 3), c2 = idx & 7;
  int rt, ct;
  decode(bid, rt, ct);
  asm volatile("" : "+s"(rt), "+s"(ct));
  P1Src src;
  if (__builtin_expect(ct != 30, 1)) phase1_start(src, rt, ct, x, wqkv, wg, wu);
  {
    const char* pc; u32 n0, n1;
    asm volatile("s_getpc_b64 %0\n.Lfk_beg:\n\ts_mov_b32 %1, .Lfk_end_h-.Lfk_beg\n\ts_mov_b32 %2, .Lfk_end_a-.Lfk_beg" : "=s"(pc), "=s"(n0), "=s"(n1));
    const int w = __builtin_amdgcn_readfirstlane(threadIdx.x >> 6);
    dma16((const char*)((size_t)pc & ~(size_t)15) + min((u32)(idx * 8 + w) * 512u, max(n0, n1) - 1024u), (threadIdx.x & 31) * 16, L_SP + (w & 3) * 1024);
  }
  u32 tag;
  u32x4 WO[5][2];
  if (ct == 30) {
    spare(rt, c2, x, wo, wd, ws, tag, WO);
    phase2<true>(r2, c2, rt, false, tag, x, wd, out, ws, WO);
  } else {
    if (ct < 18) phase1<0>(src, rt, ct, ct, c2, wd, ws, tag);
    else if (ct < 26) phase1<2>(src, rt, ct, ct - 18, c2, wd, ws, tag);
    else phase1<1>(src, rt, ct, ct - 26, c2, wd, ws, tag);
    phase2<false>(r2, c2, -1, ct >= 18 && ct < 26, tag, x, wd, out, ws, WO);
  }
}

extern "C" void kernel_launch(void* const* d_in, const int* in_sizes, int n_in, void* d_out, int out_size, void* d_ws, size_t ws_size, hipStream_t stream) {
  (void)hipFuncSetAttribute((const void*)fused_kernel, hipFuncAttributeMaxDynamicSharedMemorySize, LDS_BYTES);
  fused_kernel<<<256, 512, LDS_BYTES, stream>>>((const u16*)d_in[0], (const u16*)d_in[1], (const u16*)d_in[2], (const u16*)d_in[4], (const u16*)d_in[5],
                                                (const u16*)d_in[6], (u16*)d_out, (char*)d_ws);
}
```

```cpp
#include <hip/hip_runtime.h>
#include <stdint.h>
#include <type_traits>

#define ABL_NOREAD 0
#define ABL_NODMA 0
#define MRATE 3
#define APRIO 0
#define HPRIO 1
#define MAPPING 2
#define ABL_NOROT 0
typedef unsigned short u16;
typedef unsigned int u32;
typedef unsigned long long u64;
typedef __bf16 bf16x8 __attribute__((ext_vector_type(8)));
typedef __bf16 bf16x2 __attribute__((ext_vector_type(2)));
typedef short s16x4 __attribute__((ext_vector_type(4)));
typedef float f32x2 __attribute__((ext_vector_type(2)));
typedef float f32x4 __attribute__((ext_vector_type(4)));
typedef float f32x16 __attribute__((ext_vector_type(16)));
typedef u32 u32x2 __attribute__((ext_vector_type(2)));
typedef u32 u32x4 __attribute__((ext_vector_type(4)));
typedef __amdgpu_buffer_rsrc_t rsrc_t;
#define LAS __attribute__((address_space(3)))
#define GAS __attribute__((address_space(1)))
#define DI __device__ __forceinline__
#define RLX_AGENT __ATOMIC_RELAXED, __HIP_MEMORY_SCOPE_AGENT
#define SB0 __builtin_amdgcn_sched_barrier(0)

constexpr int NSTEP = 74;
constexpr u32 ACT_OFF = 0;
constexpr u32 MT_OFF  = ACT_OFF + 128u * NSTEP * 1024u;
constexpr u32 WT_OFF  = MT_OFF + 40u * 20u * 1024u;
constexpr u32 KVR_OFF = WT_OFF + 40u * 54u * 1024u;
constexpr u32 ZB_OFF  = KVR_OFF + 16u * 8u * 3840u;
constexpr u32 STG_OFF = ZB_OFF + 32u * 1280u;
constexpr u32 CSG_OFF = STG_OFF + 2048u * 8u;
constexpr u32 FH_OFF  = CSG_OFF + 30u * 8u * 192u * 8u;
constexpr u32 FK_OFF  = FH_OFF + 64u * 256u;
constexpr u32 FW_OFF  = FK_OFF + 8u * 256u;
constexpr u32 EP_OFF  = FW_OFF + 8u * 256u;
constexpr u32 WS_END  = EP_OFF + 256u;
constexpr u32 OOB = 0x7ffffff0u;
constexpr int SPIN_CAP = 1 << 15;

constexpr int L_W = 98304, L_SP = 159744, L_ATT = 102400, L_KX = 119808, L_MJ = 143360, L_CNT = 163808, LDS_BYTES = 163840;
constexpr int XB0 = 65536, XB1 = 0, XB2 = 32768;

#define STAMP(i) do {} while (0)
#define SSTAMP(i) do {} while (0)

DI float bf_lo(u32 v) { return __uint_as_float(v << 16); }
DI float bf_hi(u32 v) { return __uint_as_float(v & 0xffff0000u); }
DI u32 pk_bf16(float a, float b) { f32x2 v = {a, b}; return __builtin_bit_cast(u32, __builtin_convertvector(v, bf16x2)); }
DI float round_bf(float a) { return bf_lo(pk_bf16(a, a)); }
DI float dot2(u32 a, u32 b, float c) { return __builtin_amdgcn_fdot2_f32_bf16(__builtin_bit_cast(bf16x2, a), __builtin_bit_cast(bf16x2, b), c, false); }
DI float xsum32(float v) { auto r = __builtin_amdgcn_permlane32_swap(__float_as_uint(v), __float_as_uint(v), false, false); return __uint_as_float(r[0]) + __uint_as_float(r[1]); }
DI float xsum16(float v) { auto r = __builtin_amdgcn_permlane16_swap(__float_as_uint(v), __float_as_uint(v), false, false); return __uint_as_float(r[0]) + __uint_as_float(r[1]); }
DI float oct_sum(float v) {
  v += __uint_as_float(__builtin_amdgcn_mov_dpp(__float_as_uint(v), 0xB1, 0xf, 0xf, true));
  v += __uint_as_float(__builtin_amdgcn_mov_dpp(__float_as_uint(v), 0x4E, 0xf, 0xf, true));
  return v + __uint_as_float(__builtin_amdgcn_mov_dpp(__float_as_uint(v), 0x141, 0xf, 0xf, true));
}
DI float wave_sum(float v) { v = oct_sum(v); v += __uint_as_float(__builtin_amdgcn_mov_dpp(__float_as_uint(v), 0x140, 0xf, 0xf, true)); return xsum32(xsum16(v)); }
DI f32x16 mfma32(u32x4 a, u32x4 b, f32x16 c) { return __builtin_amdgcn_mfma_f32_32x32x16_bf16(__builtin_bit_cast(bf16x8, a), __builtin_bit_cast(bf16x8, b), c, 0, 0, 0); }
DI f32x4 mfma16(u32x4 a, u32x4 b, f32x4 c) { return __builtin_amdgcn_mfma_f32_16x16x32_bf16(__builtin_bit_cast(bf16x8, a), __builtin_bit_cast(bf16x8, b), c, 0, 0, 0); }
DI u32x2 tr_read(u32 a) { return __builtin_bit_cast(u32x2, __builtin_amdgcn_ds_read_tr16_b64_v4i16((LAS s16x4*)(size_t)a)); }
DI u32x4 tr_frag(u32 a, int d) { const u32x2 lo = tr_read(a), hi = tr_read(a + d); return u32x4{lo[0], lo[1], hi[0], hi[1]}; }
template <class T> DI T lds_ld(u32 a) { return *(const LAS T*)(size_t)a; }
template <class T> DI void lds_st(u32 a, T v) { *(LAS T*)(size_t)a = v; }
DI void dma16(const char* sbase, u32 voff, u32 ldst) {
  asm volatile("s_mov_b32 m0, %2\n\ts_nop 0\n\tglobal_load_lds_dwordx4 %0, %1" ::"v"(voff), "s"(sbase), "s"(ldst) : "memory");
}
template <int N> DI void wait_vm() { asm volatile("s_waitcnt vmcnt(%0)" ::"n"(N) : "memory"); }
DI void wait_lgkm0() { __builtin_amdgcn_s_waitcnt(0xC07F); }
DI void bar() { __builtin_amdgcn_s_barrier(); }
DI u32x4 bload(rsrc_t r, u32 voff, u32 soff) { return __builtin_amdgcn_raw_buffer_load_b128(r, voff, soff, 16); }
DI void bstore16(u32x4 v, rsrc_t r, u32 voff) { __builtin_amdgcn_raw_buffer_store_b128(v, r, voff, 0, 16); }
DI void bstore8(u32x2 v, rsrc_t r, u32 voff) { __builtin_amdgcn_raw_buffer_store_b64(v, r, voff, 0, 16); }
constexpr u32 SENT = 0xffffffffu;
DI u32 nosent(u32 v) { return min(v, SENT - 1u); }
DI u32x4 nosent4(u32x4 v) { return u32x4{nosent(v[0]), nosent(v[1]), nosent(v[2]), nosent(v[3])}; }
DI u32 max4(u32 m, u32x4 v) { return max(max(m, max(v[0], v[1])), max(v[2], v[3])); }
constexpr float LOG2E = 1.4426950408889634f;
template <int SLEEP = 4> DI void wait_flags(const char* ws, u32 off, bool on, u32 tag) {
  GAS u32* p = (GAS u32*)(ws + off);
#pragma unroll 1
  for (int spin = 0; spin < SPIN_CAP; ++spin) {
    const u32 v = on ? __hip_atomic_load(p, RLX_AGENT) : tag;
    if (__all(v == tag)) break;
    __builtin_amdgcn_s_sleep(SLEEP);
  }
  __builtin_amdgcn_fence(__ATOMIC_ACQUIRE, "wavefront");
  asm volatile("" ::: "memory");
}
template <int SLEEP> DI void wait_data(rsrc_t rs, u32 off) {
#pragma unroll 1
  for (int spin = 0; spin < SPIN_CAP; ++spin) {
    if (!__any(__builtin_amdgcn_raw_buffer_load_b32(rs, off, 0, 16) == SENT)) break;
    __builtin_amdgcn_s_sleep(SLEEP);
    asm volatile("" ::: "memory");
  }
  asm volatile("" ::: "memory");
}
struct Poll { u32 v[2]; };
DI void poll_flags(Poll& p, const char* ws, u32 off, u32 tag) {
  u32 cnt = SPIN_CAP;
#define POLL_STEP(LD, CK) "s_sleep 4\n\tglobal_load_dword %" #LD ", %3, %4 sc1\n\ts_waitcnt vmcnt(1)\n\tv_cmp_eq_u32 vcc, %5, %" #CK "\n\ts_nop 0\n\ts_cmp_eq_u64 vcc, -1\n\ts_cbranch_scc1 .Lpd%=\n\t"
  asm volatile("global_load_dword %0, %3, %4 sc1\n"
               ".Lpl%=:\n\t" POLL_STEP(1, 0) POLL_STEP(0, 1)
               "s_sub_u32 %2, %2, 1\n\ts_cmp_lg_u32 %2, 0\n\ts_cbranch_scc1 .Lpl%=\n"
               ".Lpd%=:"
               : "=&v"(p.v[0]), "=&v"(p.v[1]), "+s"(cnt) : "v"(off), "s"(ws), "s"(tag) : "vcc", "scc", "memory");
#undef POLL_STEP
}
DI void poll_done(Poll& p) { asm volatile("" ::"v"(p.v[0]), "v"(p.v[1])); }
template <int REP> DI void raise_flag(char* ws, u32 off, u32 tag) {
  asm volatile("s_mov_b64 exec, %3\n\tglobal_store_dword %0, %1, %2 sc1\n\ts_mov_b64 exec, -1" ::"v"(off), "v"(tag), "s"(ws), "s"((1ull << REP) - 1) : "memory");
}
template <int REP = 1, int STRIDE = 0, int KEEP = 0> DI void publish(char* ws, u32 off, u32 tag) {
  wait_vm<KEEP>();
  wait_lgkm0(); bar();
  asm volatile("" ::: "memory");
  if (__builtin_amdgcn_readfirstlane(threadIdx.x) == 0) raise_flag<REP>(ws, off + threadIdx.x * STRIDE, tag);
}

struct Sched { int op[96]; int first[20]; };
constexpr Sched make_sched(int n16) {
  Sched r{}; int n = 0;
  for (int k = 0; k < 4; ++k) r.op[n++] = 100 + k;
  r.op[n++] = 0; r.op[n++] = 1; r.op[n++] = 2;
  for (int k = 0; k < 4; ++k) r.op[n++] = 104 + k;
  r.op[n++] = 3; r.op[n++] = 4;
  for (int s = 0; s < 18; ++s) {
    r.first[s + 1] = n;
    const int it = s / 2;
    if (s == 11) { r.op[n++] = 1000; r.op[n++] = 1000; }
    constexpr int FILL[15] = {5, 108, 109, 6, 7, 110, 111, 8, 9, 112, 113, 10, 11, 114, 115};
    if (s < 3) { for (int k = 0; k < 5; ++k) r.op[n++] = FILL[5 * s + k]; }
    else if (s <= 14) {
      if ((s & 1) == 0) { r.op[n++] = 7 + 3 * it; r.op[n++] = 8 + 3 * it; r.op[n++] = 100 + 4 * (it + 2) + 2; r.op[n++] = 100 + 4 * (it + 2) + 3; }
      else { r.op[n++] = 9 + 3 * it; r.op[n++] = 100 + 4 * (it + 3); r.op[n++] = 100 + 4 * (it + 3) + 1; }
    } else for (int k = 0; k < (s == 15 ? n16 / 100 : s == 16 ? n16 / 10 % 10 : n16 % 10); ++k) r.op[n++] = 1000;
  }
  r.first[19] = n;
  return r;
}
constexpr int vm_need(int n16, int t, int b) {
  const Sched sch = make_sched(n16);
  const int issued = b < 0 ? 7 : sch.first[b + 1];
  int last = -1;
  for (int k = 0; k < issued; ++k) {
    const int op = sch.op[k];
    if (op < 100 ? (8 * op <= 12 * t + 11) : (op < 1000 && (op - 100) / 4 <= t / 2)) last = k;
  }
  return issued - 1 - last;
}

struct Frag { u32x4 x[2][2]; u32x4 w[3][2]; };

DI int p2_start(int i) { return i * 13 + min(i, 2); }
DI int p2_blkstep(int blk) { const int i = blk < 20 ? blk / 5 : (blk - 20) >> 1; return p2_start(i) + (blk < 20 ? blk - 5 * i : 5 + (blk & 1)); }
DI u32 p2_voff(int p5, int l) { const int n = p5 * 64 + l, r = n / 10, c = n - 10 * r; return (u32)((r & 19) | ((r & 4) << 1) | ((r & 8) >> 1)) * 1280u + c * 16; }
DI void p2_piece(const char* wdc, int piece, int l) {
  const int blk = piece / 5;
  dma16(wdc + (size_t)p2_blkstep(blk) * 40960, p2_voff(piece - 5 * blk, l), piece * 1024);
}

template <int TYPE> DI f32x16 p1_mfma(u32x4 w, u32x4 x, f32x16 c) { return TYPE == 2 ? mfma32(x, w, c) : mfma32(w, x, c); }

template <int TYPE, class F> DI void p1_step(f32x16 (&acc)[3][2], const Frag& cur, bool third, F&& fill) {
#pragma unroll
  for (int i = 0; i < 8; ++i) {
    const int b = (i >> 1) & 1, k = i >> 2, t = i & 1;
    acc[b][t] = p1_mfma<TYPE>(cur.w[b][k], cur.x[t][k], acc[b][t]);
    fill(i);
    SB0;
  }
  if (TYPE != 1 || third) {
#pragma unroll
    for (int i = 8; i < 12; ++i) {
      const int k = (i >> 1) & 1, t = i & 1;
      if (TYPE == 2 && t == 1) continue;
      acc[2][t] = p1_mfma<TYPE>(cur.w[2][k], cur.x[t][k], acc[2][t]);
      SB0;
    }
  }
}
template <int I, int N, class F> DI void static_for(F&& f) { if constexpr (I < N) { f(std::integral_constant<int, I>{}); static_for<I + 1, N>(f); } }
constexpr int xslot(int i) { return i % 3 == 0 ? XB0 : (i % 3 == 1 ? XB1 : XB2); }

DI float exp2_le1(float t) { return __builtin_amdgcn_fmed3f(__builtin_amdgcn_exp2f(t), 0.f, 1.f); }
DI float phi_f(float t) { return fmaxf(t, 1e-6f) + exp2_le1(t * LOG2E); }

struct P1Src { const char* xg; const char* wb[3]; u32 xvo, wvo[3]; int k0; };
DI void phase1_start(P1Src& p, int rt, int ct, const u16* x, const u16* wqkv, const u16* wg, const u16* wu) {
  const int l = threadIdx.x & 63, w = __builtin_amdgcn_readfirstlane(threadIdx.x >> 6);
  p.k0 = (0x96418530u >> (4 * rt)) & 15;
  p.xg = (const char*)x + (size_t)rt * 256 * 1280;
  p.xvo = (u32)(8 * w + (l >> 3)) * 1280 + (((l & 7) ^ ((4 * (w & 1) + (l >> 4)) & 7)) * 16);
#pragma unroll
  for (int xi = 0; xi < 4; ++xi) dma16(p.xg + (xi * 64 * 1280 + p.k0 * 128), p.xvo, XB0 + xi * 8192 + w * 1024);
  const bool gu = ct < 18, kv = ct < 26;
  const u32 stride = gu ? 3456 : 3840, lim0 = (gu || !kv) ? 12 : 10, lim1 = gu ? 12 : kv ? 10 : 8;
  const char* b0 = (gu ? (const char*)wg : (const char*)wqkv) + (gu ? 192 * ct : kv ? 160 * ct - 1600 : 320 * min(ct, 29) - 8320);
  const char* b1 = gu ? (const char*)wu + 192 * ct : b0 + (kv ? 1280 : 192);
#pragma unroll
  for (int i = 0; i < 3; ++i) {
    const int pp = 8 * i + w, st = pp >= 12, pi = pp - 12 * st, part = pi >= 6;
    const int m = (pi - 6 * part) * 64 + l, row = m / 12, cp = m - 12 * row;
    const u32 ch = part ? cp ^ 2 : cp;
    p.wb[i] = part ? b1 : b0;
    p.wvo[i] = (u32)(st * 32 + row) * stride + (ch >= (part ? lim1 : lim0) ? ch - 4 : ch) * 16;
    dma16(p.wb[i] + (size_t)((u32)p.k0 * 64u * stride), p.wvo[i], L_W + (8 * i + w) * 1024);
  }
}

template <int TYPE>
DI void phase1(const P1Src& src, int rt, int ct, int j, int c2, u32 tag, const u16* wd, char* ws) {
  const int tid = threadIdx.x, l = tid & 63, w = __builtin_amdgcn_readfirstlane(tid >> 6);
  const int tg = w & 3, cgp = w >> 2, h = l >> 5, n = l & 31;
  auto dma_x = [&](int kk, int xi, u32 slot) { dma16(src.xg + (xi * 64 * 1280 + kk * 128), src.xvo, slot + xi * 8192 + w * 1024); };
  constexpr u32 WPER = 64 * (TYPE == 0 ? 3456 : 3840);
  const u32 wl0 = L_W + w * 1024, wl1 = w >= 4 ? wl0 - 61440 : wl0;
  auto dma_w = [&](int kk, int grp) { const int pos = (8 * grp) % 60; dma16(src.wb[grp % 3] + (size_t)((u32)kk * WPER), src.wvo[grp % 3], (pos == 56 ? wl1 : wl0) + pos * 1024); };
  auto rot = [&](int kk) { return kk + 1 >= 10 ? kk - 9 : kk + 1; };
  const int k1 = rot(src.k0), k2 = rot(k1);

  auto bphys = [&](int b) { int v = b + tg; v = v >= 3 ? v - 3 : v; return v >= 3 ? v - 3 : v; };
  u32 xa[2][4], wa[3][2];
  {
    const int ksw = TYPE == 2 ? (tg & 1) : 0;
#pragma unroll
    for (int t = 0; t < 2; ++t) {
      const int row = 64 * tg + 32 * (TYPE == 2 ? t ^ cgp : t) + n;
#pragma unroll
      for (int k = 0; k < 4; ++k) xa[t][k] = row * 128 + 16 * ((2 * (k ^ ksw) + h) ^ ((row >> 1) & 7));
    }
    const int blk = (l >> 4) & 1, q = (l & 15) >> 2, p = l & 3, rowb = L_W + (8 * h + q) * 192;
    if (TYPE == 2) {
      const int a = cgp ? 3 : 0;
      const u32 b0 = rowb + (a + blk) * 32 + 8 * p, b1 = 6144 + rowb + ((a + blk) ^ 1) * 32 + 8 * p;
      const u32 b2 = rowb + (p >> 1) * 6144 + ((4 + (cgp ^ blk)) ^ (p & 2)) * 16 + 8 * (p & 1);
#pragma unroll
      for (int k = 0; k < 2; ++k) { const u32 o = (k ^ ksw) * 3072; wa[0][k] = b0 + o; wa[1][k] = b1 + o; wa[2][k] = b2 + o; }
    } else {
#pragma unroll
      for (int b = 0; b < 3; ++b) {
        const u32 in32 = 16 * (p & 1) + 8 * (p >> 1);
        if (TYPE == 0) wa[b][0] = rowb + blk * 6144 + ((3 * cgp + bphys(b)) ^ blk) * 32 + in32;
        else wa[b][0] = rowb + cgp * 6144 + ((2 * (cgp ? (b == 2 ? 2 : b ^ (tg & 1)) : bphys(b)) + blk) ^ cgp) * 32 + in32;
        wa[b][1] = 0;
      }
    }
  }
  auto rd_w = [&](int b, int k, int off) { return tr_frag((TYPE == 2 ? wa[b][k] : wa[b][0] + k * 3072) + off, 768); };
  auto rd_x = [&](int t, int k4, int off) {
    return TYPE == 2 ? lds_ld<u32x4>(xa[t][k4] + off) : lds_ld<u32x4>(xa[0][k4] + off + t * 4096);
  };
  auto fill_rd = [&](Frag& f, int i, int st) {
    const int wo_ = (st % 5) * 12288, xo = xslot(st / 2), k4 = 2 * (st & 1);
    if (i == 0) { f.w[0][0] = rd_w(0, 0, wo_); f.x[0][0] = rd_x(0, k4, xo); }
    if (i == 1) { f.x[1][0] = rd_x(1, k4, xo); f.w[1][0] = rd_w(1, 0, wo_); }
    if (i == 2) { f.w[0][1] = rd_w(0, 1, wo_); f.x[0][1] = rd_x(0, k4 + 1, xo); }
    if (i == 3) { f.x[1][1] = rd_x(1, k4 + 1, xo); f.w[1][1] = rd_w(1, 1, wo_); }
    if (i == 4) { f.w[2][0] = rd_w(2, 0, wo_); f.w[2][1] = rd_w(2, 1, wo_); }
  };
  f32x16 acc[3][2];
#pragma unroll
  for (int b = 0; b < 3; ++b)
#pragma unroll
    for (int t = 0; t < 2; ++t)
#pragma unroll
      for (int i = 0; i < 16; ++i) acc[b][t][i] = 0.f;
  auto colid = [&](int b) {
    if (TYPE == 2) return b == 2 ? ((n >> 3) & 1) * 96 + 32 + 8 * (cgp ^ (n >> 4)) + (n & 7) : b * 96 + (cgp ? 48 : 0) + n;
    const int p = (n & 15) >> 2, c16 = 8 * (p & 1) + 4 * (p >> 1) + (n & 3);
    if (TYPE == 0) return (n >> 4) * 96 + (3 * cgp + bphys(b)) * 16 + c16;
    return cgp * 96 + (cgp ? (b == 2 ? 2 : b ^ (tg & 1)) : bphys(b)) * 32 + (n & 16) + c16;
  };
  u32 goff[2], pvo[5], coff;
  {
    const int hi = (TYPE == 1 && tid >= 128) ? tid - 128 : tid;
    goff[0] = CSG_OFF + ct * 12288 + (tid & 7) * 1536 + (tid >> 3) * 16;
    goff[1] = w < 4 ? CSG_OFF + ct * 12288 + (tid & 7) * 1536 + 1024 + (hi >> 3) * 16 : STG_OFF + 2048 * rt + (tid & 127) * 16;
    coff = l < 32 ? CSG_OFF + (ct * 1536 + rt * 192 + colid(0)) * 8 : OOB;
#pragma unroll
    for (int k = 0; k < 5; ++k) { pvo[k] = p2_voff((w + k) % 5, l); asm volatile("" : "+v"(pvo[k])); }
    asm volatile("" : "+v"(goff[0]), "+v"(goff[1]), "+v"(coff));
  }
#pragma unroll
  for (int xi = 0; xi < 4; ++xi) dma_x(k1, xi, XB1);
  dma_w(k1, 3); dma_w(k1, 4);
  Frag fa, fb;
  STAMP(1);
  wait_vm<8>();
  bar(); SB0;
#pragma unroll
  for (int i = 0; i < 5; ++i) fill_rd(fa, i, 0);
  SB0;
  SB0;
  STAMP(2);
  const bool third = cgp == 0;
  float cs[3] = {0.f, 0.f, 0.f};
  auto colsum = [&](const Frag& f, int i) {
    constexpr u32 ones = 0x3f803f80u;
    if (TYPE == 2) {
      if (i < 6) { const int b = i >> 1; cs[b] = dot2(f.w[b][0][2 * (i & 1)], ones, cs[b]); cs[b] = dot2(f.w[b][0][2 * (i & 1) + 1], ones, cs[b]); asm volatile("" : "+v"(cs[b])); }
    } else { cs[0] = dot2(f.w[0][i >> 2][i & 3], ones, cs[0]); asm volatile("" : "+v"(cs[0])); }
  };
  const rsrc_t rs = __builtin_amdgcn_make_buffer_rsrc(ws, 0, WS_END, 0x00020000);
  int ka = k2, kb = rot(k2);
  const char* wdc = (const char*)(wd + 80 * c2);
  u32x4 gr[2];
  f32x2 gs[2];
  auto gload = [&]() {
#pragma unroll
    for (int k = 0; k < 2; ++k) asm volatile("global_load_dwordx4 %0, %1, %2 sc1" : "=v"(gr[k]) : "v"(goff[k]), "s"(ws) : "memory");
  };
  auto gcheck = [&]() {
    asm volatile("" : "+v"(gr[0]), "+v"(gr[1]));
    return __all(gr[0][1] == tag && gr[0][3] == tag && gr[1][1] == tag && gr[1][3] == tag);
  };
  auto pf2 = [&](int base) {
    constexpr int NP = TYPE == 2 ? 116 : 140;
    const bool clip = base + 8 > NP;
    const int piece = base + (clip ? w & 3 : w), blk = piece / 5;
    u32 vo = pvo[base % 5];
    if (clip) { u32 v1 = pvo[(base + 1) % 5]; asm volatile("" : "+v"(v1)); vo = w < 4 ? vo : v1; }
    const int lo = blk / 5, hi = (blk - 20) >> 1;
    const int st = base + 8 <= 100 || clip && base < 100 ? p2_start(lo) + blk - 5 * lo : base >= 100 ? p2_start(hi) + 5 + (blk & 1) : (w < 4 ? 45 : 5);
    dma16(wdc + (u32)st * 40960u, vo, piece * 1024);
  };
  constexpr int NX = TYPE == 2 ? 432 : 332;
  auto bp_of = [&](int b) { return TYPE == 0 ? bphys(b) : (cgp ? (b == 2 ? 2 : b ^ (tg & 1)) : bphys(b)); };
  auto oaddr = [&](int k) {
    const u32 dst = ACT_OFF + (u32)(16 * rt + 4 * tg + (n >> 4)) * (NSTEP * 1024) + (n & 15) * 16;
    if (TYPE == 0) { const int id = 12 * j + 6 * cgp + 2 * bp_of(k >> 1) + h; return dst + (k & 1) * 2 * NSTEP * 1024 + (id >> 2) * 1024 + (id & 3) * 256; }
    const int ch = 4 * (3 * cgp + bp_of(k >> 2)) + 2 * (k & 1) + h, g4 = ch / 5;
    return (k >= 8 && cgp) ? OOB : dst + ((k >> 1) & 1) * 2 * NSTEP * 1024 + (54 + 5 * j + ch - 5 * g4) * 1024 + g4 * 256;
  };
  static_for<0, 18>([&](auto SC) {
    constexpr int S = decltype(SC)::value, IT = S / 2;
    Frag& cur = (S & 1) ? fb : fa; Frag& nxt = (S & 1) ? fa : fb;
    wait_lgkm0();
    if (S > 0) { wait_vm<vm_need(NX, S + 1, S)>(); bar(); } else bar();
    SSTAMP(16 + S); SB0;
    p1_step<TYPE>(acc, cur, third, [&](int i) {
      if (!ABL_NOREAD) fill_rd(nxt, i, S + 1);
      if (!ABL_NODMA) {
        if (S == 0) { if (i == 3) dma_w(k1, 5); if (i == 4 || i == 5) dma_x(k2, i - 4, XB2); if (i >= 6) dma_w(k2, i); }
        else if (S == 1) { if (i == 3 || i == 4) dma_x(k2, i - 1, XB2); if (i == 5) dma_w(k2, 8); if (i == 6) dma_w(kb, 9); if (i == 7) { dma_x(kb, 0, XB0); ka = kb; kb = rot(kb); } }
        else if (S == 2) { if (i == 3) dma_x(ka, 1, XB0); if (i == 4 || i == 5) dma_w(ka, 6 + i); if (i >= 6) dma_x(ka, i - 4, XB0); }
        else if (S > 14) { if (i >= (S == 17 ? 6 : 5)) pf2(S == 15 ? 8 * (i - 5) : S == 16 ? 96 + 8 * (i - 5) : 8 * (i - 2)); }
        else if ((S & 1) == 0) { if (i == 4 || i == 5) dma_w(ka, 3 + 3 * IT + i); if (i >= 6) dma_x(ka, i - 4, xslot(IT + 2)); }
        else { if (i == 5) dma_w(kb, 9 + 3 * IT); if (i >= 6) dma_x(kb, i - 6, xslot(IT)); if (i == 7) { ka = kb; kb = rot(kb); } }
      }
      if (S < 2) colsum(cur, i);
      if ((S == 2 || S == 3) && i == 5 && (rt & 5) == 4) {
#pragma unroll
        for (int e = 0; e < 8; ++e) colsum(cur, e);
      }
      if (TYPE != 2) {
        if (S == 4 && i == 1) { cs[0] = xsum32(cs[0]); asm volatile("" : "+v"(cs[0])); }
        if (S == 4 && i == 3) bstore8(u32x2{__float_as_uint(cs[0]), tag}, rs, coff);
      } else {
        if (S == 4 && i >= 1 && i < 4) { const float v = xsum32(cs[i - 1]); lds_st<float>(tg < 2 ? L_SP + (tg * 192 + colid(i - 1)) * 4 : L_SP + 2048 + l * 4, v); }
        if (S == 5 && i == 1) { cs[0] = lds_ld<float>(L_SP + min(tid, 191) * 4); cs[1] = lds_ld<float>(L_SP + 768 + min(tid, 191) * 4); }
        if (S == 5 && i == 3) bstore8(u32x2{__float_as_uint(cs[0] + cs[1]), tag}, rs, tid < 192 ? CSG_OFF + (ct * 1536 + rt * 192 + tid) * 8 : OOB);
      }
      if (S == 11 && i == 0) gload();
      if (TYPE == 2 && S == 15 && i == 1 && tid < 480) bstore16(u32x4{SENT, SENT, SENT, SENT}, rs, KVR_OFF + (u32)((2 * j + (tid >= 240)) * 8 + rt) * 3840 + (tid >= 240 ? tid - 240 : tid) * 16);
      if (S == 16) {
        if (i == 1) {
          wait_vm<TYPE == 2 ? 18 : 17>();
          if (__builtin_expect(!gcheck(), 0)) {
#pragma unroll 1
            for (int spin = 0; spin < SPIN_CAP; ++spin) { __builtin_amdgcn_s_sleep(2); gload(); wait_vm<0>(); if (gcheck()) break; }
          }
        }
        if (i >= 2 && i < 6) { gs[(i - 2) >> 1][i & 1] = oct_sum(__uint_as_float(gr[(i - 2) >> 1][2 * (i & 1)])); asm volatile("" : "+v"(gs[(i - 2) >> 1][i & 1])); }
        if (i == 6) lds_st<f32x2>(L_SP + 2048 + (tid >> 3) * 8, gs[0]);
      }
      if (S == 17) {
        if (i == 0) lds_st<f32x2>(w < 4 ? L_SP + 2560 + (tid >> 3) * 8 : L_SP + (tid & 127) * 8, w < 4 ? gs[1] : f32x2{bf_lo(gr[1][0]), bf_lo(gr[1][2])});
        if (i == 2) lds_st<f32x2>((w < 4 ? L_SP + 3072 : L_SP + 1024) + (tid & 63) * 8 + (w & 1) * (w < 4 ? 0 : 512), f32x2{__builtin_amdgcn_rcpf(bf_hi(gr[1][0])), __builtin_amdgcn_rcpf(bf_hi(gr[1][2]))});
      }
    });
  });
  auto mm = [&](int b, int t, int q) { const Frag& f = q < 2 ? fa : fb; acc[b][t] = p1_mfma<TYPE>(f.w[b][q & 1], f.x[t][q & 1], acc[b][t]); };
  u32x4 fm[2], fc[3];
  auto mk_fm = [&](int t) { const u32 m = h ? 0u : (__float_as_uint(lds_ld<float>(L_SP + (64 * tg + 32 * (TYPE == 2 ? t ^ cgp : t) + n) * 4)) >> 16) ^ 0x8000u; fm[t] = u32x4{m * 0x10001u, m, 0u, 0u}; };
  auto mk_fc = [&](int b) {
    const float c = h ? 0.f : lds_ld<float>(L_SP + 2048 + colid(b) * 4), c0 = round_bf(c), c1 = round_bf(c - c0);
    fc[b] = u32x4{pk_bf16(c0, c1), pk_bf16(c - c0 - c1, 0.f), 0u, 0u};
  };
  auto mc = [&](int b, int t) { acc[b][t] = p1_mfma<TYPE>(fc[b], fm[t], acc[b][t]); };
  wait_lgkm0(); wait_vm<vm_need(NX, 19, 18)>(); bar(); SSTAMP(34); SB0;
  if constexpr (TYPE != 2) {
    float rstd[2], ce[2], sd[2];
    u32 ob[8];
    auto epi = [&](int e) {
      const int b = e >> 4, t = (e >> 3) & 1, i = e & 7;
      if (TYPE == 0) {
        const float ex = __builtin_amdgcn_exp2f(acc[b][t][i] * ce[t]);
        const float hv = acc[b][t][8 + i] * __builtin_amdgcn_rcpf(ex * sd[t] + sd[t]);
        if (i & 1) ob[i >> 1] = pk_bf16(__uint_as_float(ob[i >> 1]), hv); else ob[i >> 1] = __float_as_uint(hv);
        asm volatile("" : "+v"(ob[i >> 1]));
        if (i == 7) bstore16(u32x4{ob[0], ob[1], ob[2], ob[3]}, rs, oaddr(2 * b + t));
      } else {
        const float u0 = acc[b][t][2 * i], u1 = acc[b][t][2 * i + 1];
        ob[i] = pk_bf16(fmaxf(u0, sd[t]) * rstd[t] + exp2_le1(u0 * ce[t]), fmaxf(u1, sd[t]) * rstd[t] + exp2_le1(u1 * ce[t]));
        asm volatile("" : "+v"(ob[i]));
        if ((i & 3) == 3) bstore16(u32x4{ob[i - 3], ob[i - 2], ob[i - 1], ob[i]}, rs, oaddr(4 * b + 2 * t + (i >> 2)));
      }
    };
#pragma unroll
    for (int i = 0; i < 6; ++i) {
      mm(0, i < 4 ? i >> 1 : 0, i < 4 ? i & 1 : i - 2);
      if (i < 5) fill_rd(fb, i == 1 ? 2 : i == 2 ? 1 : i, 19);
      if (i == 0) {
#pragma unroll
        for (int t = 0; t < 2; ++t) rstd[t] = lds_ld<float>(L_SP + 1024 + (64 * tg + 32 * t + n) * 4);
      }
      if (i == 1) { mk_fm(0); mk_fm(1); mk_fc(0); }
      if (i == 2) mk_fc(1);
      if (i == 3) { mc(0, 0); mk_fc(2); }
      if (i == 3) {
#pragma unroll
        for (int t = 0; t < 2; ++t) { ce[t] = (TYPE == 0 ? -LOG2E : LOG2E) * rstd[t]; sd[t] = (TYPE == 0 ? 1.f : 1e-6f) * __builtin_amdgcn_rcpf(rstd[t]); }
      }
      if (i >= 3) pf2(96 + 8 * i);
      if (i == 5) { mc(0, 1); if (tid < 2) lds_st<u32x4>(L_CNT + tid * 16, u32x4{0u, 0u, 0u, 0u}); }
      SB0;
    }
    wait_lgkm0(); bar(); SB0;
    if (TYPE == 0 && cgp) __builtin_amdgcn_s_setprio(1);
#pragma unroll
    for (int i = 0; i < 10; ++i) {
      mm(i < 2 ? 0 : 1, i < 2 ? 1 : (i - 2) >> 2, i < 2 ? 2 + i : (i - 2) & 3);
      epi(2 * i); epi(2 * i + 1);
      if (i < 7) pf2(i < 4 ? 64 + 8 * i : i == 4 ? 24 : 8 * (i + 1));
      if (i == 0 || i == 1) mc(1, i);
      if (i == 6 || i == 7) mc(2, i - 6);
      SB0;
    }
    __builtin_amdgcn_s_setprio(0);
    if (TYPE == 0 || third) {
#pragma unroll
      for (int i = 0; i < 8; ++i) { mm(2, i >> 2, i & 3); epi(20 + 2 * i); epi(21 + 2 * i); SB0; }
      STAMP(3);
#pragma unroll
      for (int e = 36; e < 48; ++e) epi(e);
    } else {
      STAMP(3);
#pragma unroll
      for (int e = 20; e < 32; ++e) epi(e);
    }
    STAMP(4);
    publish<8, 2048>(ws, FH_OFF + rt * 256 + (TYPE == 0 ? j : 18 + j) * 4, tag);
    STAMP(5);
  } else {
    u32x4 xp[3][2][2];
    f32x4 sr[2];
    float zs[2] = {0.f, 0.f};
    constexpr int EB[5] = {2, 0, 0, 1, 1}, ET[6] = {0, 0, 1, 0, 1, 0};
    auto ld_st = [&](int p) {
      const u32 a = L_SP + (64 * tg + 32 * (ET[p >> 2] ^ cgp) + 8 * (p & 3) + 4 * h) * 4;
      sr[p & 1] = lds_ld<f32x4>(a + 1024);
    };
    auto epi2 = [&](int p) {
      const int b = EB[p >> 2], t = ET[p >> 2], r0 = 4 * (p & 3);
      const f32x4 r = sr[p & 1];
      float v[4];
#pragma unroll
      for (int e = 0; e < 4; ++e) {
        v[e] = acc[b][t][r0 + e] * r[e];
        if (b == 0) v[e] = phi_f(v[e]);
        if (b == 2) { const float pv = phi_f(v[e]); v[e] = (n & 8) ? v[e] : pv; }
      }
      if (p + 2 < 20) ld_st(p + 2);
      if (b != 1) { zs[b >> 1] += (v[0] + v[1]) + (v[2] + v[3]); asm volatile("" : "+v"(zs[b >> 1])); }
      u32 o0 = pk_bf16(v[0], v[1]), o1 = pk_bf16(v[2], v[3]);
      asm volatile("" : "+v"(o0), "+v"(o1));
      xp[b][t][(p >> 1) & 1][2 * (p & 1)] = o0; xp[b][t][(p >> 1) & 1][2 * (p & 1) + 1] = o1;
    };
#pragma unroll
    for (int i = 0; i < 6; ++i) {
      mm(i < 2 || i >= 4 ? 2 : 0, 0, i < 4 ? i & 1 : i - 2);
      if (i < 5) fill_rd(fb, i == 0 ? 4 : i == 1 ? 0 : i == 2 ? 2 : i == 3 ? 1 : 3, 19);
      if (i == 1) { mk_fm(0); mk_fm(1); mk_fc(2); }
      if (i == 2) mk_fc(0);
      if (i == 3) { mc(2, 0); mk_fc(1); }
      if (i == 5) mc(0, 0);
      if (i >= 4) ld_st(i - 4);
      SB0;
    }
#pragma unroll
    for (int i = 0; i < 6; ++i) { mm(0, i >= 2, i < 2 ? 2 + i : i - 2); epi2(i); if (i < 3) mc(i ? 1 : 0, i == 1 ? 0 : 1); SB0; }
    if (n >= 16) {
#pragma unroll
      for (int k = 0; k < 2; ++k) lds_st<u32x4>(L_KX + ((w * 2 + k) * 32 + (n - 16) + 16 * h) * 16, xp[2][0][k]);
    }
    publish<1, 0, 5>(ws, FK_OFF + j * 256 + rt * 4, tag); SB0;
#pragma unroll
    for (int k = 0; k < 2; ++k) xp[2][1][k] = lds_ld<u32x4>(L_KX + (((w ^ 4) * 2 + k) * 32 + (n & 15) + 16 * h) * 16);
    f32x16 z16;
#pragma unroll
    for (int i = 0; i < 16; ++i) z16[i] = 0.f;
    f32x16 pmk = z16, pmm = z16, pvk = z16, pvm = z16;
    constexpr u32 IMG = L_KX + 8192, DUMP = IMG + 8 * 3840;
    const int lo8 = 8 * cgp, mix = 32 - 32 * cgp;
    u32 avk[4], amk, avm[2], amm;
    auto img_at = [&](int kc, int vc, bool on) { return (on && (kc >= 20) == (vc >= 20)) ? IMG + w * 3840 + kc * 96 + (vc >= 20 ? vc - 20 : vc) * 4 : DUMP + l * 16; };
#pragma unroll
    for (int i = 0; i < 8; ++i) {
      mm(1, i >> 2, i & 3); epi2(6 + i);
      if (i < 7) pf2(i < 4 ? 64 + 8 * i : i == 4 ? 24 : 8 * (i + 1));
      if (i >= 2 && i < 6) pmm = mfma32(xp[2][(i - 2) >> 1][i & 1], xp[2][(i - 2) >> 1][i & 1], pmm);
      if (i == 6) { amm = img_at(mix + n, mix + 4 * h, n < 8); amk = img_at(lo8 + n, mix + 4 * h, true); asm volatile("" : "+v"(amm), "+v"(amk)); }
      if (i == 7) {
#pragma unroll
        for (int q = 0; q < 2; ++q) { avm[q] = img_at(mix + n, lo8 + 16 * (1 - cgp) + 8 * q + 4 * h, n < 8); asm volatile("" : "+v"(avm[q])); }
      }
      SB0;
    }
    STAMP(3);
#pragma unroll
    for (int i = 0; i < 6; ++i) {
      if (i < 4) pmk = mfma32(xp[2][i >> 1][i & 1], xp[0][i >> 1][i & 1], pmk);
      if (i >= 2 && i < 4) { pvk = mfma32(xp[1][0][i & 1], xp[0][0][i & 1], pvk); pvm = mfma32(xp[1][0][i & 1], xp[2][0][i & 1], pvm); }
      epi2(14 + i);
      if (i < 4) { avk[i] = img_at(lo8 + n, lo8 + 8 * i + 4 * h, true); asm volatile("" : "+v"(avk[i])); }
      if (i == 1) lds_st<f32x4>(amm, f32x4{pmm[4], pmm[5], pmm[6], pmm[7]});
      if (i == 4) {
        const float z0 = xsum32(zs[0]), z1 = xsum32(zs[1]);
        lds_st<float>(h == 0 ? IMG + w * 3840 + (lo8 + n) * 96 + 80 : (n & 8) ? DUMP + l * 16 : (n < 8 ? IMG + w * 3840 + (mix + n) * 96 + 80 : IMG + (w ^ 4) * 3840 + (32 - mix + n - 16) * 96 + 84), h == 0 ? z0 : z1);
      }
      if (i == 5) lds_st<f32x4>(amk, f32x4{pmk[4], pmk[5], pmk[6], pmk[7]});
      SB0;
    }
#pragma unroll
    for (int k = 0; k < 2; ++k) { pvk = mfma32(xp[1][1][k], xp[0][1][k], pvk); pvm = mfma32(xp[1][1][k], xp[2][1][k], pvm); }
    if (tid < 2) lds_st<u32x4>(L_CNT + tid * 16, u32x4{0u, 0u, 0u, 0u});
#pragma unroll
    for (int q = 0; q < 4; ++q) lds_st<f32x4>(avk[q], f32x4{pvk[4 * q], pvk[4 * q + 1], pvk[4 * q + 2], pvk[4 * q + 3]});
#pragma unroll
    for (int q = 0; q < 2; ++q) lds_st<f32x4>(avm[q], cgp ? f32x4{pvm[4 * q], pvm[4 * q + 1], pvm[4 * q + 2], pvm[4 * q + 3]} : f32x4{pvm[8 + 4 * q], pvm[9 + 4 * q], pvm[10 + 4 * q], pvm[11 + 4 * q]});
    __syncthreads();
    if (tid < 480) {
      const int cg = tid >= 240, o = (tid - 240 * cg) * 16;
      f32x4 sum = lds_ld<f32x4>(IMG + cg * 15360 + o);
#pragma unroll
      for (int k = 1; k < 4; ++k) sum += lds_ld<f32x4>(IMG + cg * 15360 + k * 3840 + o);
      bstore16(nosent4(__builtin_bit_cast(u32x4, sum)), rs, KVR_OFF + (u32)((2 * j + cg) * 8 + rt) * 3840 + o);
    }
    STAMP(4);
    wait_vm<1>(); wait_lgkm0(); bar();
    STAMP(5);
  }
}

DI void lds_ticket(u32 a) {
  asm volatile("s_mov_b64 exec, 1\n\tds_add_u32 %0, %1\n\ts_mov_b64 exec, -1" ::"v"(a), "v"(1u) : "memory");
}
DI void lds_wait(u32 a, u32 want) {
#pragma unroll 1
  for (int spin = 0; spin < SPIN_CAP * 16; ++spin) {
    if (__builtin_amdgcn_readfirstlane(__hip_atomic_load((LAS u32*)(size_t)a, __ATOMIC_RELAXED, __HIP_MEMORY_SCOPE_WORKGROUP)) >= want) break;
    __builtin_amdgcn_s_sleep(2);
  }
  asm volatile("" ::: "memory");
}

DI void lds_meet(u32 a, u32 want) { asm volatile("s_waitcnt lgkmcnt(0)" ::: "memory"); lds_ticket(a); lds_wait(a, want); }

DI void mjob(int pair, u32 tag, char* ws, rsrc_t rs, const u32x4 (&WO)[5][2]) {
  int tid = threadIdx.x;
  asm volatile("" : "+v"(tid));
  const int l = tid & 63, w = __builtin_amdgcn_readfirstlane(tid >> 6);
  constexpr u32 KVL = L_MJ;
  __builtin_amdgcn_s_setprio(3);
  STAMP(5);
  if (w < 4) {
    wait_flags<16>(ws, FK_OFF + (pair >> 1) * 256 + (l & 7) * 4, l < 8, tag);
    const int t4 = min(tid, 239);
    u32x4 pa[8];
#pragma unroll 1
    for (int spin = 0; spin < SPIN_CAP; ++spin) {
      u32 mx = 0;
#pragma unroll
      for (int k = 0; k < 8; ++k) pa[k] = bload(rs, KVR_OFF + (u32)pair * 30720 + t4 * 16, k * 3840);
#pragma unroll
      for (int k = 0; k < 8; ++k) mx = max4(mx, pa[k]);
      if (!__any(mx == SENT)) break;
      __builtin_amdgcn_s_sleep(4);
      asm volatile("" ::: "memory");
    }
    f32x4 sum = __builtin_bit_cast(f32x4, pa[0]);
#pragma unroll
    for (int k = 1; k < 8; ++k) sum += __builtin_bit_cast(f32x4, pa[k]);
    const bool zc = t4 % 6 == 5;
    lds_st<u32x2>(KVL + t4 * 8, zc ? u32x2{0u, 0u} : u32x2{pk_bf16(sum[0], sum[1]), pk_bf16(sum[2], sum[3])});
    if (zc) { const float z = sum[0] + (((pair & 1) ? t4 < 48 : t4 >= 192) ? sum[1] : 0.f); lds_st<u16>(KVL + 1920 + (t4 / 6) * 2, (u16)pk_bf16(z, z)); }
  }
  wait_lgkm0(); bar();
  asm volatile("" ::: "memory");
  if (w >= 5) { const int e = tid - 320; bstore16(nosent4(lds_ld<u32x4>(KVL + 1920 + (e % 5) * 16)), rs, e < 160 ? ZB_OFF + (e / 5) * 1280 + pair * 80 + (e % 5) * 16 : OOB); }
  STAMP(6);
  const int g = l >> 4, c = l & 15;
  const u32x4 zero4 = {0u, 0u, 0u, 0u};
  const int kx = 8 * (c >> 2) + (c & 3), ky = kx + 4, kz = 32 + (c & 7), go = min(g, 2) * 16;
  const u32x4 rx = lds_ld<u32x4>(KVL + kx * 48 + go), ry = lds_ld<u32x4>(KVL + ky * 48 + go), rz = lds_ld<u32x4>(KVL + kz * 48 + go);
  const u32x4 ax0 = (kx < 20 && g < 3) ? rx : zero4, ax1 = (kx >= 20 && g < 3) ? rx : zero4, ay0 = (ky < 20 && g < 3) ? ry : zero4, ay1 = (ky >= 20 && g < 3) ? ry : zero4, az = g < 3 ? rz : zero4;
  const u32 mo = MT_OFF + (u32)(100 * w + 5 * (pair >> 2)) * 1024 + (c + 16 * (pair & 3)) * 16;
  f32x4 dx[5], dy[5], dz[5];
#pragma unroll
  for (int cb = 0; cb < 5; ++cb) { const f32x4 z4 = {0.f, 0.f, 0.f, 0.f}; dx[cb] = mfma16(ax0, WO[cb][0], z4); dy[cb] = mfma16(ay0, WO[cb][0], z4); dz[cb] = mfma16(az, WO[cb][1], z4); }
#pragma unroll
  for (int cb = 0; cb < 5; ++cb) { dx[cb] = mfma16(ax1, WO[cb][1], dx[cb]); dy[cb] = mfma16(ay1, WO[cb][1], dy[cb]); }
  SB0;
#pragma unroll
  for (int cb = 0; cb < 5; ++cb) {
    bstore16(nosent4(u32x4{pk_bf16(dx[cb][0], dx[cb][1]), pk_bf16(dx[cb][2], dx[cb][3]), pk_bf16(dy[cb][0], dy[cb][1]), pk_bf16(dy[cb][2], dy[cb][3])}), rs, mo + g * 1024 + cb * 20480);
    bstore8(u32x2{nosent(pk_bf16(dz[cb][0], dz[cb][1])), nosent(pk_bf16(dz[cb][2], dz[cb][3]))}, rs, g < 2 ? mo + 4096 + 8 * g + cb * 20480 : OOB);
  }
  __builtin_amdgcn_s_setprio(0);
  STAMP(7);
}

DI int wt_step(int i) { const int q = i < 7 ? 0 : i < 14 ? 1 : i < 20 ? 2 : 3; return i + 7 * (q + 1); }

DI void spare(int sg, int c2, u32 tag, const u16* wo, const u16* wd, char* ws, u32x4 (&WO)[5][2]) {
  int tid = threadIdx.x;
  asm volatile("" : "+v"(tid));
  const int l = tid & 63, w = __builtin_amdgcn_readfirstlane(tid >> 6);
  const int nst = sg < 10 ? 2 : 1;
  wait_vm<0>();
  bar();
  STAMP(1);
#pragma unroll 1
  for (int a = 0; a < nst; ++a)
#pragma unroll
    for (int i = 0; i < 5; ++i) dma16((const char*)wd + (size_t)wt_step(sg + 16 * a) * 40960 + (w * 5 + i) * 1024, l * 16, a * 40960 + (w * 5 + i) * 1024);
#pragma unroll 1
  for (int pc = w; pc < 50; pc += 8) dma16((const char*)(wo + (size_t)sg * 40 * 640) + pc * 1024, l * 16, 81920 + pc * 1024);
  wait_vm<0>();
  __syncthreads();
  STAMP(2);
  const rsrc_t rs = __builtin_amdgcn_make_buffer_rsrc(ws, 0, WS_END, 0x00020000);
#pragma unroll 1
  for (int a = 0; a < nst; ++a)
#pragma unroll
    for (int i = 0; i < 5; ++i) {
      const int cb = w * 5 + i;
      const u32x4 f = tr_frag(a * 40960 + (8 * (l >> 4) + ((l & 15) >> 2)) * 1280 + cb * 32 + 8 * (l & 3), 5120);
      bstore16(f, rs, WT_OFF + (u32)(cb * 54 + wt_step(sg + 16 * a)) * 1024 + l * 16);
    }
  {
    const u32 ra = 81920 + min(8 * (l >> 4) + ((l & 15) >> 2), 19) * 1280 + w * 160 + 8 * (l & 3), rb = 81920 + min(8 * (l >> 4) + 4 + ((l & 15) >> 2), 19) * 1280 + w * 160 + 8 * (l & 3);
    const u32 mo = MT_OFF + (u32)(100 * w + 5 * (sg >> 2)) * 1024 + ((l & 15) + 16 * (sg & 3)) * 16;
#pragma unroll
    for (int cb = 0; cb < 5; ++cb) {
#pragma unroll
      for (int hh = 0; hh < 2; ++hh) { const u32x2 lo = tr_read(ra + cb * 32 + hh * 25600), hi = tr_read(rb + cb * 32 + hh * 25600); WO[cb][hh] = u32x4{lo[0], lo[1], hi[0], hi[1]}; }
      bstore16(u32x4{SENT, SENT, SENT, SENT}, rs, mo + (l >> 4) * 1024 + cb * 20480);
      bstore16(u32x4{SENT, SENT, SENT, SENT}, rs, l < 16 ? mo + 4096 + cb * 20480 : OOB);
    }
    if (w >= 5) { const int e = tid - 320; bstore16(u32x4{SENT, SENT, SENT, SENT}, rs, e < 160 ? ZB_OFF + (e / 5) * 1280 + sg * 80 + (e % 5) * 16 : OOB); }
  }
  STAMP(3);
  publish<8, 256>(ws, FW_OFF + sg * 4, tag);
  STAMP(4);
#pragma unroll 1
  for (int pc = w; pc < 140; pc += 8) p2_piece((const char*)(wd + 80 * c2), pc, l);
  if (tid < 2) lds_st<u32x4>(L_CNT + tid * 16, u32x4{0u, 0u, 0u, 0u});
  if (sg == 0) {
#pragma unroll 1
    for (int k = 0; k < 4; ++k) wait_flags<8>(ws, STG_OFF + (k * 512 + tid) * 8 + 4, true, tag);
  }
  wait_vm<0>();
  __syncthreads();
  if (sg == 0 && tid == 0) __hip_atomic_store((GAS u32*)(ws + EP_OFF), tag, RLX_AGENT);
}

DI u32 p2_attblk(int u, int j) { return j < 14 ? L_ATT + (u * 14 + j) * 1024 : u * 25600 + 20480; }
constexpr int h_step(int t) { return t < 6 ? 5 + t : t < 11 ? t - 6 : t; }
constexpr bool h_lds(int t) { return t < 2 || (t >= 6 && t < 11); }
constexpr int a_first(int t) { return t < 0 ? 0 : t < 2 ? 4 + t : t < 6 ? 2 + 2 * t : 14; }
constexpr int lds_seq(int t, int cb) { return (t < 2 ? t : t - 4) * 5 + cb; }
template <int T> DI void p2_hsteps(f32x4 (&acc)[5][4], u32x4 (&A)[14][4], u32x4 (&B)[4][5], u32x4 (&bl)[3], rsrc_t rs, u32 va, const u32 (&so)[4], u32 vb, u32 wl, u32 wl2, bool extra, Poll& pl, const u16* xp, u32x2 (&xr)[5]) {
  if constexpr (T < 14) {
#pragma unroll
    for (int cb = 0; cb < 5; ++cb) {
      const u32x4 bc = h_lds(T) ? bl[lds_seq(T, cb) % 3] : B[T < 6 ? T - 2 : T - 11][cb];
      const int q = h_lds(T) ? lds_seq(T, cb) + 2 : (T == 5 && cb >= 3) ? 7 + cb : -1;
      if (q >= 0 && q < 35 && !(T == 1 && q >= 10)) bl[q % 3] = tr_frag((q < 10 ? wl2 + (q / 5) * 5120 : wl + (q / 5 - 2) * 5120) + (q % 5) * 32, 1280);
#pragma unroll
      for (int t = 0; t < 4; ++t) acc[cb][t] = mfma16(bc, A[T][t], acc[cb][t]);
      SB0;
    }
    if constexpr (T == 0) poll_done(pl);
    if constexpr (T == 9) {
#pragma unroll
      for (int cb = 0; cb < 5; ++cb) xr[cb] = *(const u32x2*)(xp + 16 * cb);
    }
    if constexpr (T == 1) lds_ticket(L_CNT + 4);
    if constexpr (T == 10) lds_ticket(L_CNT);
#pragma unroll
    for (int n = a_first(T); n < a_first(T + 1); ++n)
#pragma unroll
      for (int t = 0; t < 4; ++t) A[n][t] = bload(rs, n == 13 ? (extra ? va : OOB) : va, so[t] + h_step(n) * 1024);
    if constexpr (T >= 6 && T < 9) {
#pragma unroll
      for (int cb = 0; cb < 5; ++cb) B[T - 6][cb] = bload(rs, T == 8 ? (extra ? vb : OOB) : vb, cb * 55296 + (T + 5) * 1024);
    }
    SB0;
    p2_hsteps<T + 1>(acc, A, B, bl, rs, va, so, vb, wl, wl2, extra, pl, xp, xr);
  }
}

template <bool JOB> DI void phase2(int r, int c, int job, bool late, u32 tag, const u16* x, const u16* wd, u16* out, char* ws, const u32x4 (&WO)[5][2]) {
  int tid = threadIdx.x;
  asm volatile("" : "+v"(tid), "+s"(ws), "+s"(tag));
  const int l = tid & 63, w = __builtin_amdgcn_readfirstlane(tid >> 6);
  const rsrc_t rs = __builtin_amdgcn_make_buffer_rsrc(ws, 0, WS_END, 0x00020000);
  const int g = l >> 4, n = l & 15;
  auto orow = [&]() { int t = threadIdx.x; asm volatile("" : "+v"(t)); return (u32)((64 * r + 16 * (w & 3) + (t & 15)) * 640 + 80 * c + ((t >> 2) & 12)); };
  const u32 fh = FH_OFF + c * 2048 + (r >> 2) * 256;
  STAMP(8);
  const int start = p2_start(w & 3);
  const u32 vb = WT_OFF + (u32)(5 * c * 54 + start) * 1024u + l * 16;
  u32x4 B[4][5];
  asm volatile("s_cmp_lg_u32 %0, 0\n\ts_cbranch_scc1 .Lnp%=\n\ts_setprio 3\n.Lnp%=:" ::"s"(w) : "scc");
  if (w < 4) {
    if (late) {
#pragma unroll 1
      for (int pc = max(100 + 10 * w, 116); pc < 110 + 10 * w; ++pc) p2_piece((const char*)(wd + 80 * c), pc, l);
    }
    wait_flags<24>(ws, FW_OFF + c * 256 + n * 4, l < 16, tag);
#pragma unroll
    for (int s = 0; s < 4; ++s) {
#pragma unroll
      for (int cb = 0; cb < 5; ++cb) B[s][cb] = bload(rs, vb, cb * 55296 + (7 + s) * 1024);
      SB0;
    }
  }
  if (JOB) mjob(job, tag, ws, rs, WO);
  if (w >= 4) {
    const int u = w - 4;
    int ta = threadIdx.x;
    asm volatile("" : "+v"(ta));
    const int l = ta & 63, g = l >> 4, n = l & 15;
    u32x4 P[4][5];
    auto load_p = [&]() {
#pragma unroll
      for (int t = 0; t < 4; ++t) {
#pragma unroll
        for (int s = 0; s < 5; ++s) P[t][s] = bload(rs, ACT_OFF + (u32)(4 * r * NSTEP + 54 + 5 * u) * 1024u + l * 16, ((t + u) & 3) * (NSTEP * 1024) + s * 1024);
        SB0;
      }
    };
    __builtin_amdgcn_s_setprio(2);
    if (u == 0) { wait_flags<8>(ws, l < 16 ? FW_OFF + c * 256 + l * 4 : fh + (18 + (l & 3)) * 4, l < 20, tag); lds_ticket(L_CNT + 20); } else lds_wait(L_CNT + 20, 1);
    load_p();
    __builtin_amdgcn_s_setprio(0);
    STAMP(9);
    f32x4 acc[5][4];
    const u32 mhint = MT_OFF + (u32)((5 * c + 4) * 20 + 5 * u + 4) * 1024u + g * 256 + 252;
    float rd[4][2];
    u32 mh;
    {
      u32x4 Z[5];
#pragma unroll 1
      for (int spin = 0; spin < SPIN_CAP; ++spin) {
        u32 mz = 0;
#pragma unroll
        for (int s = 0; s < 5; ++s) Z[s] = bload(rs, ZB_OFF + r * 1280 + 320 * u + 80 * g, s * 16);
        mh = __builtin_amdgcn_raw_buffer_load_b32(rs, mhint, 0, 16);
#pragma unroll
        for (int s = 0; s < 5; ++s) mz = max4(mz, Z[s]);
        if (!__any(mz == SENT)) break;
        __builtin_amdgcn_s_sleep(10);
        asm volatile("" ::: "memory");
      }
      STAMP(14);
      const bool m0 = n == 4 * g, m1 = n == 4 * g + 1;
#pragma unroll
      for (int s = 0; s < 5; ++s)
#pragma unroll
        for (int q = 0; q < 4; ++q) Z[s][q] = ((8 * s + 2 * q >= 20) ? m1 : m0) ? Z[s][q] : 0u;
#pragma unroll
      for (int t = 0; t < 4; ++t) {
        f32x4 den = {0.f, 0.f, 0.f, 0.f};
#pragma unroll
        for (int s = 0; s < 5; ++s) den = mfma16(Z[s], P[t][s], den);
        rd[t][0] = __builtin_amdgcn_rcpf(den[0] + 1e-6f); rd[t][1] = __builtin_amdgcn_rcpf(den[1] + 1e-6f);
      }
    }
    STAMP(11);
    if (__any(mh == SENT)) wait_data<4>(rs, mhint);
    STAMP(10);
    u32x4 M[5][5];
    const u32 vm = MT_OFF + (u32)(5 * c * 20 + 5 * u) * 1024u + l * 16;
    auto load_m = [&](int c0, int c1) {
#pragma unroll
      for (int cb = 0; cb < 5; ++cb) {
        if (cb < c0 || cb >= c1) continue;
#pragma unroll
        for (int s = 0; s < 5; ++s) M[cb][s] = bload(rs, vm, (cb * 20 + s) * 1024);
      }
    };
    __builtin_amdgcn_s_setprio(2);
#pragma unroll
    for (int t = 0; t < 4; ++t)
#pragma unroll
      for (int s = 0; s < 5; ++s) {
#pragma unroll
        for (int k = MRATE * (5 * t + s); k < MRATE * (5 * t + s + 1); ++k) if (k < 25) M[k / 5][k % 5] = bload(rs, vm, ((k / 5) * 20 + k % 5) * 1024);
#pragma unroll
        for (int q = 0; q < 4; ++q) { const float rr = rd[t][8 * s + 2 * q >= 20]; P[t][s][q] = pk_bf16(bf_lo(P[t][s][q]) * rr, bf_hi(P[t][s][q]) * rr); }
        asm volatile("" : "+v"(P[t][s]));
        SB0;
      }
    __builtin_amdgcn_s_setprio(APRIO);
    lds_wait(L_CNT + 4, 4);
    STAMP(17);
    u32 mx = 0;
#pragma unroll
    for (int s = 0; s < 5; ++s) mx = max4(mx, M[0][s]);
#pragma unroll
    for (int cb = 0; cb < 6; ++cb) {
      if (cb < 5 && __builtin_expect(__any(mx == SENT), 0)) {
#pragma unroll 1
        for (int spin = 0; spin < SPIN_CAP; ++spin) {
          asm volatile("" ::: "memory");
          load_m(cb, 5);
          mx = 0;
#pragma unroll
          for (int s = 0; s < 5; ++s) mx = max4(mx, M[cb][s]);
          if (!__any(mx == SENT)) break;
        }
      }
      mx = 0;
#pragma unroll
      for (int s = 0; s < 5; ++s) {
        if (cb < 5) {
#pragma unroll
          for (int t = 0; t < 4; ++t) acc[cb][t] = mfma16(M[cb][s], P[t][s], s == 0 ? f32x4{0.f, 0.f, 0.f, 0.f} : acc[cb][t]);
        }
        if (cb < 4 && s >= 2) { mx = max4(mx, M[cb + 1][2 * s - 4]); if (s < 4) mx = max4(mx, M[cb + 1][2 * s - 3]); }
        if (cb > 0 && s >= 1 && s < 4 && !(cb == 5 && s == 3)) lds_st<f32x4>(p2_attblk(u, cb * 3 - 4 + s) + l * 16, acc[cb - 1][s]);
        SB0;
      }
    }
    STAMP(16);
    lds_wait(L_CNT, 4);
#pragma unroll
    for (int k = 0; k < 6; ++k) lds_st<f32x4>(u * 25600 + (15 + k) * 1024 + l * 16, k < 5 ? acc[k][0] : acc[4][3]);
    STAMP(15);
    __syncthreads();
    if constexpr (!JOB) asm volatile(".Lfk_end_a:");
    __builtin_amdgcn_endpgm();
  } else {
    const u32 va = ACT_OFF + (u32)(4 * r * NSTEP + start) * 1024u + l * 16;
    u32 so[4];
#pragma unroll
    for (int t = 0; t < 4; ++t) so[t] = ((t + w) & 3) * (NSTEP * 1024);
    const u32 wi = (16 * (g >> 1) + 4 * (g & 1) + (n >> 2)) * 160 + 8 * (l & 3), wl = w * 25600 + wi, wl2 = L_ATT + w * 10240 + wi;
    u32x4 A[14][4], bl[3];
    bl[0] = tr_frag(wl2, 1280); bl[1] = tr_frag(wl2 + 32, 1280);
    Poll pl = {};
    if (w == 0) { poll_flags(pl, ws, fh + min(l, 17) * 4, tag); lds_ticket(L_CNT + 16); } else lds_wait(L_CNT + 16, 1);
    STAMP(9);
    __builtin_amdgcn_s_setprio(HPRIO);
#pragma unroll
    for (int s = 0; s < 4; ++s) {
#pragma unroll
      for (int t = 0; t < 4; ++t) A[s][t] = bload(rs, va, so[t] + h_step(s) * 1024);
      SB0;
    }
    f32x4 acc[5][4];
#pragma unroll
    for (int cb = 0; cb < 5; ++cb)
#pragma unroll
      for (int t = 0; t < 4; ++t) acc[cb][t] = f32x4{0.f, 0.f, 0.f, 0.f};
    const u32 oo = (u32)((64 * r + 16 * w + n) * 640 + 80 * c + 4 * g);
    u32x2 xr[5];
    p2_hsteps<0>(acc, A, B, bl, rs, va, so, vb, wl, wl2, w < 2, pl, x + oo, xr);
    STAMP(10);
#pragma unroll
    for (int cb = 0; cb < 5; ++cb)
#pragma unroll
      for (int t = 1; t < 4; ++t) lds_st<f32x4>(w * 25600 + (cb * 3 + (t - 1)) * 1024 + l * 16, acc[cb][t]);
#pragma unroll
    for (int cb = 0; cb < 5; ++cb) { acc[cb][0] += f32x4{bf_lo(xr[cb][0]), bf_hi(xr[cb][0]), bf_lo(xr[cb][1]), bf_hi(xr[cb][1])}; asm volatile("" : "+v"(acc[cb][0])); }
    lds_meet(L_CNT + 8, 4);
#pragma unroll
    for (int cb = 0; cb < 5; ++cb) {
#pragma unroll
      for (int o = 1; o < 4; ++o) acc[cb][0] += lds_ld<f32x4>(((w + o) & 3) * 25600 + (cb * 3 + (3 - o)) * 1024 + l * 16);
      asm volatile("" : "+v"(acc[cb][0]));
    }
    STAMP(11);
    __syncthreads();
    STAMP(12);
#pragma unroll
    for (int cb = 0; cb < 5; ++cb) {
      f32x4 s = acc[cb][0];
      s += lds_ld<f32x4>(w * 25600 + (15 + cb) * 1024 + l * 16);
#pragma unroll
      for (int o = 1; o < 4; ++o) s += lds_ld<f32x4>(p2_attblk((w + o) & 3, cb * 3 + (3 - o)) + l * 16);
      *(u32x2*)(out + oo + 16 * cb) = u32x2{pk_bf16(s[0], s[1]), pk_bf16(s[2], s[3])};
    }
    STAMP(13);
    if constexpr (!JOB) asm volatile(".Lfk_end_h:");
    __builtin_amdgcn_endpgm();
  }
}

__host__ __device__ inline void decode(int bid, int& rt, int& ct) {
  const int xcd = bid & 7, idx = bid >> 3, cgrp = xcd >> 1, ci = idx >> 2, c2 = cgrp - 2;
  const int a = 18 + 4 * c2 + ci, b = 22 + 2 * c2 + ci, c = ci == 7 ? 30 : 16 + c2;
  const int m = ci < 4 ? a : (ci < 6 ? b : c);
  ct = cgrp < 2 ? cgrp * 8 + ci : m;
  rt = 4 * (xcd & 1) + (idx & 3) + (ct == 30 ? 8 * c2 : 0);
}

__global__ __launch_bounds__(512, 2) void fused_kernel(const u16* __restrict__ x, const u16* __restrict__ wqkv, const u16* __restrict__ wo,
                                                       const u16* __restrict__ wg, const u16* __restrict__ wu, const u16* __restrict__ wd,
                                                       u16* __restrict__ out, char* ws) {
  asm volatile("" ::"s"(x), "s"(wqkv), "s"(wo), "s"(wg), "s"(wu), "s"(wd), "s"(out), "s"(ws));
  STAMP(0);
  const int bid = blockIdx.x, xcd = bid & 7, idx = bid >> 3;
  const int l = threadIdx.x & 63, w = __builtin_amdgcn_readfirstlane(threadIdx.x >> 6);
  const int r2 = xcd * 4 + (idx >> 3), c2 = idx & 7;
  u32x4 xa, xb;
  u32 ep;
  asm volatile("s_load_dword %2, %5, 0x0\n\tglobal_load_dwordx4 %0, %3, %4\n\tglobal_load_dwordx4 %1, %3, %4 offset:640"
               : "=&v"(xa), "=&v"(xb), "=&s"(ep) : "v"(min(l, 39) * 16), "s"((const char*)x + (size_t)(bid * 8 + w) * 1280), "s"(ws + EP_OFF) : "memory");
  int rt, ct;
  decode(bid, rt, ct);
  asm volatile("" : "+s"(rt), "+s"(ct));
  P1Src src;
  phase1_start(src, rt & 7, ct, x, wqkv, wg, wu);
  {
    const char* pc; u32 n0, n1;
    asm volatile("s_getpc_b64 %0\n.Lfk_beg:\n\ts_mov_b32 %1, .Lfk_end_h-.Lfk_beg\n\ts_mov_b32 %2, .Lfk_end_a-.Lfk_beg" : "=s"(pc), "=s"(n0), "=s"(n1));
    dma16((const char*)((size_t)pc & ~(size_t)15) + min((u32)(idx * 8 + w) * 512u, max(n0, n1) - 1024u), (l & 31) * 16, L_SP + (w & 3) * 1024);
  }
  asm volatile("s_waitcnt vmcnt(8) lgkmcnt(0)" : "+v"(xa), "+v"(xb), "+s"(ep)::"memory");
  const u32 tag = ep + 1u;
  {
    float s = 0.f, q = 0.f;
#pragma unroll
    for (int e = 0; e < 4; ++e) { s = dot2(xa[e], 0x3f803f80u, s); q = dot2(xa[e], xa[e], q); s = dot2(xb[e], 0x3f803f80u, s); q = dot2(xb[e], xb[e], q); }
    s = wave_sum(l < 40 ? s : 0.f); q = wave_sum(l < 40 ? q : 0.f);
    const float mean = s * (1.f / 640.f);
    const float sd = __builtin_amdgcn_sqrtf(round_bf(round_bf(fmaxf(q * (1.f / 640.f) - mean * mean, 0.f)) + 1e-5f));
    bstore8(u32x2{pk_bf16(mean, sd), tag}, __builtin_amdgcn_make_buffer_rsrc(ws, 0, WS_END, 0x00020000), l == 0 ? STG_OFF + (bid * 8 + w) * 8 : OOB);
  }
  u32x4 WO[5][2];
  if (ct == 30) {
    spare(rt, c2, tag, wo, wd, ws, WO);
    phase2<true>(r2, c2, rt, false, tag, x, wd, out, ws, WO);
  } else {
    if (ct < 18) phase1<0>(src, rt, ct, ct, c2, tag, wd, ws);
    else if (ct < 26) phase1<2>(src, rt, ct, ct - 18, c2, tag, wd, ws);
    else phase1<1>(src, rt, ct, ct - 26, c2, tag, wd, ws);
    phase2<false>(r2, c2, -1, ct >= 18 && ct < 26, tag, x, wd, out, ws, WO);
  }
}

extern "C" void kernel_launch(void* const* d_in, const int* in_sizes, int n_in, void* d_out, int out_size, void* d_ws, size_t ws_size, hipStream_t stream) {
  (void)hipFuncSetAttribute((const void*)fused_kernel, hipFuncAttributeMaxDynamicSharedMemorySize, LDS_BYTES);
  fused_kernel<<<256, 512, LDS_BYTES, stream>>>((const u16*)d_in[0], (const u16*)d_in[1], (const u16*)d_in[2], (const u16*)d_in[4], (const u16*)d_in[5],
                                                (const u16*)d_in[6], (u16*)d_out, (char*)d_ws);
}
```

```cpp
#include <hip/hip_runtime.h>
#include <stdint.h>
#include <type_traits>

typedef unsigned short u16;
typedef unsigned int u32;
typedef unsigned long long u64;
typedef __bf16 bf16x8 __attribute__((ext_vector_type(8)));
typedef __bf16 bf16x2 __attribute__((ext_vector_type(2)));
typedef short s16x4 __attribute__((ext_vector_type(4)));
typedef float f32x2 __attribute__((ext_vector_type(2)));
typedef float f32x4 __attribute__((ext_vector_type(4)));
typedef float f32x16 __attribute__((ext_vector_type(16)));
typedef u32 u32x2 __attribute__((ext_vector_type(2)));
typedef u32 u32x4 __attribute__((ext_vector_type(4)));
typedef __amdgpu_buffer_rsrc_t rsrc_t;
#define LAS __attribute__((address_space(3)))
#define GAS __attribute__((address_space(1)))
#define DI __device__ __forceinline__
#define RLX_AGENT __ATOMIC_RELAXED, __HIP_MEMORY_SCOPE_AGENT
#define SB0 __builtin_amdgcn_sched_barrier(0)

constexpr int NSTEP = 74;
constexpr u32 ACT_OFF = 0;
constexpr u32 MT_OFF  = ACT_OFF + 128u * NSTEP * 1024u;
constexpr u32 WT_OFF  = MT_OFF + 40u * 20u * 1024u;
constexpr u32 KVR_OFF = WT_OFF + 40u * 54u * 1024u;
constexpr u32 ZB_OFF  = KVR_OFF + 16u * 8u * 3840u;
constexpr u32 STG_OFF = ZB_OFF + 32u * 1280u;
constexpr u32 CSG_OFF = STG_OFF + 2048u * 8u;
constexpr u32 FH_OFF  = CSG_OFF + 30u * 8u * 192u * 8u;
constexpr u32 FK_OFF  = FH_OFF + 64u * 256u;
constexpr u32 FW_OFF  = FK_OFF + 8u * 256u;
constexpr u32 EP_OFF  = FW_OFF + 8u * 256u;
constexpr u32 TS_OFF = EP_OFF + 256u;
constexpr u32 WS_END  = TS_OFF + 16384u;
constexpr u32 OOB = 0x7ffffff0u;
constexpr int SPIN_CAP = 1 << 17;

constexpr int L_W = 98304, L_SP = 159744, L_ATT = 102400, L_KX = 119808, L_MJ = 143360, L_CNT = 163808, LDS_BYTES = 163840;
constexpr int XB0 = 65536, XB1 = 0, XB2 = 32768;

DI float bf_lo(u32 v) { return __uint_as_float(v << 16); }
DI float bf_hi(u32 v) { return __uint_as_float(v & 0xffff0000u); }
DI u32 pk_bf16(float a, float b) { f32x2 v = {a, b}; return __builtin_bit_cast(u32, __builtin_convertvector(v, bf16x2)); }
DI float round_bf(float a) { return bf_lo(pk_bf16(a, a)); }
DI float dot2(u32 a, u32 b, float c) { return __builtin_amdgcn_fdot2_f32_bf16(__builtin_bit_cast(bf16x2, a), __builtin_bit_cast(bf16x2, b), c, false); }
DI float xsum32(float v) { auto r = __builtin_amdgcn_permlane32_swap(__float_as_uint(v), __float_as_uint(v), false, false); return __uint_as_float(r[0]) + __uint_as_float(r[1]); }
DI float xsum16(float v) { auto r = __builtin_amdgcn_permlane16_swap(__float_as_uint(v), __float_as_uint(v), false, false); return __uint_as_float(r[0]) + __uint_as_float(r[1]); }
DI float oct_sum(float v) {
  v += __uint_as_float(__builtin_amdgcn_mov_dpp(__float_as_uint(v), 0xB1, 0xf, 0xf, true));
  v += __uint_as_float(__builtin_amdgcn_mov_dpp(__float_as_uint(v), 0x4E, 0xf, 0xf, true));
  return v + __uint_as_float(__builtin_amdgcn_mov_dpp(__float_as_uint(v), 0x141, 0xf, 0xf, true));
}
DI float wave_sum(float v) { v = oct_sum(v); v += __uint_as_float(__builtin_amdgcn_mov_dpp(__float_as_uint(v), 0x140, 0xf, 0xf, true)); return xsum32(xsum16(v)); }
DI f32x16 mfma32(u32x4 a, u32x4 b, f32x16 c) { return __builtin_amdgcn_mfma_f32_32x32x16_bf16(__builtin_bit_cast(bf16x8, a), __builtin_bit_cast(bf16x8, b), c, 0, 0, 0); }
DI f32x4 mfma16(u32x4 a, u32x4 b, f32x4 c) { return __builtin_amdgcn_mfma_f32_16x16x32_bf16(__builtin_bit_cast(bf16x8, a), __builtin_bit_cast(bf16x8, b), c, 0, 0, 0); }
DI u32x2 tr_read(u32 a) { return __builtin_bit_cast(u32x2, __builtin_amdgcn_ds_read_tr16_b64_v4i16((LAS s16x4*)(size_t)a)); }
DI u32x4 tr_frag(u32 a, int d) { const u32x2 lo = tr_read(a), hi = tr_read(a + d); return u32x4{lo[0], lo[1], hi[0], hi[1]}; }
template <class T> DI T lds_ld(u32 a) { return *(const LAS T*)(size_t)a; }
template <class T> DI void lds_st(u32 a, T v) { *(LAS T*)(size_t)a = v; }
DI void dma16(const char* sbase, u32 voff, u32 ldst) {
  asm volatile("s_mov_b32 m0, %2\n\ts_nop 0\n\tglobal_load_lds_dwordx4 %0, %1" ::"v"(voff), "s"(sbase), "s"(ldst) : "memory");
}
template <int N> DI void wait_vm() { asm volatile("s_waitcnt vmcnt(%0)" ::"n"(N) : "memory"); }
DI void wait_lgkm0() { __builtin_amdgcn_s_waitcnt(0xC07F); }
DI void bar() { __builtin_amdgcn_s_barrier(); }
DI u32x4 bload(rsrc_t r, u32 voff, u32 soff) { return __builtin_amdgcn_raw_buffer_load_b128(r, voff, soff, 16); }
DI void bstore16(u32x4 v, rsrc_t r, u32 voff) { __builtin_amdgcn_raw_buffer_store_b128(v, r, voff, 0, 16); }
DI void bstore8(u32x2 v, rsrc_t r, u32 voff) { __builtin_amdgcn_raw_buffer_store_b64(v, r, voff, 0, 16); }
constexpr u32 SENT = 0xffffffffu;
DI u32 nosent(u32 v) { return min(v, SENT - 1u); }
DI u32x4 nosent4(u32x4 v) { return u32x4{nosent(v[0]), nosent(v[1]), nosent(v[2]), nosent(v[3])}; }
DI u32 max4(u32 m, u32x4 v) { return max(max(m, max(v[0], v[1])), max(v[2], v[3])); }
constexpr float LOG2E = 1.4426950408889634f;
template <int SLEEP = 4> DI void wait_flags(const char* ws, u32 off, bool on, u32 tag) {
  GAS u32* p = (GAS u32*)(ws + off);
#pragma unroll 1
  for (int spin = 0; spin < SPIN_CAP; ++spin) {
    const u32 v = on ? __hip_atomic_load(p, RLX_AGENT) : tag;
    if (__all(v == tag)) break;
    __builtin_amdgcn_s_sleep(SLEEP);
  }
  __builtin_amdgcn_fence(__ATOMIC_ACQUIRE, "wavefront");
  asm volatile("" ::: "memory");
}
template <int SLEEP> DI void wait_data(rsrc_t rs, u32 off) {
#pragma unroll 1
  for (int spin = 0; spin < SPIN_CAP; ++spin) {
    if (!__any(__builtin_amdgcn_raw_buffer_load_b32(rs, off, 0, 16) == SENT)) break;
    __builtin_amdgcn_s_sleep(SLEEP);
    asm volatile("" ::: "memory");
  }
  asm volatile("" ::: "memory");
}
struct Poll { u32 v[2]; };
DI void poll_flags(Poll& p, const char* ws, u32 off, u32 tag) {
  u32 cnt = SPIN_CAP;
#define POLL_STEP(LD, CK) "s_sleep 4\n\tglobal_load_dword %" #LD ", %3, %4 sc1\n\ts_waitcnt vmcnt(1)\n\tv_cmp_eq_u32 vcc, %5, %" #CK "\n\ts_nop 0\n\ts_cmp_eq_u64 vcc, -1\n\ts_cbranch_scc1 .Lpd%=\n\t"
  asm volatile("global_load_dword %0, %3, %4 sc1\n"
               ".Lpl%=:\n\t" POLL_STEP(1, 0) POLL_STEP(0, 1)
               "s_sub_u32 %2, %2, 1\n\ts_cmp_lg_u32 %2, 0\n\ts_cbranch_scc1 .Lpl%=\n"
               ".Lpd%=:"
               : "=&v"(p.v[0]), "=&v"(p.v[1]), "+s"(cnt) : "v"(off), "s"(ws), "s"(tag) : "vcc", "scc", "memory");
#undef POLL_STEP
}
DI void poll_done(Poll& p) { asm volatile("" ::"v"(p.v[0]), "v"(p.v[1])); }
template <int REP> DI void raise_flag(char* ws, u32 off, u32 tag) {
  asm volatile("s_mov_b64 exec, %3\n\tglobal_store_dword %0, %1, %2 sc1\n\ts_mov_b64 exec, -1" ::"v"(off), "v"(tag), "s"(ws), "s"((1ull << REP) - 1) : "memory");
}
template <int REP = 1, int STRIDE = 0, int KEEP = 0> DI void publish(char* ws, u32 off, u32 tag) {
  wait_vm<KEEP>();
  wait_lgkm0(); bar();
  asm volatile("" ::: "memory");
  if (__builtin_amdgcn_readfirstlane(threadIdx.x) == 0) raise_flag<REP>(ws, off + threadIdx.x * STRIDE, tag);
}

struct Sched { int op[96]; int first[20]; };
constexpr Sched make_sched(int n16) {
  Sched r{}; int n = 0;
  for (int k = 0; k < 4; ++k) r.op[n++] = 100 + k;
  r.op[n++] = 0; r.op[n++] = 1; r.op[n++] = 2;
  for (int k = 0; k < 4; ++k) r.op[n++] = 104 + k;
  r.op[n++] = 3; r.op[n++] = 4;
  for (int s = 0; s < 18; ++s) {
    r.first[s + 1] = n;
    const int it = s / 2;
    if (s == 11) { r.op[n++] = 1000; r.op[n++] = 1000; }
    constexpr int FILL[15] = {5, 108, 109, 6, 7, 110, 111, 8, 9, 112, 113, 10, 11, 114, 115};
    if (s < 3) { for (int k = 0; k < 5; ++k) r.op[n++] = FILL[5 * s + k]; }
    else if (s <= 14) {
      if ((s & 1) == 0) { r.op[n++] = 7 + 3 * it; r.op[n++] = 8 + 3 * it; r.op[n++] = 100 + 4 * (it + 2) + 2; r.op[n++] = 100 + 4 * (it + 2) + 3; }
      else { r.op[n++] = 9 + 3 * it; r.op[n++] = 100 + 4 * (it + 3); r.op[n++] = 100 + 4 * (it + 3) + 1; }
    } else for (int k = 0; k < (s == 15 ? n16 / 100 : s == 16 ? n16 / 10 % 10 : n16 % 10); ++k) r.op[n++] = 1000;
  }
  r.first[19] = n;
  return r;
}
constexpr int vm_need(int n16, int t, int b) {
  const Sched sch = make_sched(n16);
  const int issued = sch.first[b + 1];
  int last = -1;
  for (int k = 0; k < issued; ++k) {
    const int op = sch.op[k];
    if (op < 100 ? (8 * op <= 12 * t + 11) : (op < 1000 && (op - 100) / 4 <= t / 2)) last = k;
  }
  return issued - 1 - last;
}

struct Frag { u32x4 x[2][2]; u32x4 w[3][2]; };

DI int p2_start(int i) { return i * 13 + min(i, 2); }
DI int p2_blkstep(int blk) { const int i = blk < 20 ? blk / 5 : (blk - 20) >> 1; return p2_start(i) + (blk < 20 ? blk - 5 * i : 5 + (blk & 1)); }
DI u32 p2_voff(int p5, int l) { const int n = p5 * 64 + l, r = n / 10, c = n - 10 * r; return (u32)((r & 19) | ((r & 4) << 1) | ((r & 8) >> 1)) * 1280u + c * 16; }
DI void p2_piece(const char* wdc, int piece, int l) {
  const int blk = piece / 5;
  dma16(wdc + (size_t)p2_blkstep(blk) * 40960, p2_voff(piece - 5 * blk, l), piece * 1024);
}

template <int TYPE> DI f32x16 p1_mfma(u32x4 w, u32x4 x, f32x16 c) { return TYPE == 2 ? mfma32(x, w, c) : mfma32(w, x, c); }

template <int TYPE, class F> DI void p1_step(f32x16 (&acc)[3][2], const Frag& cur, bool third, F&& fill) {
#pragma unroll
  for (int i = 0; i < 8; ++i) {
    const int b = (i >> 1) & 1, k = i >> 2, t = i & 1;
    acc[b][t] = p1_mfma<TYPE>(cur.w[b][k], cur.x[t][k], acc[b][t]);
    fill(i);
    SB0;
  }
  if (TYPE != 1 || third) {
#pragma unroll
    for (int i = 8; i < 12; ++i) {
      const int k = (i >> 1) & 1, t = i & 1;
      if (TYPE == 2 && t == 1) continue;
      acc[2][t] = p1_mfma<TYPE>(cur.w[2][k], cur.x[t][k], acc[2][t]);
      SB0;
    }
  }
}
template <int I, int N, class F> DI void static_for(F&& f) { if constexpr (I < N) { f(std::integral_constant<int, I>{}); static_for<I + 1, N>(f); } }
constexpr int xslot(int i) { return i % 3 == 0 ? XB0 : (i % 3 == 1 ? XB1 : XB2); }

DI float exp2_le1(float t) { return __builtin_amdgcn_fmed3f(__builtin_amdgcn_exp2f(t), 0.f, 1.f); }
DI float phi_f(float t) { return fmaxf(t, 1e-6f) + exp2_le1(t * LOG2E); }

struct P1Src { const char* xg; const char* wb[3]; u32 xvo, wvo[3]; int k0; };
DI void phase1_start(P1Src& p, int rt, int ct, const u16* x, const u16* wqkv, const u16* wg, const u16* wu) {
  const int l = threadIdx.x & 63, w = __builtin_amdgcn_readfirstlane(threadIdx.x >> 6);
  p.k0 = (0x96418530u >> (4 * rt)) & 15;
  p.xg = (const char*)x + (size_t)rt * 256 * 1280;
  p.xvo = (u32)(8 * w + (l >> 3)) * 1280 + (((l & 7) ^ ((4 * (w & 1) + (l >> 4)) & 7)) * 16);
#pragma unroll
  for (int xi = 0; xi < 4; ++xi) dma16(p.xg + (xi * 64 * 1280 + p.k0 * 128), p.xvo, XB0 + xi * 8192 + w * 1024);
  const bool gu = ct < 18, kv = ct < 26;
  const u32 stride = gu ? 3456 : 3840, lim0 = (gu || !kv) ? 12 : 10, lim1 = gu ? 12 : kv ? 10 : 8;
  const char* b0 = (gu ? (const char*)wg : (const char*)wqkv) + (gu ? 192 * ct : kv ? 160 * ct - 1600 : 320 * min(ct, 29) - 8320);
  const char* b1 = gu ? (const char*)wu + 192 * ct : b0 + (kv ? 1280 : 192);
#pragma unroll
  for (int i = 0; i < 3; ++i) {
    const int pp = 8 * i + w, st = pp >= 12, pi = pp - 12 * st, part = pi >= 6;
    const int m = (pi - 6 * part) * 64 + l, row = m / 12, cp = m - 12 * row;
    const u32 ch = part ? cp ^ 2 : cp;
    p.wb[i] = part ? b1 : b0;
    p.wvo[i] = (u32)(st * 32 + row) * stride + (ch >= (part ? lim1 : lim0) ? ch - 4 : ch) * 16;
    dma16(p.wb[i] + (size_t)((u32)p.k0 * 64u * stride), p.wvo[i], L_W + (8 * i + w) * 1024);
  }
}

template <int TYPE>
DI void phase1(const P1Src& src, int rt, int ct, int j, int c2, u32 tag, const u16* wd, char* ws) {
  const int tid = threadIdx.x, l = tid & 63, w = __builtin_amdgcn_readfirstlane(tid >> 6);
  const int tg = w & 3, cgp = w >> 2, h = l >> 5, n = l & 31;
  auto dma_x = [&](int kk, int xi, u32 slot) { dma16(src.xg + (xi * 64 * 1280 + kk * 128), src.xvo, slot + xi * 8192 + w * 1024); };
  constexpr u32 WPER = 64 * (TYPE == 0 ? 3456 : 3840);
  const u32 wl0 = L_W + w * 1024, wl1 = w >= 4 ? wl0 - 61440 : wl0;
  auto dma_w = [&](int kk, int grp) { const int pos = (8 * grp) % 60; dma16(src.wb[grp % 3] + (size_t)((u32)kk * WPER), src.wvo[grp % 3], (pos == 56 ? wl1 : wl0) + pos * 1024); };
  auto rot = [&](int kk) { return kk + 1 >= 10 ? kk - 9 : kk + 1; };
  const int k1 = rot(src.k0), k2 = rot(k1);

  auto bphys = [&](int b) { int v = b + tg; v = v >= 3 ? v - 3 : v; return v >= 3 ? v - 3 : v; };
  u32 xa[2][4], wa[3][2];
  {
    const int ksw = TYPE == 2 ? (tg & 1) : 0;
#pragma unroll
    for (int t = 0; t < 2; ++t) {
      const int row = 64 * tg + 32 * (TYPE == 2 ? t ^ cgp : t) + n;
#pragma unroll
      for (int k = 0; k < 4; ++k) xa[t][k] = row * 128 + 16 * ((2 * (k ^ ksw) + h) ^ ((row >> 1) & 7));
    }
    const int blk = (l >> 4) & 1, q = (l & 15) >> 2, p = l & 3, rowb = L_W + (8 * h + q) * 192;
    if (TYPE == 2) {
      const int a = cgp ? 3 : 0;
      const u32 b0 = rowb + (a + blk) * 32 + 8 * p, b1 = 6144 + rowb + ((a + blk) ^ 1) * 32 + 8 * p;
      const u32 b2 = rowb + (p >> 1) * 6144 + ((4 + (cgp ^ blk)) ^ (p & 2)) * 16 + 8 * (p & 1);
#pragma unroll
      for (int k = 0; k < 2; ++k) { const u32 o = (k ^ ksw) * 3072; wa[0][k] = b0 + o; wa[1][k] = b1 + o; wa[2][k] = b2 + o; }
    } else {
#pragma unroll
      for (int b = 0; b < 3; ++b) {
        const u32 in32 = 16 * (p & 1) + 8 * (p >> 1);
        if (TYPE == 0) wa[b][0] = rowb + blk * 6144 + ((3 * cgp + bphys(b)) ^ blk) * 32 + in32;
        else wa[b][0] = rowb + cgp * 6144 + ((2 * (cgp ? (b == 2 ? 2 : b ^ (tg & 1)) : bphys(b)) + blk) ^ cgp) * 32 + in32;
        wa[b][1] = 0;
      }
    }
  }
  auto rd_w = [&](int b, int k, int off) { return tr_frag((TYPE == 2 ? wa[b][k] : wa[b][0] + k * 3072) + off, 768); };
  auto rd_x = [&](int t, int k4, int off) {
    return TYPE == 2 ? lds_ld<u32x4>(xa[t][k4] + off) : lds_ld<u32x4>(xa[0][k4] + off + t * 4096);
  };
  auto fill_rd = [&](Frag& f, int i, int st) {
    const int wo_ = (st % 5) * 12288, xo = xslot(st / 2), k4 = 2 * (st & 1);
    if (i == 0) { f.w[0][0] = rd_w(0, 0, wo_); f.x[0][0] = rd_x(0, k4, xo); }
    if (i == 1) { f.x[1][0] = rd_x(1, k4, xo); f.w[1][0] = rd_w(1, 0, wo_); }
    if (i == 2) { f.w[0][1] = rd_w(0, 1, wo_); f.x[0][1] = rd_x(0, k4 + 1, xo); }
    if (i == 3) { f.x[1][1] = rd_x(1, k4 + 1, xo); f.w[1][1] = rd_w(1, 1, wo_); }
    if (i == 4) { f.w[2][0] = rd_w(2, 0, wo_); f.w[2][1] = rd_w(2, 1, wo_); }
  };
  f32x16 acc[3][2];
#pragma unroll
  for (int b = 0; b < 3; ++b)
#pragma unroll
    for (int t = 0; t < 2; ++t)
#pragma unroll
      for (int i = 0; i < 16; ++i) acc[b][t][i] = 0.f;
  auto colid = [&](int b) {
    if (TYPE == 2) return b == 2 ? ((n >> 3) & 1) * 96 + 32 + 8 * (cgp ^ (n >> 4)) + (n & 7) : b * 96 + (cgp ? 48 : 0) + n;
    const int p = (n & 15) >> 2, c16 = 8 * (p & 1) + 4 * (p >> 1) + (n & 3);
    if (TYPE == 0) return (n >> 4) * 96 + (3 * cgp + bphys(b)) * 16 + c16;
    return cgp * 96 + (cgp ? (b == 2 ? 2 : b ^ (tg & 1)) : bphys(b)) * 32 + (n & 16) + c16;
  };
  u32 goff[2], pvo[5], coff;
  {
    const int hi = (TYPE == 1 && tid >= 128) ? tid - 128 : tid;
    goff[0] = CSG_OFF + ct * 12288 + (tid & 7) * 1536 + (tid >> 3) * 16;
    goff[1] = w < 4 ? CSG_OFF + ct * 12288 + (tid & 7) * 1536 + 1024 + (hi >> 3) * 16 : STG_OFF + 2048 * rt + (tid & 127) * 16;
    coff = l < 32 ? CSG_OFF + (ct * 1536 + rt * 192 + colid(0)) * 8 : OOB;
#pragma unroll
    for (int k = 0; k < 5; ++k) { pvo[k] = p2_voff((w + k) % 5, l); asm volatile("" : "+v"(pvo[k])); }
    asm volatile("" : "+v"(goff[0]), "+v"(goff[1]), "+v"(coff));
  }
#pragma unroll
  for (int xi = 0; xi < 4; ++xi) dma_x(k1, xi, XB1);
  dma_w(k1, 3); dma_w(k1, 4);
  Frag fa, fb;
  wait_vm<9>();
  bar(); SB0;
#pragma unroll
  for (int i = 0; i < 5; ++i) fill_rd(fa, i, 0);
  SB0;
  SB0;
  const bool third = cgp == 0;
  float cs[3] = {0.f, 0.f, 0.f};
  auto colsum = [&](const Frag& f, int i) {
    constexpr u32 ones = 0x3f803f80u;
    if (TYPE == 2) {
      if (i < 6) { const int b = i >> 1; cs[b] = dot2(f.w[b][0][2 * (i & 1)], ones, cs[b]); cs[b] = dot2(f.w[b][0][2 * (i & 1) + 1], ones, cs[b]); asm volatile("" : "+v"(cs[b])); }
    } else { cs[0] = dot2(f.w[0][i >> 2][i & 3], ones, cs[0]); asm volatile("" : "+v"(cs[0])); }
  };
  const rsrc_t rs = __builtin_amdgcn_make_buffer_rsrc(ws, 0, WS_END, 0x00020000);
  int ka = k2, kb = rot(k2);
  const char* wdc = (const char*)(wd + 80 * c2);
  u32x4 gr[2];
  f32x2 gs[2];
  auto gload = [&]() {
#pragma unroll
    for (int k = 0; k < 2; ++k) asm volatile("global_load_dwordx4 %0, %1, %2 sc1" : "=v"(gr[k]) : "v"(goff[k]), "s"(ws) : "memory");
  };
  auto gcheck = [&]() {
    asm volatile("" : "+v"(gr[0]), "+v"(gr[1]));
    return __all(gr[0][1] == tag && gr[0][3] == tag && gr[1][1] == tag && gr[1][3] == tag);
  };
  auto pf2 = [&](int base) {
    constexpr int NP = TYPE == 2 ? 116 : 140;
    const bool clip = base + 8 > NP;
    const int piece = base + (clip ? w & 3 : w), blk = piece / 5;
    u32 vo = pvo[base % 5];
    if (clip) { u32 v1 = pvo[(base + 1) % 5]; asm volatile("" : "+v"(v1)); vo = w < 4 ? vo : v1; }
    const int lo = blk / 5, hi = (blk - 20) >> 1;
    const int st = base + 8 <= 100 ? p2_start(lo) + blk - 5 * lo : base >= 100 ? p2_start(hi) + 5 + (blk & 1) : (w < 4 ? 45 : 5);
    dma16(wdc + (u32)st * 40960u, vo, piece * 1024);
  };
  constexpr int NX = TYPE == 2 ? 432 : 332;
  auto bp_of = [&](int b) { return TYPE == 0 ? bphys(b) : (cgp ? (b == 2 ? 2 : b ^ (tg & 1)) : bphys(b)); };
  auto oaddr = [&](int k) {
    const u32 dst = ACT_OFF + (u32)(16 * rt + 4 * tg + (n >> 4)) * (NSTEP * 1024) + (n & 15) * 16;
    if (TYPE == 0) { const int id = 12 * j + 6 * cgp + 2 * bp_of(k >> 1) + h; return dst + (k & 1) * 2 * NSTEP * 1024 + (id >> 2) * 1024 + (id & 3) * 256; }
    const int ch = 4 * (3 * cgp + bp_of(k >> 2)) + 2 * (k & 1) + h, g4 = ch / 5;
    return (k >= 8 && cgp) ? OOB : dst + ((k >> 1) & 1) * 2 * NSTEP * 1024 + (54 + 5 * j + ch - 5 * g4) * 1024 + g4 * 256;
  };
  static_for<0, 18>([&](auto SC) {
    constexpr int S = decltype(SC)::value, IT = S / 2;
    Frag& cur = (S & 1) ? fb : fa; Frag& nxt = (S & 1) ? fa : fb;
    wait_lgkm0();
    if (S > 0) wait_vm<vm_need(NX, S + 1, S)>();
    bar(); SB0;
    p1_step<TYPE>(acc, cur, third, [&](int i) {
      fill_rd(nxt, i, S + 1);
      if (S == 0) { if (i == 3) dma_w(k1, 5); if (i == 4 || i == 5) dma_x(k2, i - 4, XB2); if (i >= 6) dma_w(k2, i); }
      else if (S == 1) { if (i == 3 || i == 4) dma_x(k2, i - 1, XB2); if (i == 5) dma_w(k2, 8); if (i == 6) dma_w(kb, 9); if (i == 7) { dma_x(kb, 0, XB0); ka = kb; kb = rot(kb); } }
      else if (S == 2) { if (i == 3) dma_x(ka, 1, XB0); if (i == 4 || i == 5) dma_w(ka, 6 + i); if (i >= 6) dma_x(ka, i - 4, XB0); }
      else if (S > 14) { if (i >= (S == 17 ? 6 : 5)) pf2(S == 15 ? 8 * (i - 5) : S == 16 ? 96 + 8 * (i - 5) : 8 * (i - 2)); }
      else if ((S & 1) == 0) { if (i == 4 || i == 5) dma_w(ka, 3 + 3 * IT + i); if (i >= 6) dma_x(ka, i - 4, xslot(IT + 2)); }
      else { if (i == 5) dma_w(kb, 9 + 3 * IT); if (i >= 6) dma_x(kb, i - 6, xslot(IT)); if (i == 7) { ka = kb; kb = rot(kb); } }
      if (S < 2) colsum(cur, i);
      if ((S == 2 || S == 3) && i == 5 && (rt & 5) == 4) {
#pragma unroll
        for (int e = 0; e < 8; ++e) colsum(cur, e);
      }
      if (TYPE != 2) {
        if (S == 4 && i == 1) { cs[0] = xsum32(cs[0]); asm volatile("" : "+v"(cs[0])); }
        if (S == 4 && i == 3) bstore8(u32x2{__float_as_uint(cs[0]), tag}, rs, coff);
      } else {
        if (S == 4 && i >= 1 && i < 4) { const float v = xsum32(cs[i - 1]); lds_st<float>(tg < 2 ? L_SP + (tg * 192 + colid(i - 1)) * 4 : L_SP + 2048 + l * 4, v); }
        if (S == 5 && i == 1) { cs[0] = lds_ld<float>(L_SP + min(tid, 191) * 4); cs[1] = lds_ld<float>(L_SP + 768 + min(tid, 191) * 4); }
        if (S == 5 && i == 3) bstore8(u32x2{__float_as_uint(cs[0] + cs[1]), tag}, rs, tid < 192 ? CSG_OFF + (ct * 1536 + rt * 192 + tid) * 8 : OOB);
      }
      if (S == 11 && i == 0) gload();
      if (TYPE == 2 && S == 15 && i == 1 && tid < 480) bstore16(u32x4{SENT, SENT, SENT, SENT}, rs, KVR_OFF + (u32)((2 * j + (tid >= 240)) * 8 + rt) * 3840 + (tid >= 240 ? tid - 240 : tid) * 16);
      if (S == 16) {
        if (i == 1) {
          wait_vm<TYPE == 2 ? 18 : 17>();
          if (__builtin_expect(!gcheck(), 0)) {
#pragma unroll 1
            for (int spin = 0; spin < SPIN_CAP; ++spin) { __builtin_amdgcn_s_sleep(2); gload(); wait_vm<0>(); if (gcheck()) break; }
          }
        }
        if (i >= 2 && i < 6) { gs[(i - 2) >> 1][i & 1] = oct_sum(__uint_as_float(gr[(i - 2) >> 1][2 * (i & 1)])); asm volatile("" : "+v"(gs[(i - 2) >> 1][i & 1])); }
        if (i == 6) lds_st<f32x2>(L_SP + 2048 + (tid >> 3) * 8, gs[0]);
      }
      if (S == 17) {
        if (i == 0) lds_st<f32x2>(w < 4 ? L_SP + 2560 + (tid >> 3) * 8 : L_SP + (tid & 127) * 8, w < 4 ? gs[1] : f32x2{bf_lo(gr[1][0]), bf_lo(gr[1][2])});
        if (i == 2) lds_st<f32x2>((w < 4 ? L_SP + 3072 : L_SP + 1024) + (tid & 63) * 8 + (w & 1) * (w < 4 ? 0 : 512), f32x2{__builtin_amdgcn_rcpf(bf_hi(gr[1][0])), __builtin_amdgcn_rcpf(bf_hi(gr[1][2]))});
      }
    });
  });
  auto mm = [&](int b, int t, int q) { const Frag& f = q < 2 ? fa : fb; acc[b][t] = p1_mfma<TYPE>(f.w[b][q & 1], f.x[t][q & 1], acc[b][t]); };
  u32x4 fm[2], fc[3];
  auto mk_fm = [&](int t) { const u32 m = h ? 0u : (__float_as_uint(lds_ld<float>(L_SP + (64 * tg + 32 * (TYPE == 2 ? t ^ cgp : t) + n) * 4)) >> 16) ^ 0x8000u; fm[t] = u32x4{m * 0x10001u, m, 0u, 0u}; };
  auto mk_fc = [&](int b) {
    const float c = h ? 0.f : lds_ld<float>(L_SP + 2048 + colid(b) * 4), c0 = round_bf(c), c1 = round_bf(c - c0);
    fc[b] = u32x4{pk_bf16(c0, c1), pk_bf16(c - c0 - c1, 0.f), 0u, 0u};
  };
  auto mc = [&](int b, int t) { acc[b][t] = p1_mfma<TYPE>(fc[b], fm[t], acc[b][t]); };
  wait_lgkm0(); wait_vm<vm_need(NX, 19, 18)>(); bar(); SB0;
  if constexpr (TYPE != 2) {
    float rstd[2], ce[2], sd[2];
    u32 ob[8];
    auto epi = [&](int e) {
      const int b = e >> 4, t = (e >> 3) & 1, i = e & 7;
      if (TYPE == 0) {
        const float ex = __builtin_amdgcn_exp2f(acc[b][t][i] * ce[t]);
        const float hv = acc[b][t][8 + i] * __builtin_amdgcn_rcpf(ex * sd[t] + sd[t]);
        if (i & 1) ob[i >> 1] = pk_bf16(__uint_as_float(ob[i >> 1]), hv); else ob[i >> 1] = __float_as_uint(hv);
        asm volatile("" : "+v"(ob[i >> 1]));
        if (i == 7) bstore16(u32x4{ob[0], ob[1], ob[2], ob[3]}, rs, oaddr(2 * b + t));
      } else {
        const float u0 = acc[b][t][2 * i], u1 = acc[b][t][2 * i + 1];
        ob[i] = pk_bf16(fmaxf(u0, sd[t]) * rstd[t] + exp2_le1(u0 * ce[t]), fmaxf(u1, sd[t]) * rstd[t] + exp2_le1(u1 * ce[t]));
        asm volatile("" : "+v"(ob[i]));
        if ((i & 3) == 3) bstore16(u32x4{ob[i - 3], ob[i - 2], ob[i - 1], ob[i]}, rs, oaddr(4 * b + 2 * t + (i >> 2)));
      }
    };
#pragma unroll
    for (int i = 0; i < 6; ++i) {
      mm(0, i < 4 ? i >> 1 : 0, i < 4 ? i & 1 : i - 2);
      if (i < 5) fill_rd(fb, i == 1 ? 2 : i == 2 ? 1 : i, 19);
      if (i == 0) {
#pragma unroll
        for (int t = 0; t < 2; ++t) rstd[t] = lds_ld<float>(L_SP + 1024 + (64 * tg + 32 * t + n) * 4);
      }
      if (i == 1) { mk_fm(0); mk_fm(1); mk_fc(0); }
      if (i == 2) mk_fc(1);
      if (i == 3) { mc(0, 0); mk_fc(2); }
      if (i == 3) {
#pragma unroll
        for (int t = 0; t < 2; ++t) { ce[t] = (TYPE == 0 ? -LOG2E : LOG2E) * rstd[t]; sd[t] = (TYPE == 0 ? 1.f : 1e-6f) * __builtin_amdgcn_rcpf(rstd[t]); }
      }
      if (i >= 3) pf2(96 + 8 * i);
      if (i == 5) { mc(0, 1); if (tid < 2) lds_st<u32x4>(L_CNT + tid * 16, u32x4{0u, 0u, 0u, 0u}); }
      SB0;
    }
    wait_lgkm0(); bar(); SB0;
    if (TYPE == 0 && cgp) __builtin_amdgcn_s_setprio(1);
#pragma unroll
    for (int i = 0; i < 10; ++i) {
      mm(i < 2 ? 0 : 1, i < 2 ? 1 : (i - 2) >> 2, i < 2 ? 2 + i : (i - 2) & 3);
      epi(2 * i); epi(2 * i + 1);
      if (i < 7) pf2(i < 4 ? 64 + 8 * i : i == 4 ? 24 : 8 * (i + 1));
      if (i == 0 || i == 1) mc(1, i);
      if (i == 6 || i == 7) mc(2, i - 6);
      SB0;
    }
    __builtin_amdgcn_s_setprio(0);
    if (TYPE == 0 || third) {
#pragma unroll
      for (int i = 0; i < 8; ++i) { mm(2, i >> 2, i & 3); epi(20 + 2 * i); epi(21 + 2 * i); SB0; }
#pragma unroll
      for (int e = 36; e < 48; ++e) epi(e);
    } else {
#pragma unroll
      for (int e = 20; e < 32; ++e) epi(e);
    }
    publish<8, 2048>(ws, FH_OFF + rt * 256 + (TYPE == 0 ? j : 18 + j) * 4, tag);
  } else {
    u32x4 xp[3][2][2];
    f32x4 sr[2];
    float zs[2] = {0.f, 0.f};
    constexpr int EB[5] = {2, 0, 0, 1, 1}, ET[6] = {0, 0, 1, 0, 1, 0};
    auto ld_st = [&](int p) {
      const u32 a = L_SP + (64 * tg + 32 * (ET[p >> 2] ^ cgp) + 8 * (p & 3) + 4 * h) * 4;
      sr[p & 1] = lds_ld<f32x4>(a + 1024);
    };
    auto epi2 = [&](int p) {
      const int b = EB[p >> 2], t = ET[p >> 2], r0 = 4 * (p & 3);
      const f32x4 r = sr[p & 1];
      float v[4];
#pragma unroll
      for (int e = 0; e < 4; ++e) {
        v[e] = acc[b][t][r0 + e] * r[e];
        if (b == 0) v[e] = phi_f(v[e]);
        if (b == 2) { const float pv = phi_f(v[e]); v[e] = (n & 8) ? v[e] : pv; }
      }
      if (p + 2 < 20) ld_st(p + 2);
      if (b != 1) { zs[b >> 1] += (v[0] + v[1]) + (v[2] + v[3]); asm volatile("" : "+v"(zs[b >> 1])); }
      u32 o0 = pk_bf16(v[0], v[1]), o1 = pk_bf16(v[2], v[3]);
      asm volatile("" : "+v"(o0), "+v"(o1));
      xp[b][t][(p >> 1) & 1][2 * (p & 1)] = o0; xp[b][t][(p >> 1) & 1][2 * (p & 1) + 1] = o1;
    };
#pragma unroll
    for (int i = 0; i < 6; ++i) {
      mm(i < 2 || i >= 4 ? 2 : 0, 0, i < 4 ? i & 1 : i - 2);
      if (i < 5) fill_rd(fb, i == 0 ? 4 : i == 1 ? 0 : i == 2 ? 2 : i == 3 ? 1 : 3, 19);
      if (i == 1) { mk_fm(0); mk_fm(1); mk_fc(2); }
      if (i == 2) mk_fc(0);
      if (i == 3) { mc(2, 0); mk_fc(1); }
      if (i == 5) mc(0, 0);
      if (i >= 4) ld_st(i - 4);
      SB0;
    }
#pragma unroll
    for (int i = 0; i < 6; ++i) { mm(0, i >= 2, i < 2 ? 2 + i : i - 2); epi2(i); if (i < 3) mc(i ? 1 : 0, i == 1 ? 0 : 1); SB0; }
    if (n >= 16) {
#pragma unroll
      for (int k = 0; k < 2; ++k) lds_st<u32x4>(L_KX + ((w * 2 + k) * 32 + (n - 16) + 16 * h) * 16, xp[2][0][k]);
    }
    publish<1, 0, 5>(ws, FK_OFF + j * 256 + rt * 4, tag); SB0;
#pragma unroll
    for (int k = 0; k < 2; ++k) xp[2][1][k] = lds_ld<u32x4>(L_KX + (((w ^ 4) * 2 + k) * 32 + (n & 15) + 16 * h) * 16);
    f32x16 z16;
#pragma unroll
    for (int i = 0; i < 16; ++i) z16[i] = 0.f;
    f32x16 pmk = z16, pmm = z16, pvk = z16, pvm = z16;
    constexpr u32 IMG = L_KX + 8192, DUMP = IMG + 8 * 3840;
    const int lo8 = 8 * cgp, mix = 32 - 32 * cgp;
    u32 avk[4], amk, avm[2], amm;
    auto img_at = [&](int kc, int vc, bool on) { return (on && (kc >= 20) == (vc >= 20)) ? IMG + w * 3840 + kc * 96 + (vc >= 20 ? vc - 20 : vc) * 4 : DUMP + l * 16; };
#pragma unroll
    for (int i = 0; i < 8; ++i) {
      mm(1, i >> 2, i & 3); epi2(6 + i);
      if (i < 7) pf2(i < 4 ? 64 + 8 * i : i == 4 ? 24 : 8 * (i + 1));
      if (i >= 2 && i < 6) pmm = mfma32(xp[2][(i - 2) >> 1][i & 1], xp[2][(i - 2) >> 1][i & 1], pmm);
      if (i == 6) { amm = img_at(mix + n, mix + 4 * h, n < 8); amk = img_at(lo8 + n, mix + 4 * h, true); asm volatile("" : "+v"(amm), "+v"(amk)); }
      if (i == 7) {
#pragma unroll
        for (int q = 0; q < 2; ++q) { avm[q] = img_at(mix + n, lo8 + 16 * (1 - cgp) + 8 * q + 4 * h, n < 8); asm volatile("" : "+v"(avm[q])); }
      }
      SB0;
    }
#pragma unroll
    for (int i = 0; i < 6; ++i) {
      if (i < 4) pmk = mfma32(xp[2][i >> 1][i & 1], xp[0][i >> 1][i & 1], pmk);
      if (i >= 2 && i < 4) { pvk = mfma32(xp[1][0][i & 1], xp[0][0][i & 1], pvk); pvm = mfma32(xp[1][0][i & 1], xp[2][0][i & 1], pvm); }
      epi2(14 + i);
      if (i < 4) { avk[i] = img_at(lo8 + n, lo8 + 8 * i + 4 * h, true); asm volatile("" : "+v"(avk[i])); }
      if (i == 1) lds_st<f32x4>(amm, f32x4{pmm[4], pmm[5], pmm[6], pmm[7]});
      if (i == 4) {
        const float z0 = xsum32(zs[0]), z1 = xsum32(zs[1]);
        lds_st<float>(h == 0 ? IMG + w * 3840 + (lo8 + n) * 96 + 80 : (n & 8) ? DUMP + l * 16 : (n < 8 ? IMG + w * 3840 + (mix + n) * 96 + 80 : IMG + (w ^ 4) * 3840 + (32 - mix + n - 16) * 96 + 84), h == 0 ? z0 : z1);
      }
      if (i == 5) lds_st<f32x4>(amk, f32x4{pmk[4], pmk[5], pmk[6], pmk[7]});
      SB0;
    }
#pragma unroll
    for (int k = 0; k < 2; ++k) { pvk = mfma32(xp[1][1][k], xp[0][1][k], pvk); pvm = mfma32(xp[1][1][k], xp[2][1][k], pvm); }
    if (tid < 2) lds_st<u32x4>(L_CNT + tid * 16, u32x4{0u, 0u, 0u, 0u});
#pragma unroll
    for (int q = 0; q < 4; ++q) lds_st<f32x4>(avk[q], f32x4{pvk[4 * q], pvk[4 * q + 1], pvk[4 * q + 2], pvk[4 * q + 3]});
#pragma unroll
    for (int q = 0; q < 2; ++q) lds_st<f32x4>(avm[q], cgp ? f32x4{pvm[4 * q], pvm[4 * q + 1], pvm[4 * q + 2], pvm[4 * q + 3]} : f32x4{pvm[8 + 4 * q], pvm[9 + 4 * q], pvm[10 + 4 * q], pvm[11 + 4 * q]});
    __syncthreads();
    if (tid < 480) {
      const int cg = tid >= 240, o = (tid - 240 * cg) * 16;
      f32x4 sum = lds_ld<f32x4>(IMG + cg * 15360 + o);
#pragma unroll
      for (int k = 1; k < 4; ++k) sum += lds_ld<f32x4>(IMG + cg * 15360 + k * 3840 + o);
      bstore16(nosent4(__builtin_bit_cast(u32x4, sum)), rs, KVR_OFF + (u32)((2 * j + cg) * 8 + rt) * 3840 + o);
    }
    wait_vm<1>(); wait_lgkm0(); bar();
  }
}

DI void lds_ticket(u32 a) {
  asm volatile("s_mov_b64 exec, 1\n\tds_add_u32 %0, %1\n\ts_mov_b64 exec, -1" ::"v"(a), "v"(1u) : "memory");
}
DI void lds_wait(u32 a, u32 want) {
#pragma unroll 1
  for (int spin = 0; spin < SPIN_CAP * 16; ++spin) {
    if (__builtin_amdgcn_readfirstlane(__hip_atomic_load((LAS u32*)(size_t)a, __ATOMIC_RELAXED, __HIP_MEMORY_SCOPE_WORKGROUP)) >= want) break;
    __builtin_amdgcn_s_sleep(2);
  }
  asm volatile("" ::: "memory");
}

DI void lds_meet(u32 a, u32 want) { asm volatile("s_waitcnt lgkmcnt(0)" ::: "memory"); lds_ticket(a); lds_wait(a, want); }

DI void mjob(int pair, u32 tag, char* ws, rsrc_t rs, const u32x4 (&WO)[5][2]) {
  int tid = threadIdx.x;
  asm volatile("" : "+v"(tid));
  const int l = tid & 63, w = __builtin_amdgcn_readfirstlane(tid >> 6);
  constexpr u32 KVL = L_MJ;
  __builtin_amdgcn_s_setprio(3);
  if (w < 4) {
    wait_flags<16>(ws, FK_OFF + (pair >> 1) * 256 + (l & 7) * 4, l < 8, tag);
    const int t4 = min(tid, 239);
    u32x4 pa[8];
#pragma unroll 1
    for (int spin = 0; spin < SPIN_CAP; ++spin) {
      u32 mx = 0;
#pragma unroll
      for (int k = 0; k < 8; ++k) pa[k] = bload(rs, KVR_OFF + (u32)pair * 30720 + t4 * 16, k * 3840);
#pragma unroll
      for (int k = 0; k < 8; ++k) mx = max4(mx, pa[k]);
      if (!__any(mx == SENT)) break;
      __builtin_amdgcn_s_sleep(4);
      asm volatile("" ::: "memory");
    }
    f32x4 sum = __builtin_bit_cast(f32x4, pa[0]);
#pragma unroll
    for (int k = 1; k < 8; ++k) sum += __builtin_bit_cast(f32x4, pa[k]);
    const bool zc = t4 % 6 == 5;
    lds_st<u32x2>(KVL + t4 * 8, zc ? u32x2{0u, 0u} : u32x2{pk_bf16(sum[0], sum[1]), pk_bf16(sum[2], sum[3])});
    if (zc) { const float z = sum[0] + (((pair & 1) ? t4 < 48 : t4 >= 192) ? sum[1] : 0.f); lds_st<u16>(KVL + 1920 + (t4 / 6) * 2, (u16)pk_bf16(z, z)); }
  }
  wait_lgkm0(); bar();
  asm volatile("" ::: "memory");
  if (w >= 5) { const int e = tid - 320; bstore16(nosent4(lds_ld<u32x4>(KVL + 1920 + (e % 5) * 16)), rs, e < 160 ? ZB_OFF + (e / 5) * 1280 + pair * 80 + (e % 5) * 16 : OOB); }
  const int g = l >> 4, c = l & 15;
  const u32x4 zero4 = {0u, 0u, 0u, 0u};
  const int kx = 8 * (c >> 2) + (c & 3), ky = kx + 4, kz = 32 + (c & 7), go = min(g, 2) * 16;
  const u32x4 rx = lds_ld<u32x4>(KVL + kx * 48 + go), ry = lds_ld<u32x4>(KVL + ky * 48 + go), rz = lds_ld<u32x4>(KVL + kz * 48 + go);
  const u32x4 ax0 = (kx < 20 && g < 3) ? rx : zero4, ax1 = (kx >= 20 && g < 3) ? rx : zero4, ay0 = (ky < 20 && g < 3) ? ry : zero4, ay1 = (ky >= 20 && g < 3) ? ry : zero4, az = g < 3 ? rz : zero4;
  const u32 mo = MT_OFF + (u32)(100 * w + 5 * (pair >> 2)) * 1024 + (c + 16 * (pair & 3)) * 16;
  f32x4 dx[5], dy[5], dz[5];
#pragma unroll
  for (int cb = 0; cb < 5; ++cb) { const f32x4 z4 = {0.f, 0.f, 0.f, 0.f}; dx[cb] = mfma16(ax0, WO[cb][0], z4); dy[cb] = mfma16(ay0, WO[cb][0], z4); dz[cb] = mfma16(az, WO[cb][1], z4); }
#pragma unroll
  for (int cb = 0; cb < 5; ++cb) { dx[cb] = mfma16(ax1, WO[cb][1], dx[cb]); dy[cb] = mfma16(ay1, WO[cb][1], dy[cb]); }
  SB0;
#pragma unroll
  for (int cb = 0; cb < 5; ++cb) {
    bstore16(nosent4(u32x4{pk_bf16(dx[cb][0], dx[cb][1]), pk_bf16(dx[cb][2], dx[cb][3]), pk_bf16(dy[cb][0], dy[cb][1]), pk_bf16(dy[cb][2], dy[cb][3])}), rs, mo + g * 1024 + cb * 20480);
    bstore8(u32x2{nosent(pk_bf16(dz[cb][0], dz[cb][1])), nosent(pk_bf16(dz[cb][2], dz[cb][3]))}, rs, g < 2 ? mo + 4096 + 8 * g + cb * 20480 : OOB);
  }
  __builtin_amdgcn_s_setprio(0);
}

DI int wt_step(int i) { const int q = i < 7 ? 0 : i < 14 ? 1 : i < 20 ? 2 : 3; return i + 7 * (q + 1); }

DI void spare(int sg, int c2, u32 tag, const u16* wo, const u16* wd, char* ws, u32x4 (&WO)[5][2]) {
  int tid = threadIdx.x;
  asm volatile("" : "+v"(tid));
  const int l = tid & 63, w = __builtin_amdgcn_readfirstlane(tid >> 6);
  const int nst = sg < 10 ? 2 : 1;
  wait_vm<0>();
  bar();
#pragma unroll 1
  for (int a = 0; a < nst; ++a)
#pragma unroll
    for (int i = 0; i < 5; ++i) dma16((const char*)wd + (size_t)wt_step(sg + 16 * a) * 40960 + (w * 5 + i) * 1024, l * 16, a * 40960 + (w * 5 + i) * 1024);
#pragma unroll 1
  for (int pc = w; pc < 50; pc += 8) dma16((const char*)(wo + (size_t)sg * 40 * 640) + pc * 1024, l * 16, 81920 + pc * 1024);
  wait_vm<0>();
  __syncthreads();
  const rsrc_t rs = __builtin_amdgcn_make_buffer_rsrc(ws, 0, WS_END, 0x00020000);
#pragma unroll 1
  for (int a = 0; a < nst; ++a)
#pragma unroll
    for (int i = 0; i < 5; ++i) {
      const int cb = w * 5 + i;
      const u32x4 f = tr_frag(a * 40960 + (8 * (l >> 4) + ((l & 15) >> 2)) * 1280 + cb * 32 + 8 * (l & 3), 5120);
      bstore16(f, rs, WT_OFF + (u32)(cb * 54 + wt_step(sg + 16 * a)) * 1024 + l * 16);
    }
  {
    const u32 ra = 81920 + min(8 * (l >> 4) + ((l & 15) >> 2), 19) * 1280 + w * 160 + 8 * (l & 3), rb = 81920 + min(8 * (l >> 4) + 4 + ((l & 15) >> 2), 19) * 1280 + w * 160 + 8 * (l & 3);
    const u32 mo = MT_OFF + (u32)(100 * w + 5 * (sg >> 2)) * 1024 + ((l & 15) + 16 * (sg & 3)) * 16;
#pragma unroll
    for (int cb = 0; cb < 5; ++cb) {
#pragma unroll
      for (int hh = 0; hh < 2; ++hh) { const u32x2 lo = tr_read(ra + cb * 32 + hh * 25600), hi = tr_read(rb + cb * 32 + hh * 25600); WO[cb][hh] = u32x4{lo[0], lo[1], hi[0], hi[1]}; }
      bstore16(u32x4{SENT, SENT, SENT, SENT}, rs, mo + (l >> 4) * 1024 + cb * 20480);
      bstore16(u32x4{SENT, SENT, SENT, SENT}, rs, l < 16 ? mo + 4096 + cb * 20480 : OOB);
    }
    if (w >= 5) { const int e = tid - 320; bstore16(u32x4{SENT, SENT, SENT, SENT}, rs, e < 160 ? ZB_OFF + (e / 5) * 1280 + sg * 80 + (e % 5) * 16 : OOB); }
  }
  publish<8, 256>(ws, FW_OFF + sg * 4, tag);
#pragma unroll 1
  for (int pc = w; pc < 140; pc += 8) p2_piece((const char*)(wd + 80 * c2), pc, l);
  if (tid < 2) lds_st<u32x4>(L_CNT + tid * 16, u32x4{0u, 0u, 0u, 0u});
  if (sg == 0) {
#pragma unroll 1
    for (int k = 0; k < 4; ++k) wait_flags<8>(ws, STG_OFF + (k * 512 + tid) * 8 + 4, true, tag);
  }
  wait_vm<0>();
  __syncthreads();
  if (sg == 0 && tid == 0) __hip_atomic_store((GAS u32*)(ws + EP_OFF), tag, RLX_AGENT);
}

DI u32 p2_attblk(int u, int j) { return j < 14 ? L_ATT + (u * 14 + j) * 1024 : u * 25600 + 20480; }
constexpr int h_step(int t) { return t < 6 ? 5 + t : t < 11 ? t - 6 : t; }
constexpr bool h_lds(int t) { return t < 2 || (t >= 6 && t < 11); }
constexpr int a_first(int t) { return t < 0 ? 0 : t < 2 ? 4 + t : t < 6 ? 2 + 2 * t : 14; }
constexpr int lds_seq(int t, int cb) { return (t < 2 ? t : t - 4) * 5 + cb; }
template <int T> DI void p2_hsteps(f32x4 (&acc)[5][4], u32x4 (&A)[14][4], u32x4 (&B)[4][5], u32x4 (&bl)[3], rsrc_t rs, u32 va, const u32 (&so)[4], u32 vb, u32 wl, u32 wl2, bool extra, Poll& pl, const u16* xp, u32x2 (&xr)[5]) {
  if constexpr (T < 14) {
#pragma unroll
    for (int cb = 0; cb < 5; ++cb) {
      const u32x4 bc = h_lds(T) ? bl[lds_seq(T, cb) % 3] : B[T < 6 ? T - 2 : T - 11][cb];
      const int q = h_lds(T) ? lds_seq(T, cb) + 2 : (T == 5 && cb >= 3) ? 7 + cb : -1;
      if (q >= 0 && q < 35 && !(T == 1 && q >= 10)) bl[q % 3] = tr_frag((q < 10 ? wl2 + (q / 5) * 5120 : wl + (q / 5 - 2) * 5120) + (q % 5) * 32, 1280);
#pragma unroll
      for (int t = 0; t < 4; ++t) acc[cb][t] = mfma16(bc, A[T][t], acc[cb][t]);
      SB0;
    }
    if constexpr (T == 0) poll_done(pl);
    if constexpr (T == 9) {
#pragma unroll
      for (int cb = 0; cb < 5; ++cb) xr[cb] = *(const u32x2*)(xp + 16 * cb);
    }
    if constexpr (T == 1) lds_ticket(L_CNT + 4);
    if constexpr (T == 10) lds_ticket(L_CNT);
#pragma unroll
    for (int n = a_first(T); n < a_first(T + 1); ++n)
#pragma unroll
      for (int t = 0; t < 4; ++t) A[n][t] = bload(rs, n == 13 ? (extra ? va : OOB) : va, so[t] + h_step(n) * 1024);
    if constexpr (T >= 6 && T < 9) {
#pragma unroll
      for (int cb = 0; cb < 5; ++cb) B[T - 6][cb] = bload(rs, T == 8 ? (extra ? vb : OOB) : vb, cb * 55296 + (T + 5) * 1024);
    }
    SB0;
    p2_hsteps<T + 1>(acc, A, B, bl, rs, va, so, vb, wl, wl2, extra, pl, xp, xr);
  }
}

template <bool JOB> DI void phase2(int r, int c, int job, bool late, u32 tag, const u16* x, const u16* wd, u16* out, char* ws, const u32x4 (&WO)[5][2]) {
  int tid = threadIdx.x;
  asm volatile("" : "+v"(tid), "+s"(ws), "+s"(tag));
  const int l = tid & 63, w = __builtin_amdgcn_readfirstlane(tid >> 6);
  const rsrc_t rs = __builtin_amdgcn_make_buffer_rsrc(ws, 0, WS_END, 0x00020000);
  const int g = l >> 4, n = l & 15;
  const u32 fh = FH_OFF + c * 2048 + (r >> 2) * 256;
  const int start = p2_start(w & 3);
  const u32 vb = WT_OFF + (u32)(5 * c * 54 + start) * 1024u + l * 16;
  u32x4 B[4][5];
  asm volatile("s_cmp_lg_u32 %0, 0\n\ts_cbranch_scc1 .Lnp%=\n\ts_setprio 3\n.Lnp%=:" ::"s"(w) : "scc");
  if (w < 4) {
    if (late) {
#pragma unroll 1
      for (int pc = max(100 + 10 * w, 116); pc < 110 + 10 * w; ++pc) p2_piece((const char*)(wd + 80 * c), pc, l);
    }
    wait_flags<24>(ws, FW_OFF + c * 256 + n * 4, l < 16, tag);
#pragma unroll
    for (int s = 0; s < 4; ++s) {
#pragma unroll
      for (int cb = 0; cb < 5; ++cb) B[s][cb] = bload(rs, vb, cb * 55296 + (7 + s) * 1024);
      SB0;
    }
  }
  if (JOB) mjob(job, tag, ws, rs, WO);
  if (w >= 4) {
    const int u = w - 4;
    int ta = threadIdx.x;
    asm volatile("" : "+v"(ta));
    const int l = ta & 63, g = l >> 4, n = l & 15;
    u32x4 P[4][5];
    auto load_p = [&]() {
#pragma unroll
      for (int t = 0; t < 4; ++t) {
#pragma unroll
        for (int s = 0; s < 5; ++s) P[t][s] = bload(rs, ACT_OFF + (u32)(4 * r * NSTEP + 54 + 5 * u) * 1024u + l * 16, ((t + u) & 3) * (NSTEP * 1024) + s * 1024);
        SB0;
      }
    };
    __builtin_amdgcn_s_setprio(2);
    if (u == 0) { wait_flags<8>(ws, l < 16 ? FW_OFF + c * 256 + l * 4 : fh + (18 + (l & 3)) * 4, l < 20, tag); lds_ticket(L_CNT + 20); } else lds_wait(L_CNT + 20, 1);
    load_p();
    __builtin_amdgcn_s_setprio(0);
    f32x4 acc[5][4];
    const u32 mhint = MT_OFF + (u32)((5 * c + 4) * 20 + 5 * u + 4) * 1024u + g * 256 + 252;
    float rd[4][2];
    u32 mh;
    {
      u32x4 Z[5];
#pragma unroll 1
      for (int spin = 0; spin < SPIN_CAP; ++spin) {
        u32 mz = 0;
#pragma unroll
        for (int s = 0; s < 5; ++s) Z[s] = bload(rs, ZB_OFF + r * 1280 + 320 * u + 80 * g, s * 16);
        mh = __builtin_amdgcn_raw_buffer_load_b32(rs, mhint, 0, 16);
#pragma unroll
        for (int s = 0; s < 5; ++s) mz = max4(mz, Z[s]);
        if (!__any(mz == SENT)) break;
        __builtin_amdgcn_s_sleep(10);
        asm volatile("" ::: "memory");
      }
      const bool m0 = n == 4 * g, m1 = n == 4 * g + 1;
#pragma unroll
      for (int s = 0; s < 5; ++s)
#pragma unroll
        for (int q = 0; q < 4; ++q) Z[s][q] = ((8 * s + 2 * q >= 20) ? m1 : m0) ? Z[s][q] : 0u;
#pragma unroll
      for (int t = 0; t < 4; ++t) {
        f32x4 den = {0.f, 0.f, 0.f, 0.f};
#pragma unroll
        for (int s = 0; s < 5; ++s) den = mfma16(Z[s], P[t][s], den);
        rd[t][0] = __builtin_amdgcn_rcpf(den[0] + 1e-6f); rd[t][1] = __builtin_amdgcn_rcpf(den[1] + 1e-6f);
      }
    }
    if (__any(mh == SENT)) wait_data<4>(rs, mhint);
    u32x4 M[5][5];
    const u32 vm = MT_OFF + (u32)(5 * c * 20 + 5 * u) * 1024u + l * 16;
    auto load_m = [&](int c0, int c1) {
#pragma unroll
      for (int cb = 0; cb < 5; ++cb) {
        if (cb < c0 || cb >= c1) continue;
#pragma unroll
        for (int s = 0; s < 5; ++s) M[cb][s] = bload(rs, vm, (cb * 20 + s) * 1024);
      }
    };
    __builtin_amdgcn_s_setprio(2);
#pragma unroll
    for (int t = 0; t < 4; ++t)
#pragma unroll
      for (int s = 0; s < 5; ++s) {
#pragma unroll
        for (int k = 3 * (5 * t + s); k < 3 * (5 * t + s + 1); ++k) if (k < 25) M[k / 5][k % 5] = bload(rs, vm, ((k / 5) * 20 + k % 5) * 1024);
#pragma unroll
        for (int q = 0; q < 4; ++q) { const float rr = rd[t][8 * s + 2 * q >= 20]; P[t][s][q] = pk_bf16(bf_lo(P[t][s][q]) * rr, bf_hi(P[t][s][q]) * rr); }
        asm volatile("" : "+v"(P[t][s]));
        SB0;
      }
    __builtin_amdgcn_s_setprio(0);
    lds_wait(L_CNT + 4, 4);
    u32 mx = 0;
#pragma unroll
    for (int s = 0; s < 5; ++s) mx = max4(mx, M[0][s]);
#pragma unroll
    for (int cb = 0; cb < 6; ++cb) {
      if (cb < 5 && __builtin_expect(__any(mx == SENT), 0)) {
#pragma unroll 1
        for (int spin = 0; spin < SPIN_CAP; ++spin) {
          asm volatile("" ::: "memory");
          load_m(cb, 5);
          mx = 0;
#pragma unroll
          for (int s = 0; s < 5; ++s) mx = max4(mx, M[cb][s]);
          if (!__any(mx == SENT)) break;
        }
      }
      mx = 0;
#pragma unroll
      for (int s = 0; s < 5; ++s) {
        if (cb < 5) {
#pragma unroll
          for (int t = 0; t < 4; ++t) acc[cb][t] = mfma16(M[cb][s], P[t][s], s == 0 ? f32x4{0.f, 0.f, 0.f, 0.f} : acc[cb][t]);
        }
        if (cb < 4 && s >= 2) { mx = max4(mx, M[cb + 1][2 * s - 4]); if (s < 4) mx = max4(mx, M[cb + 1][2 * s - 3]); }
        if (cb > 0 && s >= 1 && s < 4 && !(cb == 5 && s == 3)) lds_st<f32x4>(p2_attblk(u, cb * 3 - 4 + s) + l * 16, acc[cb - 1][s]);
        SB0;
      }
    }
    lds_wait(L_CNT, 4);
#pragma unroll
    for (int k = 0; k < 6; ++k) lds_st<f32x4>(u * 25600 + (15 + k) * 1024 + l * 16, k < 5 ? acc[k][0] : acc[4][3]);
    __syncthreads();
    if constexpr (!JOB) asm volatile(".Lfk_end_a:");
    __builtin_amdgcn_endpgm();
  } else {
    const u32 va = ACT_OFF + (u32)(4 * r * NSTEP + start) * 1024u + l * 16;
    u32 so[4];
#pragma unroll
    for (int t = 0; t < 4; ++t) so[t] = ((t + w) & 3) * (NSTEP * 1024);
    const u32 wi = (16 * (g >> 1) + 4 * (g & 1) + (n >> 2)) * 160 + 8 * (l & 3), wl = w * 25600 + wi, wl2 = L_ATT + w * 10240 + wi;
    u32x4 A[14][4], bl[3];
    bl[0] = tr_frag(wl2, 1280); bl[1] = tr_frag(wl2 + 32, 1280);
    Poll pl = {};
    if (w == 0) { poll_flags(pl, ws, fh + min(l, 17) * 4, tag); lds_ticket(L_CNT + 16); } else lds_wait(L_CNT + 16, 1);
    __builtin_amdgcn_s_setprio(1);
#pragma unroll
    for (int s = 0; s < 4; ++s) {
#pragma unroll
      for (int t = 0; t < 4; ++t) A[s][t] = bload(rs, va, so[t] + h_step(s) * 1024);
      SB0;
    }
    f32x4 acc[5][4];
#pragma unroll
    for (int cb = 0; cb < 5; ++cb)
#pragma unroll
      for (int t = 0; t < 4; ++t) acc[cb][t] = f32x4{0.f, 0.f, 0.f, 0.f};
    const u32 oo = (u32)((64 * r + 16 * w + n) * 640 + 80 * c + 4 * g);
    u32x2 xr[5];
    p2_hsteps<0>(acc, A, B, bl, rs, va, so, vb, wl, wl2, w < 2, pl, x + oo, xr);
#pragma unroll
    for (int cb = 0; cb < 5; ++cb)
#pragma unroll
      for (int t = 1; t < 4; ++t) lds_st<f32x4>(w * 25600 + (cb * 3 + (t - 1)) * 1024 + l * 16, acc[cb][t]);
#pragma unroll
    for (int cb = 0; cb < 5; ++cb) { acc[cb][0] += f32x4{bf_lo(xr[cb][0]), bf_hi(xr[cb][0]), bf_lo(xr[cb][1]), bf_hi(xr[cb][1])}; asm volatile("" : "+v"(acc[cb][0])); }
    lds_meet(L_CNT + 8, 4);
#pragma unroll
    for (int cb = 0; cb < 5; ++cb) {
#pragma unroll
      for (int o = 1; o < 4; ++o) acc[cb][0] += lds_ld<f32x4>(((w + o) & 3) * 25600 + (cb * 3 + (3 - o)) * 1024 + l * 16);
      asm volatile("" : "+v"(acc[cb][0]));
    }
    __syncthreads();
#pragma unroll
    for (int cb = 0; cb < 5; ++cb) {
      f32x4 s = acc[cb][0];
      s += lds_ld<f32x4>(w * 25600 + (15 + cb) * 1024 + l * 16);
#pragma unroll
      for (int o = 1; o < 4; ++o) s += lds_ld<f32x4>(p2_attblk((w + o) & 3, cb * 3 + (3 - o)) + l * 16);
      *(u32x2*)(out + oo + 16 * cb) = u32x2{pk_bf16(s[0], s[1]), pk_bf16(s[2], s[3])};
    }
    if (JOB && job == 0 && w == 0) {
      int lq = threadIdx.x; asm volatile("" : "+v"(lq));
      u32 mn = 0xffffffffu, mx = 0; const u32 now = (u32)__builtin_amdgcn_s_memrealtime();
#pragma unroll 1
      for (int k = 0; k < 32; ++k) { const u32 d = now - __builtin_amdgcn_raw_buffer_load_b32(rs, TS_OFF + (k * 64 + lq) * 8, 0, 16); mn = min(mn, d); mx = max(mx, d); }
      { u32 a = 0xffffffffu, b = 0;
#pragma unroll 1
      for (int o = 0; o < 64; ++o) { a = min(a, (u32)__builtin_amdgcn_readlane(mn, o)); b = max(b, (u32)__builtin_amdgcn_readlane(mx, o)); } mn = a; mx = b; }
      const u32 dl = __builtin_amdgcn_readfirstlane(min(100u, 20000u));
      while ((u32)__builtin_amdgcn_s_memrealtime() - now < dl) __builtin_amdgcn_s_sleep(1);
    }
    if constexpr (!JOB) asm volatile(".Lfk_end_h:");
    __builtin_amdgcn_endpgm();
  }
}

__host__ __device__ inline void decode(int bid, int& rt, int& ct) {
  const int xcd = bid & 7, idx = bid >> 3, cgrp = xcd >> 1, ci = idx >> 2, c2 = cgrp - 2;
  const int a = 18 + 4 * c2 + ci, b = 22 + 2 * c2 + ci, c = ci == 7 ? 30 : 16 + c2;
  const int m = ci < 4 ? a : (ci < 6 ? b : c);
  ct = cgrp < 2 ? cgrp * 8 + ci : m;
  rt = 4 * (xcd & 1) + (idx & 3) + (ct == 30 ? 8 * c2 : 0);
}

__global__ __launch_bounds__(512, 2) void fused_kernel(const u16* __restrict__ x, const u16* __restrict__ wqkv, const u16* __restrict__ wo,
                                                       const u16* __restrict__ wg, const u16* __restrict__ wu, const u16* __restrict__ wd,
                                                       u16* __restrict__ out, char* ws) {
  u64 T064;
  asm volatile("s_memrealtime %0\n\ts_waitcnt lgkmcnt(0)" : "=s"(T064));
  __builtin_amdgcn_sched_barrier(0);
  const u32 T0_ = (u32)T064;
  asm volatile("" ::"s"(x), "s"(wqkv), "s"(wo), "s"(wg), "s"(wu), "s"(wd), "s"(out), "s"(ws));
  const int bid = blockIdx.x, xcd = bid & 7, idx = bid >> 3;
  const int l = threadIdx.x & 63, w = __builtin_amdgcn_readfirstlane(threadIdx.x >> 6);
  const int r2 = xcd * 4 + (idx >> 3), c2 = idx & 7;
  u32x4 xa, xb;
  u32 ep;
  asm volatile("s_load_dword %2, %5, 0x0\n\tglobal_load_dwordx4 %0, %3, %4\n\tglobal_load_dwordx4 %1, %3, %4 offset:640"
               : "=&v"(xa), "=&v"(xb), "=&s"(ep) : "v"(min(l, 39) * 16), "s"((const char*)x + (size_t)(bid * 8 + w) * 1280), "s"(ws + EP_OFF) : "memory");
  int rt, ct;
  decode(bid, rt, ct);
  asm volatile("" : "+s"(rt), "+s"(ct));
  P1Src src;
  phase1_start(src, rt & 7, ct, x, wqkv, wg, wu);
  {
    const char* pc; u32 n0, n1;
    asm volatile("s_getpc_b64 %0\n.Lfk_beg:\n\ts_mov_b32 %1, .Lfk_end_h-.Lfk_beg\n\ts_mov_b32 %2, .Lfk_end_a-.Lfk_beg" : "=s"(pc), "=s"(n0), "=s"(n1));
    dma16((const char*)((size_t)pc & ~(size_t)15) + min((u32)(idx * 8 + w) * 512u, max(n0, n1) - 1024u), (l & 31) * 16, L_SP + (w & 3) * 1024);
  }
  asm volatile("s_waitcnt vmcnt(8) lgkmcnt(0)" : "+v"(xa), "+v"(xb), "+s"(ep)::"memory");
  const u32 tag = ep + 1u;
  {
    float s = 0.f, q = 0.f;
#pragma unroll
    for (int e = 0; e < 4; ++e) { s = dot2(xa[e], 0x3f803f80u, s); q = dot2(xa[e], xa[e], q); s = dot2(xb[e], 0x3f803f80u, s); q = dot2(xb[e], xb[e], q); }
    s = wave_sum(l < 40 ? s : 0.f); q = wave_sum(l < 40 ? q : 0.f);
    const float mean = s * (1.f / 640.f);
    const float sd = __builtin_amdgcn_sqrtf(round_bf(round_bf(fmaxf(q * (1.f / 640.f) - mean * mean, 0.f)) + 1e-5f));
    bstore8(u32x2{pk_bf16(mean, sd), tag}, __builtin_amdgcn_make_buffer_rsrc(ws, 0, WS_END, 0x00020000), l == 0 ? STG_OFF + (bid * 8 + w) * 8 : OOB);
  }
  bstore8(u32x2{T0_, tag}, __builtin_amdgcn_make_buffer_rsrc(ws, 0, WS_END, 0x00020000), l == 0 ? TS_OFF + (bid * 8 + w) * 8 : OOB);
  u32x4 WO[5][2];
  if (ct == 30) {
    spare(rt, c2, tag, wo, wd, ws, WO);
    phase2<true>(r2, c2, rt, false, tag, x, wd, out, ws, WO);
  } else {
    if (ct < 18) phase1<0>(src, rt, ct, ct, c2, tag, wd, ws);
    else if (ct < 26) phase1<2>(src, rt, ct, ct - 18, c2, tag, wd, ws);
    else phase1<1>(src, rt, ct, ct - 26, c2, tag, wd, ws);
    phase2<false>(r2, c2, -1, ct >= 18 && ct < 26, tag, x, wd, out, ws, WO);
  }
}

extern "C" void kernel_launch(void* const* d_in, const int* in_sizes, int n_in, void* d_out, int out_size, void* d_ws, size_t ws_size, hipStream_t stream) {
  (void)hipFuncSetAttribute((const void*)fused_kernel, hipFuncAttributeMaxDynamicSharedMemorySize, LDS_BYTES);
  fused_kernel<<<256, 512, LDS_BYTES, stream>>>((const u16*)d_in[0], (const u16*)d_in[1], (const u16*)d_in[2], (const u16*)d_in[4], (const u16*)d_in[5],
                                                (const u16*)d_in[6], (u16*)d_out, (char*)d_ws);
}
```

```cpp
#include <hip/hip_runtime.h>
#include <stdint.h>
#include <type_traits>

typedef unsigned short u16;
typedef unsigned int u32;
typedef unsigned long long u64;
typedef __bf16 bf16x8 __attribute__((ext_vector_type(8)));
typedef __bf16 bf16x2 __attribute__((ext_vector_type(2)));
typedef short s16x4 __attribute__((ext_vector_type(4)));
typedef float f32x2 __attribute__((ext_vector_type(2)));
typedef float f32x4 __attribute__((ext_vector_type(4)));
typedef float f32x16 __attribute__((ext_vector_type(16)));
typedef u32 u32x2 __attribute__((ext_vector_type(2)));
typedef u32 u32x4 __attribute__((ext_vector_type(4)));
typedef __amdgpu_buffer_rsrc_t rsrc_t;
#define LAS __attribute__((address_space(3)))
#define GAS __attribute__((address_space(1)))
#define DI __device__ __forceinline__
#define RLX_AGENT __ATOMIC_RELAXED, __HIP_MEMORY_SCOPE_AGENT
#define SB0 __builtin_amdgcn_sched_barrier(0)

constexpr int NSTEP = 74;
constexpr u32 ACT_OFF = 0;
constexpr u32 MT_OFF  = ACT_OFF + 128u * NSTEP * 1024u;
constexpr u32 WT_OFF  = MT_OFF + 40u * 20u * 1024u;
constexpr u32 KVR_OFF = WT_OFF + 40u * 54u * 1024u;
constexpr u32 ZB_OFF  = KVR_OFF + 16u * 8u * 3840u;
constexpr u32 STG_OFF = ZB_OFF + 32u * 1280u;
constexpr u32 CSG_OFF = STG_OFF + 2048u * 8u;
constexpr u32 FH_OFF  = CSG_OFF + 30u * 8u * 192u * 8u;
constexpr u32 FK_OFF  = FH_OFF + 64u * 256u;
constexpr u32 FW_OFF  = FK_OFF + 8u * 256u;
constexpr u32 EP_OFF  = FW_OFF + 8u * 256u;
constexpr u32 WS_END  = EP_OFF + 256u;
constexpr u32 OOB = 0x7ffffff0u;
constexpr int SPIN_CAP = 1 << 17;

constexpr int L_W = 98304, L_SP = 159744, L_ATT = 102400, L_KX = 119808, L_MJ = 143360, L_CNT = 163808, LDS_BYTES = 163840;
constexpr int XB0 = 65536, XB1 = 0, XB2 = 32768;

DI float bf_lo(u32 v) { return __uint_as_float(v << 16); }
DI float bf_hi(u32 v) { return __uint_as_float(v & 0xffff0000u); }
DI u32 pk_bf16(float a, float b) { f32x2 v = {a, b}; return __builtin_bit_cast(u32, __builtin_convertvector(v, bf16x2)); }
DI float round_bf(float a) { return bf_lo(pk_bf16(a, a)); }
DI float dot2(u32 a, u32 b, float c) { return __builtin_amdgcn_fdot2_f32_bf16(__builtin_bit_cast(bf16x2, a), __builtin_bit_cast(bf16x2, b), c, false); }
DI float xsum32(float v) { auto r = __builtin_amdgcn_permlane32_swap(__float_as_uint(v), __float_as_uint(v), false, false); return __uint_as_float(r[0]) + __uint_as_float(r[1]); }
DI float xsum16(float v) { auto r = __builtin_amdgcn_permlane16_swap(__float_as_uint(v), __float_as_uint(v), false, false); return __uint_as_float(r[0]) + __uint_as_float(r[1]); }
DI float oct_sum(float v) {
  v += __uint_as_float(__builtin_amdgcn_mov_dpp(__float_as_uint(v), 0xB1, 0xf, 0xf, true));
  v += __uint_as_float(__builtin_amdgcn_mov_dpp(__float_as_uint(v), 0x4E, 0xf, 0xf, true));
  return v + __uint_as_float(__builtin_amdgcn_mov_dpp(__float_as_uint(v), 0x141, 0xf, 0xf, true));
}
DI float wave_sum(float v) { v = oct_sum(v); v += __uint_as_float(__builtin_amdgcn_mov_dpp(__float_as_uint(v), 0x140, 0xf, 0xf, true)); return xsum32(xsum16(v)); }
DI f32x16 mfma32(u32x4 a, u32x4 b, f32x16 c) { return __builtin_amdgcn_mfma_f32_32x32x16_bf16(__builtin_bit_cast(bf16x8, a), __builtin_bit_cast(bf16x8, b), c, 0, 0, 0); }
DI f32x4 mfma16(u32x4 a, u32x4 b, f32x4 c) { return __builtin_amdgcn_mfma_f32_16x16x32_bf16(__builtin_bit_cast(bf16x8, a), __builtin_bit_cast(bf16x8, b), c, 0, 0, 0); }
DI u32x2 tr_read(u32 a) { return __builtin_bit_cast(u32x2, __builtin_amdgcn_ds_read_tr16_b64_v4i16((LAS s16x4*)(size_t)a)); }
DI u32x4 tr_frag(u32 a, int d) { const u32x2 lo = tr_read(a), hi = tr_read(a + d); return u32x4{lo[0], lo[1], hi[0], hi[1]}; }
template <class T> DI T lds_ld(u32 a) { return *(const LAS T*)(size_t)a; }
template <class T> DI void lds_st(u32 a, T v) { *(LAS T*)(size_t)a = v; }
DI void dma16(const char* sbase, u32 voff, u32 ldst) {
  asm volatile("s_mov_b32 m0, %2\n\ts_nop 0\n\tglobal_load_lds_dwordx4 %0, %1" ::"v"(voff), "s"(sbase), "s"(ldst) : "memory");
}
template <int N> DI void wait_vm() { asm volatile("s_waitcnt vmcnt(%0)" ::"n"(N) : "memory"); }
DI void wait_lgkm0() { __builtin_amdgcn_s_waitcnt(0xC07F); }
DI void bar() { __builtin_amdgcn_s_barrier(); }
DI u32x4 bload(rsrc_t r, u32 voff, u32 soff) { return __builtin_amdgcn_raw_buffer_load_b128(r, voff, soff, 16); }
DI void bstore16(u32x4 v, rsrc_t r, u32 voff) { __builtin_amdgcn_raw_buffer_store_b128(v, r, voff, 0, 16); }
DI void bstore8(u32x2 v, rsrc_t r, u32 voff) { __builtin_amdgcn_raw_buffer_store_b64(v, r, voff, 0, 16); }
constexpr u32 SENT = 0xffffffffu;
DI u32 nosent(u32 v) { return min(v, SENT - 1u); }
DI u32x4 nosent4(u32x4 v) { return u32x4{nosent(v[0]), nosent(v[1]), nosent(v[2]), nosent(v[3])}; }
DI u32 max4(u32 m, u32x4 v) { return max(max(m, max(v[0], v[1])), max(v[2], v[3])); }
constexpr float LOG2E = 1.4426950408889634f;
template <int SLEEP = 4> DI void wait_flags(const char* ws, u32 off, bool on, u32 tag) {
  GAS u32* p = (GAS u32*)(ws + off);
#pragma unroll 1
  for (int spin = 0; spin < SPIN_CAP; ++spin) {
    const u32 v = on ? __hip_atomic_load(p, RLX_AGENT) : tag;
    if (__all(v == tag)) break;
    __builtin_amdgcn_s_sleep(SLEEP);
  }
  __builtin_amdgcn_fence(__ATOMIC_ACQUIRE, "wavefront");
  asm volatile("" ::: "memory");
}
template <int SLEEP> DI void wait_data(rsrc_t rs, u32 off) {
#pragma unroll 1
  for (int spin = 0; spin < SPIN_CAP; ++spin) {
    if (!__any(__builtin_amdgcn_raw_buffer_load_b32(rs, off, 0, 16) == SENT)) break;
    __builtin_amdgcn_s_sleep(SLEEP);
    asm volatile("" ::: "memory");
  }
  asm volatile("" ::: "memory");
}
struct Poll { u32 v[2]; };
DI void poll_flags(Poll& p, const char* ws, u32 off, u32 tag) {
  u32 cnt = SPIN_CAP;
#define POLL_STEP(LD, CK) "s_sleep 4\n\tglobal_load_dword %" #LD ", %3, %4 sc1\n\ts_waitcnt vmcnt(1)\n\tv_cmp_eq_u32 vcc, %5, %" #CK "\n\ts_nop 0\n\ts_cmp_eq_u64 vcc, -1\n\ts_cbranch_scc1 .Lpd%=\n\t"
  asm volatile("global_load_dword %0, %3, %4 sc1\n"
               ".Lpl%=:\n\t" POLL_STEP(1, 0) POLL_STEP(0, 1)
               "s_sub_u32 %2, %2, 1\n\ts_cmp_lg_u32 %2, 0\n\ts_cbranch_scc1 .Lpl%=\n"
               ".Lpd%=:"
               : "=&v"(p.v[0]), "=&v"(p.v[1]), "+s"(cnt) : "v"(off), "s"(ws), "s"(tag) : "vcc", "scc", "memory");
#undef POLL_STEP
}
DI void poll_done(Poll& p) { asm volatile("" ::"v"(p.v[0]), "v"(p.v[1])); }
template <int REP> DI void raise_flag(char* ws, u32 off, u32 tag) {
  asm volatile("s_mov_b64 exec, %3\n\tglobal_store_dword %0, %1, %2 sc1\n\ts_mov_b64 exec, -1" ::"v"(off), "v"(tag), "s"(ws), "s"((1ull << REP) - 1) : "memory");
}
template <int REP = 1, int STRIDE = 0, int KEEP = 0> DI void publish(char* ws, u32 off, u32 tag) {
  wait_vm<KEEP>();
  wait_lgkm0(); bar();
  asm volatile("" ::: "memory");
  if (__builtin_amdgcn_readfirstlane(threadIdx.x) == 0) raise_flag<REP>(ws, off + threadIdx.x * STRIDE, tag);
}

struct Sched { int op[96]; int first[20]; };
constexpr Sched make_sched(int n16) {
  Sched r{}; int n = 0;
  for (int k = 0; k < 4; ++k) r.op[n++] = 100 + k;
  r.op[n++] = 0; r.op[n++] = 1; r.op[n++] = 2;
  for (int k = 0; k < 4; ++k) r.op[n++] = 104 + k;
  r.op[n++] = 3; r.op[n++] = 4;
  for (int s = 0; s < 18; ++s) {
    r.first[s + 1] = n;
    const int it = s / 2;
    if (s == 11) { r.op[n++] = 1000; r.op[n++] = 1000; }
    constexpr int FILL[15] = {5, 108, 109, 6, 7, 110, 111, 8, 9, 112, 113, 10, 11, 114, 115};
    if (s < 3) { for (int k = 0; k < 5; ++k) r.op[n++] = FILL[5 * s + k]; }
    else if (s <= 14) {
      if ((s & 1) == 0) { r.op[n++] = 7 + 3 * it; r.op[n++] = 8 + 3 * it; r.op[n++] = 100 + 4 * (it + 2) + 2; r.op[n++] = 100 + 4 * (it + 2) + 3; }
      else { r.op[n++] = 9 + 3 * it; r.op[n++] = 100 + 4 * (it + 3); r.op[n++] = 100 + 4 * (it + 3) + 1; }
    } else for (int k = 0; k < (s == 15 ? n16 / 100 : s == 16 ? n16 / 10 % 10 : n16 % 10); ++k) r.op[n++] = 1000;
  }
  r.first[19] = n;
  return r;
}
constexpr int vm_need(int n16, int t, int b) {
  const Sched sch = make_sched(n16);
  const int issued = sch.first[b + 1];
  int last = -1;
  for (int k = 0; k < issued; ++k) {
    const int op = sch.op[k];
    if (op < 100 ? (8 * op <= 12 * t + 11) : (op < 1000 && (op - 100) / 4 <= t / 2)) last = k;
  }
  return issued - 1 - last;
}

struct Frag { u32x4 x[2][2]; u32x4 w[3][2]; };

DI int p2_start(int i) { return i * 13 + min(i, 2); }
DI int p2_blkstep(int blk) { const int i = blk < 20 ? blk / 5 : (blk - 20) >> 1; return p2_start(i) + (blk < 20 ? blk - 5 * i : 5 + (blk & 1)); }
DI u32 p2_voff(int p5, int l) { const int n = p5 * 64 + l, r = n / 10, c = n - 10 * r; return (u32)((r & 19) | ((r & 4) << 1) | ((r & 8) >> 1)) * 1280u + c * 16; }
DI void p2_piece(const char* wdc, int piece, int l) {
  const int blk = piece / 5;
  dma16(wdc + (size_t)p2_blkstep(blk) * 40960, p2_voff(piece - 5 * blk, l), piece * 1024);
}

template <int TYPE> DI f32x16 p1_mfma(u32x4 w, u32x4 x, f32x16 c) { return TYPE == 2 ? mfma32(x, w, c) : mfma32(w, x, c); }

template <int TYPE, class F> DI void p1_step(f32x16 (&acc)[3][2], const Frag& cur, bool third, F&& fill) {
#pragma unroll
  for (int i = 0; i < 8; ++i) {
    const int b = (i >> 1) & 1, k = i >> 2, t = i & 1;
    acc[b][t] = p1_mfma<TYPE>(cur.w[b][k], cur.x[t][k], acc[b][t]);
    fill(i);
    SB0;
  }
  if (TYPE != 1 || third) {
#pragma unroll
    for (int i = 8; i < 12; ++i) {
      const int k = (i >> 1) & 1, t = i & 1;
      if (TYPE == 2 && t == 1) continue;
      acc[2][t] = p1_mfma<TYPE>(cur.w[2][k], cur.x[t][k], acc[2][t]);
      SB0;
    }
  }
}
template <int I, int N, class F> DI void static_for(F&& f) { if constexpr (I < N) { f(std::integral_constant<int, I>{}); static_for<I + 1, N>(f); } }
constexpr int xslot(int i) { return i % 3 == 0 ? XB0 : (i % 3 == 1 ? XB1 : XB2); }

DI float exp2_le1(float t) { return __builtin_amdgcn_fmed3f(__builtin_amdgcn_exp2f(t), 0.f, 1.f); }
DI float phi_f(float t) { return fmaxf(t, 1e-6f) + exp2_le1(t * LOG2E); }

struct P1Src { const char* xg; const char* wb[3]; u32 xvo, wvo[3]; int k0; };
DI void phase1_start(P1Src& p, int rt, int ct, const u16* x, const u16* wqkv, const u16* wg, const u16* wu) {
  const int l = threadIdx.x & 63, w = __builtin_amdgcn_readfirstlane(threadIdx.x >> 6);
  p.k0 = (0x96418530u >> (4 * rt)) & 15;
  p.xg = (const char*)x + (size_t)rt * 256 * 1280;
  p.xvo = (u32)(8 * w + (l >> 3)) * 1280 + (((l & 7) ^ ((4 * (w & 1) + (l >> 4)) & 7)) * 16);
#pragma unroll
  for (int xi = 0; xi < 4; ++xi) dma16(p.xg + (xi * 64 * 1280 + p.k0 * 128), p.xvo, XB0 + xi * 8192 + w * 1024);
  const bool gu = ct < 18, kv = ct < 26;
  const u32 stride = gu ? 3456 : 3840, lim0 = (gu || !kv) ? 12 : 10, lim1 = gu ? 12 : kv ? 10 : 8;
  const char* b0 = (gu ? (const char*)wg : (const char*)wqkv) + (gu ? 192 * ct : kv ? 160 * ct - 1600 : 320 * min(ct, 29) - 8320);
  const char* b1 = gu ? (const char*)wu + 192 * ct : b0 + (kv ? 1280 : 192);
#pragma unroll
  for (int i = 0; i < 3; ++i) {
    const int pp = 8 * i + w, st = pp >= 12, pi = pp - 12 * st, part = pi >= 6;
    const int m = (pi - 6 * part) * 64 + l, row = m / 12, cp = m - 12 * row;
    const u32 ch = part ? cp ^ 2 : cp;
    p.wb[i] = part ? b1 : b0;
    p.wvo[i] = (u32)(st * 32 + row) * stride + (ch >= (part ? lim1 : lim0) ? ch - 4 : ch) * 16;
    dma16(p.wb[i] + (size_t)((u32)p.k0 * 64u * stride), p.wvo[i], L_W + (8 * i + w) * 1024);
  }
}

template <int TYPE>
DI void phase1(const P1Src& src, int rt, int ct, int j, int c2, u32 tag, const u16* wd, char* ws) {
  const int tid = threadIdx.x, l = tid & 63, w = __builtin_amdgcn_readfirstlane(tid >> 6);
  const int tg = w & 3, cgp = w >> 2, h = l >> 5, n = l & 31;
  auto dma_x = [&](int kk, int xi, u32 slot) { dma16(src.xg + (xi * 64 * 1280 + kk * 128), src.xvo, slot + xi * 8192 + w * 1024); };
  constexpr u32 WPER = 64 * (TYPE == 0 ? 3456 : 3840);
  const u32 wl0 = L_W + w * 1024, wl1 = w >= 4 ? wl0 - 61440 : wl0;
  auto dma_w = [&](int kk, int grp) { const int pos = (8 * grp) % 60; dma16(src.wb[grp % 3] + (size_t)((u32)kk * WPER), src.wvo[grp % 3], (pos == 56 ? wl1 : wl0) + pos * 1024); };
  auto rot = [&](int kk) { return kk + 1 >= 10 ? kk - 9 : kk + 1; };
  const int k1 = rot(src.k0), k2 = rot(k1);

  auto bphys = [&](int b) { int v = b + tg; v = v >= 3 ? v - 3 : v; return v >= 3 ? v - 3 : v; };
  u32 xa[2][4], wa[3][2];
  {
    const int ksw = TYPE == 2 ? (tg & 1) : 0;
#pragma unroll
    for (int t = 0; t < 2; ++t) {
      const int row = 64 * tg + 32 * (TYPE == 2 ? t ^ cgp : t) + n;
#pragma unroll
      for (int k = 0; k < 4; ++k) xa[t][k] = row * 128 + 16 * ((2 * (k ^ ksw) + h) ^ ((row >> 1) & 7));
    }
    const int blk = (l >> 4) & 1, q = (l & 15) >> 2, p = l & 3, rowb = L_W + (8 * h + q) * 192;
    if (TYPE == 2) {
      const int a = cgp ? 3 : 0;
      const u32 b0 = rowb + (a + blk) * 32 + 8 * p, b1 = 6144 + rowb + ((a + blk) ^ 1) * 32 + 8 * p;
      const u32 b2 = rowb + (p >> 1) * 6144 + ((4 + (cgp ^ blk)) ^ (p & 2)) * 16 + 8 * (p & 1);
#pragma unroll
      for (int k = 0; k < 2; ++k) { const u32 o = (k ^ ksw) * 3072; wa[0][k] = b0 + o; wa[1][k] = b1 + o; wa[2][k] = b2 + o; }
    } else {
#pragma unroll
      for (int b = 0; b < 3; ++b) {
        const u32 in32 = 16 * (p & 1) + 8 * (p >> 1);
        if (TYPE == 0) wa[b][0] = rowb + blk * 6144 + ((3 * cgp + bphys(b)) ^ blk) * 32 + in32;
        else wa[b][0] = rowb + cgp * 6144 + ((2 * (cgp ? (b == 2 ? 2 : b ^ (tg & 1)) : bphys(b)) + blk) ^ cgp) * 32 + in32;
        wa[b][1] = 0;
      }
    }
  }
  auto rd_w = [&](int b, int k, int off) { return tr_frag((TYPE == 2 ? wa[b][k] : wa[b][0] + k * 3072) + off, 768); };
  auto rd_x = [&](int t, int k4, int off) {
    return TYPE == 2 ? lds_ld<u32x4>(xa[t][k4] + off) : lds_ld<u32x4>(xa[0][k4] + off + t * 4096);
  };
  auto fill_rd = [&](Frag& f, int i, int st) {
    const int wo_ = (st % 5) * 12288, xo = xslot(st / 2), k4 = 2 * (st & 1);
    if (i == 0) { f.w[0][0] = rd_w(0, 0, wo_); f.x[0][0] = rd_x(0, k4, xo); }
    if (i == 1) { f.x[1][0] = rd_x(1, k4, xo); f.w[1][0] = rd_w(1, 0, wo_); }
    if (i == 2) { f.w[0][1] = rd_w(0, 1, wo_); f.x[0][1] = rd_x(0, k4 + 1, xo); }
    if (i == 3) { f.x[1][1] = rd_x(1, k4 + 1, xo); f.w[1][1] = rd_w(1, 1, wo_); }
    if (i == 4) { f.w[2][0] = rd_w(2, 0, wo_); f.w[2][1] = rd_w(2, 1, wo_); }
  };
  f32x16 acc[3][2];
#pragma unroll
  for (int b = 0; b < 3; ++b)
#pragma unroll
    for (int t = 0; t < 2; ++t)
#pragma unroll
      for (int i = 0; i < 16; ++i) acc[b][t][i] = 0.f;
  auto colid = [&](int b) {
    if (TYPE == 2) return b == 2 ? ((n >> 3) & 1) * 96 + 32 + 8 * (cgp ^ (n >> 4)) + (n & 7) : b * 96 + (cgp ? 48 : 0) + n;
    const int p = (n & 15) >> 2, c16 = 8 * (p & 1) + 4 * (p >> 1) + (n & 3);
    if (TYPE == 0) return (n >> 4) * 96 + (3 * cgp + bphys(b)) * 16 + c16;
    return cgp * 96 + (cgp ? (b == 2 ? 2 : b ^ (tg & 1)) : bphys(b)) * 32 + (n & 16) + c16;
  };
  u32 goff[2], pvo[5], coff;
  {
    const int hi = (TYPE == 1 && tid >= 128) ? tid - 128 : tid;
    goff[0] = CSG_OFF + ct * 12288 + (tid & 7) * 1536 + (tid >> 3) * 16;
    goff[1] = w < 4 ? CSG_OFF + ct * 12288 + (tid & 7) * 1536 + 1024 + (hi >> 3) * 16 : STG_OFF + 2048 * rt + (tid & 127) * 16;
    coff = l < 32 ? CSG_OFF + (ct * 1536 + rt * 192 + colid(0)) * 8 : OOB;
#pragma unroll
    for (int k = 0; k < 5; ++k) { pvo[k] = p2_voff((w + k) % 5, l); asm volatile("" : "+v"(pvo[k])); }
    asm volatile("" : "+v"(goff[0]), "+v"(goff[1]), "+v"(coff));
  }
#pragma unroll
  for (int xi = 0; xi < 4; ++xi) dma_x(k1, xi, XB1);
  dma_w(k1, 3); dma_w(k1, 4);
  Frag fa, fb;
  wait_vm<8>();
  bar(); SB0;
#pragma unroll
  for (int i = 0; i < 5; ++i) fill_rd(fa, i, 0);
  SB0;
  SB0;
  const bool third = cgp == 0;
  float cs[3] = {0.f, 0.f, 0.f};
  auto colsum = [&](const Frag& f, int i) {
    constexpr u32 ones = 0x3f803f80u;
    if (TYPE == 2) {
      if (i < 6) { const int b = i >> 1; cs[b] = dot2(f.w[b][0][2 * (i & 1)], ones, cs[b]); cs[b] = dot2(f.w[b][0][2 * (i & 1) + 1], ones, cs[b]); asm volatile("" : "+v"(cs[b])); }
    } else { cs[0] = dot2(f.w[0][i >> 2][i & 3], ones, cs[0]); asm volatile("" : "+v"(cs[0])); }
  };
  const rsrc_t rs = __builtin_amdgcn_make_buffer_rsrc(ws, 0, WS_END, 0x00020000);
  int ka = k2, kb = rot(k2);
  const char* wdc = (const char*)(wd + 80 * c2);
  u32x4 gr[2];
  f32x2 gs[2];
  auto gload = [&]() {
#pragma unroll
    for (int k = 0; k < 2; ++k) asm volatile("global_load_dwordx4 %0, %1, %2 sc1" : "=v"(gr[k]) : "v"(goff[k]), "s"(ws) : "memory");
  };
  auto gcheck = [&]() {
    asm volatile("" : "+v"(gr[0]), "+v"(gr[1]));
    return __all(gr[0][1] == tag && gr[0][3] == tag && gr[1][1] == tag && gr[1][3] == tag);
  };
  auto pf2 = [&](int base) {
    constexpr int NP = TYPE == 2 ? 116 : 140;
    const bool clip = base + 8 > NP;
    const int piece = base + (clip ? w & 3 : w), blk = piece / 5;
    u32 vo = pvo[base % 5];
    if (clip) { u32 v1 = pvo[(base + 1) % 5]; asm volatile("" : "+v"(v1)); vo = w < 4 ? vo : v1; }
    const int lo = blk / 5, hi = (blk - 20) >> 1;
    const int st = base + 8 <= 100 ? p2_start(lo) + blk - 5 * lo : base >= 100 ? p2_start(hi) + 5 + (blk & 1) : (w < 4 ? 45 : 5);
    dma16(wdc + (u32)st * 40960u, vo, piece * 1024);
  };
  constexpr int NX = TYPE == 2 ? 432 : 332;
  auto bp_of = [&](int b) { return TYPE == 0 ? bphys(b) : (cgp ? (b == 2 ? 2 : b ^ (tg & 1)) : bphys(b)); };
  auto oaddr = [&](int k) {
    const u32 dst = ACT_OFF + (u32)(16 * rt + 4 * tg + (n >> 4)) * (NSTEP * 1024) + (n & 15) * 16;
    if (TYPE == 0) { const int id = 12 * j + 6 * cgp + 2 * bp_of(k >> 1) + h; return dst + (k & 1) * 2 * NSTEP * 1024 + (id >> 2) * 1024 + (id & 3) * 256; }
    const int ch = 4 * (3 * cgp + bp_of(k >> 2)) + 2 * (k & 1) + h, g4 = ch / 5;
    return (k >= 8 && cgp) ? OOB : dst + ((k >> 1) & 1) * 2 * NSTEP * 1024 + (54 + 5 * j + ch - 5 * g4) * 1024 + g4 * 256;
  };
  static_for<0, 18>([&](auto SC) {
    constexpr int S = decltype(SC)::value, IT = S / 2;
    Frag& cur = (S & 1) ? fb : fa; Frag& nxt = (S & 1) ? fa : fb;
    wait_lgkm0();
    if (S > 0) wait_vm<vm_need(NX, S + 1, S)>();
    bar(); SB0;
    p1_step<TYPE>(acc, cur, third, [&](int i) {
      fill_rd(nxt, i, S + 1);
      if (S == 0) { if (i == 3) dma_w(k1, 5); if (i == 4 || i == 5) dma_x(k2, i - 4, XB2); if (i >= 6) dma_w(k2, i); }
      else if (S == 1) { if (i == 3 || i == 4) dma_x(k2, i - 1, XB2); if (i == 5) dma_w(k2, 8); if (i == 6) dma_w(kb, 9); if (i == 7) { dma_x(kb, 0, XB0); ka = kb; kb = rot(kb); } }
      else if (S == 2) { if (i == 3) dma_x(ka, 1, XB0); if (i == 4 || i == 5) dma_w(ka, 6 + i); if (i >= 6) dma_x(ka, i - 4, XB0); }
      else if (S > 14) { if (i >= (S == 17 ? 6 : 5)) pf2(S == 15 ? 8 * (i - 5) : S == 16 ? 96 + 8 * (i - 5) : 8 * (i - 2)); }
      else if ((S & 1) == 0) { if (i == 4 || i == 5) dma_w(ka, 3 + 3 * IT + i); if (i >= 6) dma_x(ka, i - 4, xslot(IT + 2)); }
      else { if (i == 5) dma_w(kb, 9 + 3 * IT); if (i >= 6) dma_x(kb, i - 6, xslot(IT)); if (i == 7) { ka = kb; kb = rot(kb); } }
      if (S < 2) colsum(cur, i);
      if ((S == 2 || S == 3) && i == 5 && (rt & 5) == 4) {
#pragma unroll
        for (int e = 0; e < 8; ++e) colsum(cur, e);
      }
      if (TYPE != 2) {
        if (S == 4 && i == 1) { cs[0] = xsum32(cs[0]); asm volatile("" : "+v"(cs[0])); }
        if (S == 4 && i == 3) bstore8(u32x2{__float_as_uint(cs[0]), tag}, rs, coff);
      } else {
        if (S == 4 && i >= 1 && i < 4) { const float v = xsum32(cs[i - 1]); lds_st<float>(tg < 2 ? L_SP + (tg * 192 + colid(i - 1)) * 4 : L_SP + 2048 + l * 4, v); }
        if (S == 5 && i == 1) { cs[0] = lds_ld<float>(L_SP + min(tid, 191) * 4); cs[1] = lds_ld<float>(L_SP + 768 + min(tid, 191) * 4); }
        if (S == 5 && i == 3) bstore8(u32x2{__float_as_uint(cs[0] + cs[1]), tag}, rs, tid < 192 ? CSG_OFF + (ct * 1536 + rt * 192 + tid) * 8 : OOB);
      }
      if (S == 11 && i == 0) gload();
      if (TYPE == 2 && S == 15 && i == 1 && tid < 480) bstore16(u32x4{SENT, SENT, SENT, SENT}, rs, KVR_OFF + (u32)((2 * j + (tid >= 240)) * 8 + rt) * 3840 + (tid >= 240 ? tid - 240 : tid) * 16);
      if (S == 16) {
        if (i == 1) {
          wait_vm<TYPE == 2 ? 18 : 17>();
          if (__builtin_expect(!gcheck(), 0)) {
#pragma unroll 1
            for (int spin = 0; spin < SPIN_CAP; ++spin) { __builtin_amdgcn_s_sleep(2); gload(); wait_vm<0>(); if (gcheck()) break; }
          }
        }
        if (i >= 2 && i < 6) { gs[(i - 2) >> 1][i & 1] = oct_sum(__uint_as_float(gr[(i - 2) >> 1][2 * (i & 1)])); asm volatile("" : "+v"(gs[(i - 2) >> 1][i & 1])); }
        if (i == 6) lds_st<f32x2>(L_SP + 2048 + (tid >> 3) * 8, gs[0]);
      }
      if (S == 17) {
        if (i == 0) lds_st<f32x2>(w < 4 ? L_SP + 2560 + (tid >> 3) * 8 : L_SP + (tid & 127) * 8, w < 4 ? gs[1] : f32x2{bf_lo(gr[1][0]), bf_lo(gr[1][2])});
        if (i == 2) lds_st<f32x2>((w < 4 ? L_SP + 3072 : L_SP + 1024) + (tid & 63) * 8 + (w & 1) * (w < 4 ? 0 : 512), f32x2{__builtin_amdgcn_rcpf(bf_hi(gr[1][0])), __builtin_amdgcn_rcpf(bf_hi(gr[1][2]))});
      }
    });
  });
  auto mm = [&](int b, int t, int q) { const Frag& f = q < 2 ? fa : fb; acc[b][t] = p1_mfma<TYPE>(f.w[b][q & 1], f.x[t][q & 1], acc[b][t]); };
  u32x4 fm[2], fc[3];
  auto mk_fm = [&](int t) { const u32 m = h ? 0u : (__float_as_uint(lds_ld<float>(L_SP + (64 * tg + 32 * (TYPE == 2 ? t ^ cgp : t) + n) * 4)) >> 16) ^ 0x8000u; fm[t] = u32x4{m * 0x10001u, m, 0u, 0u}; };
  auto mk_fc = [&](int b) {
    const float c = h ? 0.f : lds_ld<float>(L_SP + 2048 + colid(b) * 4), c0 = round_bf(c), c1 = round_bf(c - c0);
    fc[b] = u32x4{pk_bf16(c0, c1), pk_bf16(c - c0 - c1, 0.f), 0u, 0u};
  };
  auto mc = [&](int b, int t) { acc[b][t] = p1_mfma<TYPE>(fc[b], fm[t], acc[b][t]); };
  wait_lgkm0(); wait_vm<vm_need(NX, 19, 18)>(); bar(); SB0;
  if constexpr (TYPE != 2) {
    float rstd[2], ce[2], sd[2];
    u32 ob[8];
    auto epi = [&](int e) {
      const int b = e >> 4, t = (e >> 3) & 1, i = e & 7;
      if (TYPE == 0) {
        const float ex = __builtin_amdgcn_exp2f(acc[b][t][i] * ce[t]);
        const float hv = acc[b][t][8 + i] * __builtin_amdgcn_rcpf(ex * sd[t] + sd[t]);
        if (i & 1) ob[i >> 1] = pk_bf16(__uint_as_float(ob[i >> 1]), hv); else ob[i >> 1] = __float_as_uint(hv);
        asm volatile("" : "+v"(ob[i >> 1]));
        if (i == 7) bstore16(u32x4{ob[0], ob[1], ob[2], ob[3]}, rs, oaddr(2 * b + t));
      } else {
        const float u0 = acc[b][t][2 * i], u1 = acc[b][t][2 * i + 1];
        ob[i] = pk_bf16(fmaxf(u0, sd[t]) * rstd[t] + exp2_le1(u0 * ce[t]), fmaxf(u1, sd[t]) * rstd[t] + exp2_le1(u1 * ce[t]));
        asm volatile("" : "+v"(ob[i]));
        if ((i & 3) == 3) bstore16(u32x4{ob[i - 3], ob[i - 2], ob[i - 1], ob[i]}, rs, oaddr(4 * b + 2 * t + (i >> 2)));
      }
    };
#pragma unroll
    for (int i = 0; i < 6; ++i) {
      mm(0, i < 4 ? i >> 1 : 0, i < 4 ? i & 1 : i - 2);
      if (i < 5) fill_rd(fb, i == 1 ? 2 : i == 2 ? 1 : i, 19);
      if (i == 0) {
#pragma unroll
        for (int t = 0; t < 2; ++t) rstd[t] = lds_ld<float>(L_SP + 1024 + (64 * tg + 32 * t + n) * 4);
      }
      if (i == 1) { mk_fm(0); mk_fm(1); mk_fc(0); }
      if (i == 2) mk_fc(1);
      if (i == 3) { mc(0, 0); mk_fc(2); }
      if (i == 3) {
#pragma unroll
        for (int t = 0; t < 2; ++t) { ce[t] = (TYPE == 0 ? -LOG2E : LOG2E) * rstd[t]; sd[t] = (TYPE == 0 ? 1.f : 1e-6f) * __builtin_amdgcn_rcpf(rstd[t]); }
      }
      if (i >= 3) pf2(96 + 8 * i);
      if (i == 5) { mc(0, 1); if (tid < 2) lds_st<u32x4>(L_CNT + tid * 16, u32x4{0u, 0u, 0u, 0u}); }
      SB0;
    }
    wait_lgkm0(); bar(); SB0;
    if (TYPE == 0 && cgp) __builtin_amdgcn_s_setprio(1);
#pragma unroll
    for (int i = 0; i < 10; ++i) {
      mm(i < 2 ? 0 : 1, i < 2 ? 1 : (i - 2) >> 2, i < 2 ? 2 + i : (i - 2) & 3);
      epi(2 * i); epi(2 * i + 1);
      if (i < 7) pf2(i < 4 ? 64 + 8 * i : i == 4 ? 24 : 8 * (i + 1));
      if (i == 0 || i == 1) mc(1, i);
      if (i == 6 || i == 7) mc(2, i - 6);
      SB0;
    }
    __builtin_amdgcn_s_setprio(0);
    if (TYPE == 0 || third) {
#pragma unroll
      for (int i = 0; i < 8; ++i) { mm(2, i >> 2, i & 3); epi(20 + 2 * i); epi(21 + 2 * i); SB0; }
#pragma unroll
      for (int e = 36; e < 48; ++e) epi(e);
    } else {
#pragma unroll
      for (int e = 20; e < 32; ++e) epi(e);
    }
    publish<8, 2048>(ws, FH_OFF + rt * 256 + (TYPE == 0 ? j : 18 + j) * 4, tag);
  } else {
    u32x4 xp[3][2][2];
    f32x4 sr[2];
    float zs[2] = {0.f, 0.f};
    constexpr int EB[5] = {2, 0, 0, 1, 1}, ET[6] = {0, 0, 1, 0, 1, 0};
    auto ld_st = [&](int p) {
      const u32 a = L_SP + (64 * tg + 32 * (ET[p >> 2] ^ cgp) + 8 * (p & 3) + 4 * h) * 4;
      sr[p & 1] = lds_ld<f32x4>(a + 1024);
    };
    auto epi2 = [&](int p) {
      const int b = EB[p >> 2], t = ET[p >> 2], r0 = 4 * (p & 3);
      const f32x4 r = sr[p & 1];
      float v[4];
#pragma unroll
      for (int e = 0; e < 4; ++e) {
        v[e] = acc[b][t][r0 + e] * r[e];
        if (b == 0) v[e] = phi_f(v[e]);
        if (b == 2) { const float pv = phi_f(v[e]); v[e] = (n & 8) ? v[e] : pv; }
      }
      if (p + 2 < 20) ld_st(p + 2);
      if (b != 1) { zs[b >> 1] += (v[0] + v[1]) + (v[2] + v[3]); asm volatile("" : "+v"(zs[b >> 1])); }
      u32 o0 = pk_bf16(v[0], v[1]), o1 = pk_bf16(v[2], v[3]);
      asm volatile("" : "+v"(o0), "+v"(o1));
      xp[b][t][(p >> 1) & 1][2 * (p & 1)] = o0; xp[b][t][(p >> 1) & 1][2 * (p & 1) + 1] = o1;
    };
#pragma unroll
    for (int i = 0; i < 6; ++i) {
      mm(i < 2 || i >= 4 ? 2 : 0, 0, i < 4 ? i & 1 : i - 2);
      if (i < 5) fill_rd(fb, i == 0 ? 4 : i == 1 ? 0 : i == 2 ? 2 : i == 3 ? 1 : 3, 19);
      if (i == 1) { mk_fm(0); mk_fm(1); mk_fc(2); }
      if (i == 2) mk_fc(0);
      if (i == 3) { mc(2, 0); mk_fc(1); }
      if (i == 5) mc(0, 0);
      if (i >= 4) ld_st(i - 4);
      SB0;
    }
#pragma unroll
    for (int i = 0; i < 6; ++i) { mm(0, i >= 2, i < 2 ? 2 + i : i - 2); epi2(i); if (i < 3) mc(i ? 1 : 0, i == 1 ? 0 : 1); SB0; }
    if (n >= 16) {
#pragma unroll
      for (int k = 0; k < 2; ++k) lds_st<u32x4>(L_KX + ((w * 2 + k) * 32 + (n - 16) + 16 * h) * 16, xp[2][0][k]);
    }
    publish<1, 0, 5>(ws, FK_OFF + j * 256 + rt * 4, tag); SB0;
#pragma unroll
    for (int k = 0; k < 2; ++k) xp[2][1][k] = lds_ld<u32x4>(L_KX + (((w ^ 4) * 2 + k) * 32 + (n & 15) + 16 * h) * 16);
    f32x16 z16;
#pragma unroll
    for (int i = 0; i < 16; ++i) z16[i] = 0.f;
    f32x16 pmk = z16, pmm = z16, pvk = z16, pvm = z16;
    constexpr u32 IMG = L_KX + 8192, DUMP = IMG + 8 * 3840;
    const int lo8 = 8 * cgp, mix = 32 - 32 * cgp;
    u32 avk[4], amk, avm[2], amm;
    auto img_at = [&](int kc, int vc, bool on) { return (on && (kc >= 20) == (vc >= 20)) ? IMG + w * 3840 + kc * 96 + (vc >= 20 ? vc - 20 : vc) * 4 : DUMP + l * 16; };
#pragma unroll
    for (int i = 0; i < 8; ++i) {
      mm(1, i >> 2, i & 3); epi2(6 + i);
      if (i < 7) pf2(i < 4 ? 64 + 8 * i : i == 4 ? 24 : 8 * (i + 1));
      if (i >= 2 && i < 6) pmm = mfma32(xp[2][(i - 2) >> 1][i & 1], xp[2][(i - 2) >> 1][i & 1], pmm);
      if (i == 6) { amm = img_at(mix + n, mix + 4 * h, n < 8); amk = img_at(lo8 + n, mix + 4 * h, true); asm volatile("" : "+v"(amm), "+v"(amk)); }
      if (i == 7) {
#pragma unroll
        for (int q = 0; q < 2; ++q) { avm[q] = img_at(mix + n, lo8 + 16 * (1 - cgp) + 8 * q + 4 * h, n < 8); asm volatile("" : "+v"(avm[q])); }
      }
      SB0;
    }
#pragma unroll
    for (int i = 0; i < 6; ++i) {
      if (i < 4) pmk = mfma32(xp[2][i >> 1][i & 1], xp[0][i >> 1][i & 1], pmk);
      if (i >= 2 && i < 4) { pvk = mfma32(xp[1][0][i & 1], xp[0][0][i & 1], pvk); pvm = mfma32(xp[1][0][i & 1], xp[2][0][i & 1], pvm); }
      epi2(14 + i);
      if (i < 4) { avk[i] = img_at(lo8 + n, lo8 + 8 * i + 4 * h, true); asm volatile("" : "+v"(avk[i])); }
      if (i == 1) lds_st<f32x4>(amm, f32x4{pmm[4], pmm[5], pmm[6], pmm[7]});
      if (i == 4) {
        const float z0 = xsum32(zs[0]), z1 = xsum32(zs[1]);
        lds_st<float>(h == 0 ? IMG + w * 3840 + (lo8 + n) * 96 + 80 : (n & 8) ? DUMP + l * 16 : (n < 8 ? IMG + w * 3840 + (mix + n) * 96 + 80 : IMG + (w ^ 4) * 3840 + (32 - mix + n - 16) * 96 + 84), h == 0 ? z0 : z1);
      }
      if (i == 5) lds_st<f32x4>(amk, f32x4{pmk[4], pmk[5], pmk[6], pmk[7]});
      SB0;
    }
#pragma unroll
    for (int k = 0; k < 2; ++k) { pvk = mfma32(xp[1][1][k], xp[0][1][k], pvk); pvm = mfma32(xp[1][1][k], xp[2][1][k], pvm); }
    if (tid < 2) lds_st<u32x4>(L_CNT + tid * 16, u32x4{0u, 0u, 0u, 0u});
#pragma unroll
    for (int q = 0; q < 4; ++q) lds_st<f32x4>(avk[q], f32x4{pvk[4 * q], pvk[4 * q + 1], pvk[4 * q + 2], pvk[4 * q + 3]});
#pragma unroll
    for (int q = 0; q < 2; ++q) lds_st<f32x4>(avm[q], cgp ? f32x4{pvm[4 * q], pvm[4 * q + 1], pvm[4 * q + 2], pvm[4 * q + 3]} : f32x4{pvm[8 + 4 * q], pvm[9 + 4 * q], pvm[10 + 4 * q], pvm[11 + 4 * q]});
    __syncthreads();
    if (tid < 480) {
      const int cg = tid >= 240, o = (tid - 240 * cg) * 16;
      f32x4 sum = lds_ld<f32x4>(IMG + cg * 15360 + o);
#pragma unroll
      for (int k = 1; k < 4; ++k) sum += lds_ld<f32x4>(IMG + cg * 15360 + k * 3840 + o);
      bstore16(nosent4(__builtin_bit_cast(u32x4, sum)), rs, KVR_OFF + (u32)((2 * j + cg) * 8 + rt) * 3840 + o);
    }
    wait_vm<1>(); wait_lgkm0(); bar();
  }
}

DI void lds_ticket(u32 a) {
  asm volatile("s_mov_b64 exec, 1\n\tds_add_u32 %0, %1\n\ts_mov_b64 exec, -1" ::"v"(a), "v"(1u) : "memory");
}
DI void lds_wait(u32 a, u32 want) {
#pragma unroll 1
  for (int spin = 0; spin < SPIN_CAP * 16; ++spin) {
    if (__builtin_amdgcn_readfirstlane(__hip_atomic_load((LAS u32*)(size_t)a, __ATOMIC_RELAXED, __HIP_MEMORY_SCOPE_WORKGROUP)) >= want) break;
    __builtin_amdgcn_s_sleep(2);
  }
  asm volatile("" ::: "memory");
}

DI void lds_meet(u32 a, u32 want) { asm volatile("s_waitcnt lgkmcnt(0)" ::: "memory"); lds_ticket(a); lds_wait(a, want); }

DI void mjob(int pair, u32 tag, char* ws, rsrc_t rs, const u32x4 (&WO)[5][2]) {
  int tid = threadIdx.x;
  asm volatile("" : "+v"(tid));
  const int l = tid & 63, w = __builtin_amdgcn_readfirstlane(tid >> 6);
  constexpr u32 KVL = L_MJ;
  __builtin_amdgcn_s_setprio(3);
  if (w < 4) {
    wait_flags<16>(ws, FK_OFF + (pair >> 1) * 256 + (l & 7) * 4, l < 8, tag);
    const int t4 = min(tid, 239);
    u32x4 pa[8];
#pragma unroll 1
    for (int spin = 0; spin < SPIN_CAP; ++spin) {
      u32 mx = 0;
#pragma unroll
      for (int k = 0; k < 8; ++k) pa[k] = bload(rs, KVR_OFF + (u32)pair * 30720 + t4 * 16, k * 3840);
#pragma unroll
      for (int k = 0; k < 8; ++k) mx = max4(mx, pa[k]);
      if (!__any(mx == SENT)) break;
      __builtin_amdgcn_s_sleep(4);
      asm volatile("" ::: "memory");
    }
    f32x4 sum = __builtin_bit_cast(f32x4, pa[0]);
#pragma unroll
    for (int k = 1; k < 8; ++k) sum += __builtin_bit_cast(f32x4, pa[k]);
    const bool zc = t4 % 6 == 5;
    lds_st<u32x2>(KVL + t4 * 8, zc ? u32x2{0u, 0u} : u32x2{pk_bf16(sum[0], sum[1]), pk_bf16(sum[2], sum[3])});
    if (zc) { const float z = sum[0] + (((pair & 1) ? t4 < 48 : t4 >= 192) ? sum[1] : 0.f); lds_st<u16>(KVL + 1920 + (t4 / 6) * 2, (u16)pk_bf16(z, z)); }
  }
  wait_lgkm0(); bar();
  asm volatile("" ::: "memory");
  if (w >= 5) { const int e = tid - 320; bstore16(nosent4(lds_ld<u32x4>(KVL + 1920 + (e % 5) * 16)), rs, e < 160 ? ZB_OFF + (e / 5) * 1280 + pair * 80 + (e % 5) * 16 : OOB); }
  const int g = l >> 4, c = l & 15;
  const u32x4 zero4 = {0u, 0u, 0u, 0u};
  const int kx = 8 * (c >> 2) + (c & 3), ky = kx + 4, kz = 32 + (c & 7), go = min(g, 2) * 16;
  const u32x4 rx = lds_ld<u32x4>(KVL + kx * 48 + go), ry = lds_ld<u32x4>(KVL + ky * 48 + go), rz = lds_ld<u32x4>(KVL + kz * 48 + go);
  const u32x4 ax0 = (kx < 20 && g < 3) ? rx : zero4, ax1 = (kx >= 20 && g < 3) ? rx : zero4, ay0 = (ky < 20 && g < 3) ? ry : zero4, ay1 = (ky >= 20 && g < 3) ? ry : zero4, az = g < 3 ? rz : zero4;
  const u32 mo = MT_OFF + (u32)(100 * w + 5 * (pair >> 2)) * 1024 + (c + 16 * (pair & 3)) * 16;
  f32x4 dx[5], dy[5], dz[5];
#pragma unroll
  for (int cb = 0; cb < 5; ++cb) { const f32x4 z4 = {0.f, 0.f, 0.f, 0.f}; dx[cb] = mfma16(ax0, WO[cb][0], z4); dy[cb] = mfma16(ay0, WO[cb][0], z4); dz[cb] = mfma16(az, WO[cb][1], z4); }
#pragma unroll
  for (int cb = 0; cb < 5; ++cb) { dx[cb] = mfma16(ax1, WO[cb][1], dx[cb]); dy[cb] = mfma16(ay1, WO[cb][1], dy[cb]); }
  SB0;
#pragma unroll
  for (int cb = 0; cb < 5; ++cb) {
    bstore16(nosent4(u32x4{pk_bf16(dx[cb][0], dx[cb][1]), pk_bf16(dx[cb][2], dx[cb][3]), pk_bf16(dy[cb][0], dy[cb][1]), pk_bf16(dy[cb][2], dy[cb][3])}), rs, mo + g * 1024 + cb * 20480);
    bstore8(u32x2{nosent(pk_bf16(dz[cb][0], dz[cb][1])), nosent(pk_bf16(dz[cb][2], dz[cb][3]))}, rs, g < 2 ? mo + 4096 + 8 * g + cb * 20480 : OOB);
  }
  __builtin_amdgcn_s_setprio(0);
}

DI int wt_step(int i) { const int q = i < 7 ? 0 : i < 14 ? 1 : i < 20 ? 2 : 3; return i + 7 * (q + 1); }

DI void spare(int sg, int c2, u32 tag, const u16* wo, const u16* wd, char* ws, u32x4 (&WO)[5][2]) {
  int tid = threadIdx.x;
  asm volatile("" : "+v"(tid));
  const int l = tid & 63, w = __builtin_amdgcn_readfirstlane(tid >> 6);
  const int nst = sg < 10 ? 2 : 1;
  wait_vm<0>();
  bar();
#pragma unroll 1
  for (int a = 0; a < nst; ++a)
#pragma unroll
    for (int i = 0; i < 5; ++i) dma16((const char*)wd + (size_t)wt_step(sg + 16 * a) * 40960 + (w * 5 + i) * 1024, l * 16, a * 40960 + (w * 5 + i) * 1024);
#pragma unroll 1
  for (int pc = w; pc < 50; pc += 8) dma16((const char*)(wo + (size_t)sg * 40 * 640) + pc * 1024, l * 16, 81920 + pc * 1024);
  wait_vm<0>();
  __syncthreads();
  const rsrc_t rs = __builtin_amdgcn_make_buffer_rsrc(ws, 0, WS_END, 0x00020000);
#pragma unroll 1
  for (int a = 0; a < nst; ++a)
#pragma unroll
    for (int i = 0; i < 5; ++i) {
      const int cb = w * 5 + i;
      const u32x4 f = tr_frag(a * 40960 + (8 * (l >> 4) + ((l & 15) >> 2)) * 1280 + cb * 32 + 8 * (l & 3), 5120);
      bstore16(f, rs, WT_OFF + (u32)(cb * 54 + wt_step(sg + 16 * a)) * 1024 + l * 16);
    }
  {
    const u32 ra = 81920 + min(8 * (l >> 4) + ((l & 15) >> 2), 19) * 1280 + w * 160 + 8 * (l & 3), rb = 81920 + min(8 * (l >> 4) + 4 + ((l & 15) >> 2), 19) * 1280 + w * 160 + 8 * (l & 3);
    const u32 mo = MT_OFF + (u32)(100 * w + 5 * (sg >> 2)) * 1024 + ((l & 15) + 16 * (sg & 3)) * 16;
#pragma unroll
    for (int cb = 0; cb < 5; ++cb) {
#pragma unroll
      for (int hh = 0; hh < 2; ++hh) { const u32x2 lo = tr_read(ra + cb * 32 + hh * 25600), hi = tr_read(rb + cb * 32 + hh * 25600); WO[cb][hh] = u32x4{lo[0], lo[1], hi[0], hi[1]}; }
      bstore16(u32x4{SENT, SENT, SENT, SENT}, rs, mo + (l >> 4) * 1024 + cb * 20480);
      bstore16(u32x4{SENT, SENT, SENT, SENT}, rs, l < 16 ? mo + 4096 + cb * 20480 : OOB);
    }
    if (w >= 5) { const int e = tid - 320; bstore16(u32x4{SENT, SENT, SENT, SENT}, rs, e < 160 ? ZB_OFF + (e / 5) * 1280 + sg * 80 + (e % 5) * 16 : OOB); }
  }
  publish<8, 256>(ws, FW_OFF + sg * 4, tag);
#pragma unroll 1
  for (int pc = w; pc < 140; pc += 8) p2_piece((const char*)(wd + 80 * c2), pc, l);
  if (tid < 2) lds_st<u32x4>(L_CNT + tid * 16, u32x4{0u, 0u, 0u, 0u});
  if (sg == 0) {
#pragma unroll 1
    for (int k = 0; k < 4; ++k) wait_flags<8>(ws, STG_OFF + (k * 512 + tid) * 8 + 4, true, tag);
  }
  wait_vm<0>();
  __syncthreads();
  if (sg == 0 && tid == 0) __hip_atomic_store((GAS u32*)(ws + EP_OFF), tag, RLX_AGENT);
}

DI u32 p2_attblk(int u, int j) { return j < 14 ? L_ATT + (u * 14 + j) * 1024 : u * 25600 + 20480; }
constexpr int h_step(int t) { return t < 6 ? 5 + t : t < 11 ? t - 6 : t; }
constexpr bool h_lds(int t) { return t < 2 || (t >= 6 && t < 11); }
constexpr int a_first(int t) { return t < 0 ? 0 : t < 2 ? 4 + t : t < 6 ? 2 + 2 * t : 14; }
constexpr int lds_seq(int t, int cb) { return (t < 2 ? t : t - 4) * 5 + cb; }
template <int T> DI void p2_hsteps(f32x4 (&acc)[5][4], u32x4 (&A)[14][4], u32x4 (&B)[4][5], u32x4 (&bl)[3], rsrc_t rs, u32 va, const u32 (&so)[4], u32 vb, u32 wl, u32 wl2, bool extra, Poll& pl, const u16* xp, u32x2 (&xr)[5]) {
  if constexpr (T < 14) {
#pragma unroll
    for (int cb = 0; cb < 5; ++cb) {
      const u32x4 bc = h_lds(T) ? bl[lds_seq(T, cb) % 3] : B[T < 6 ? T - 2 : T - 11][cb];
      const int q = h_lds(T) ? lds_seq(T, cb) + 2 : (T == 5 && cb >= 3) ? 7 + cb : -1;
      if (q >= 0 && q < 35 && !(T == 1 && q >= 10)) bl[q % 3] = tr_frag((q < 10 ? wl2 + (q / 5) * 5120 : wl + (q / 5 - 2) * 5120) + (q % 5) * 32, 1280);
#pragma unroll
      for (int t = 0; t < 4; ++t) acc[cb][t] = mfma16(bc, A[T][t], acc[cb][t]);
      SB0;
    }
    if constexpr (T == 0) poll_done(pl);
    if constexpr (T == 9) {
#pragma unroll
      for (int cb = 0; cb < 5; ++cb) xr[cb] = *(const u32x2*)(xp + 16 * cb);
    }
    if constexpr (T == 1) lds_ticket(L_CNT + 4);
    if constexpr (T == 10) lds_ticket(L_CNT);
#pragma unroll
    for (int n = a_first(T); n < a_first(T + 1); ++n)
#pragma unroll
      for (int t = 0; t < 4; ++t) A[n][t] = bload(rs, n == 13 ? (extra ? va : OOB) : va, so[t] + h_step(n) * 1024);
    if constexpr (T >= 6 && T < 9) {
#pragma unroll
      for (int cb = 0; cb < 5; ++cb) B[T - 6][cb] = bload(rs, T == 8 ? (extra ? vb : OOB) : vb, cb * 55296 + (T + 5) * 1024);
    }
    SB0;
    p2_hsteps<T + 1>(acc, A, B, bl, rs, va, so, vb, wl, wl2, extra, pl, xp, xr);
  }
}

template <bool JOB> DI void phase2(int r, int c, int job, bool late, u32 tag, const u16* x, const u16* wd, u16* out, char* ws, const u32x4 (&WO)[5][2]) {
  int tid = threadIdx.x;
  asm volatile("" : "+v"(tid), "+s"(ws), "+s"(tag));
  const int l = tid & 63, w = __builtin_amdgcn_readfirstlane(tid >> 6);
  const rsrc_t rs = __builtin_amdgcn_make_buffer_rsrc(ws, 0, WS_END, 0x00020000);
  const int g = l >> 4, n = l & 15;
  const u32 fh = FH_OFF + c * 2048 + (r >> 2) * 256;
  const int start = p2_start(w & 3);
  const u32 vb = WT_OFF + (u32)(5 * c * 54 + start) * 1024u + l * 16;
  u32x4 B[4][5];
  asm volatile("s_cmp_lg_u32 %0, 0\n\ts_cbranch_scc1 .Lnp%=\n\ts_setprio 3\n.Lnp%=:" ::"s"(w) : "scc");
  if (w < 4) {
    if (late) {
#pragma unroll 1
      for (int pc = max(100 + 10 * w, 116); pc < 110 + 10 * w; ++pc) p2_piece((const char*)(wd + 80 * c), pc, l);
    }
    wait_flags<24>(ws, FW_OFF + c * 256 + n * 4, l < 16, tag);
#pragma unroll
    for (int s = 0; s < 4; ++s) {
#pragma unroll
      for (int cb = 0; cb < 5; ++cb) B[s][cb] = bload(rs, vb, cb * 55296 + (7 + s) * 1024);
      SB0;
    }
  }
  if (JOB) mjob(job, tag, ws, rs, WO);
  if (w >= 4) {
    const int u = w - 4;
    int ta = threadIdx.x;
    asm volatile("" : "+v"(ta));
    const int l = ta & 63, g = l >> 4, n = l & 15;
    u32x4 P[4][5];
    auto load_p = [&]() {
#pragma unroll
      for (int t = 0; t < 4; ++t) {
#pragma unroll
        for (int s = 0; s < 5; ++s) P[t][s] = bload(rs, ACT_OFF + (u32)(4 * r * NSTEP + 54 + 5 * u) * 1024u + l * 16, ((t + u) & 3) * (NSTEP * 1024) + s * 1024);
        SB0;
      }
    };
    __builtin_amdgcn_s_setprio(2);
    if (u == 0) { wait_flags<8>(ws, l < 16 ? FW_OFF + c * 256 + l * 4 : fh + (18 + (l & 3)) * 4, l < 20, tag); lds_ticket(L_CNT + 20); } else lds_wait(L_CNT + 20, 1);
    load_p();
    __builtin_amdgcn_s_setprio(0);
    f32x4 acc[5][4];
    const u32 mhint = MT_OFF + (u32)((5 * c + 4) * 20 + 5 * u + 4) * 1024u + g * 256 + 252;
    float rd[4][2];
    u32 mh;
    {
      u32x4 Z[5];
#pragma unroll 1
      for (int spin = 0; spin < SPIN_CAP; ++spin) {
        u32 mz = 0;
#pragma unroll
        for (int s = 0; s < 5; ++s) Z[s] = bload(rs, ZB_OFF + r * 1280 + 320 * u + 80 * g, s * 16);
        mh = __builtin_amdgcn_raw_buffer_load_b32(rs, mhint, 0, 16);
#pragma unroll
        for (int s = 0; s < 5; ++s) mz = max4(mz, Z[s]);
        if (!__any(mz == SENT)) break;
        __builtin_amdgcn_s_sleep(10);
        asm volatile("" ::: "memory");
      }
      const bool m0 = n == 4 * g, m1 = n == 4 * g + 1;
#pragma unroll
      for (int s = 0; s < 5; ++s)
#pragma unroll
        for (int q = 0; q < 4; ++q) Z[s][q] = ((8 * s + 2 * q >= 20) ? m1 : m0) ? Z[s][q] : 0u;
#pragma unroll
      for (int t = 0; t < 4; ++t) {
        f32x4 den = {0.f, 0.f, 0.f, 0.f};
#pragma unroll
        for (int s = 0; s < 5; ++s) den = mfma16(Z[s], P[t][s], den);
        rd[t][0] = __builtin_amdgcn_rcpf(den[0] + 1e-6f); rd[t][1] = __builtin_amdgcn_rcpf(den[1] + 1e-6f);
      }
    }
    if (__any(mh == SENT)) wait_data<4>(rs, mhint);
    u32x4 M[5][5];
    const u32 vm = MT_OFF + (u32)(5 * c * 20 + 5 * u) * 1024u + l * 16;
    auto load_m = [&](int c0, int c1) {
#pragma unroll
      for (int cb = 0; cb < 5; ++cb) {
        if (cb < c0 || cb >= c1) continue;
#pragma unroll
        for (int s = 0; s < 5; ++s) M[cb][s] = bload(rs, vm, (cb * 20 + s) * 1024);
      }
    };
    __builtin_amdgcn_s_setprio(2);
#pragma unroll
    for (int t = 0; t < 4; ++t)
#pragma unroll
      for (int s = 0; s < 5; ++s) {
#pragma unroll
        for (int k = 3 * (5 * t + s); k < 3 * (5 * t + s + 1); ++k) if (k < 25) M[k / 5][k % 5] = bload(rs, vm, ((k / 5) * 20 + k % 5) * 1024);
#pragma unroll
        for (int q = 0; q < 4; ++q) { const float rr = rd[t][8 * s + 2 * q >= 20]; P[t][s][q] = pk_bf16(bf_lo(P[t][s][q]) * rr, bf_hi(P[t][s][q]) * rr); }
        asm volatile("" : "+v"(P[t][s]));
        SB0;
      }
    __builtin_amdgcn_s_setprio(0);
    lds_wait(L_CNT + 4, 4);
    u32 mx = 0;
#pragma unroll
    for (int s = 0; s < 5; ++s) mx = max4(mx, M[0][s]);
#pragma unroll
    for (int cb = 0; cb < 6; ++cb) {
      if (cb < 5 && __builtin_expect(__any(mx == SENT), 0)) {
#pragma unroll 1
        for (int spin = 0; spin < SPIN_CAP; ++spin) {
          asm volatile("" ::: "memory");
          load_m(cb, 5);
          mx = 0;
#pragma unroll
          for (int s = 0; s < 5; ++s) mx = max4(mx, M[cb][s]);
          if (!__any(mx == SENT)) break;
        }
      }
      mx = 0;
#pragma unroll
      for (int s = 0; s < 5; ++s) {
        if (cb < 5) {
#pragma unroll
          for (int t = 0; t < 4; ++t) acc[cb][t] = mfma16(M[cb][s], P[t][s], s == 0 ? f32x4{0.f, 0.f, 0.f, 0.f} : acc[cb][t]);
        }
        if (cb < 4 && s >= 2) { mx = max4(mx, M[cb + 1][2 * s - 4]); if (s < 4) mx = max4(mx, M[cb + 1][2 * s - 3]); }
        if (cb > 0 && s >= 1 && s < 4 && !(cb == 5 && s == 3)) lds_st<f32x4>(p2_attblk(u, cb * 3 - 4 + s) + l * 16, acc[cb - 1][s]);
        SB0;
      }
    }
    lds_wait(L_CNT, 4);
#pragma unroll
    for (int k = 0; k < 6; ++k) lds_st<f32x4>(u * 25600 + (15 + k) * 1024 + l * 16, k < 5 ? acc[k][0] : acc[4][3]);
    __syncthreads();
    if constexpr (!JOB) asm volatile(".Lfk_end_a:");
    __builtin_amdgcn_endpgm();
  } else {
    const u32 va = ACT_OFF + (u32)(4 * r * NSTEP + start) * 1024u + l * 16;
    u32 so[4];
#pragma unroll
    for (int t = 0; t < 4; ++t) so[t] = ((t + w) & 3) * (NSTEP * 1024);
    const u32 wi = (16 * (g >> 1) + 4 * (g & 1) + (n >> 2)) * 160 + 8 * (l & 3), wl = w * 25600 + wi, wl2 = L_ATT + w * 10240 + wi;
    u32x4 A[14][4], bl[3];
    bl[0] = tr_frag(wl2, 1280); bl[1] = tr_frag(wl2 + 32, 1280);
    Poll pl = {};
    if (w == 0) { poll_flags(pl, ws, fh + min(l, 17) * 4, tag); lds_ticket(L_CNT + 16); } else lds_wait(L_CNT + 16, 1);
    __builtin_amdgcn_s_setprio(1);
#pragma unroll
    for (int s = 0; s < 4; ++s) {
#pragma unroll
      for (int t = 0; t < 4; ++t) A[s][t] = bload(rs, va, so[t] + h_step(s) * 1024);
      SB0;
    }
    f32x4 acc[5][4];
#pragma unroll
    for (int cb = 0; cb < 5; ++cb)
#pragma unroll
      for (int t = 0; t < 4; ++t) acc[cb][t] = f32x4{0.f, 0.f, 0.f, 0.f};
    const u32 oo = (u32)((64 * r + 16 * w + n) * 640 + 80 * c + 4 * g);
    u32x2 xr[5];
    p2_hsteps<0>(acc, A, B, bl, rs, va, so, vb, wl, wl2, w < 2, pl, x + oo, xr);
#pragma unroll
    for (int cb = 0; cb < 5; ++cb)
#pragma unroll
      for (int t = 1; t < 4; ++t) lds_st<f32x4>(w * 25600 + (cb * 3 + (t - 1)) * 1024 + l * 16, acc[cb][t]);
#pragma unroll
    for (int cb = 0; cb < 5; ++cb) { acc[cb][0] += f32x4{bf_lo(xr[cb][0]), bf_hi(xr[cb][0]), bf_lo(xr[cb][1]), bf_hi(xr[cb][1])}; asm volatile("" : "+v"(acc[cb][0])); }
    lds_meet(L_CNT + 8, 4);
#pragma unroll
    for (int cb = 0; cb < 5; ++cb) {
#pragma unroll
      for (int o = 1; o < 4; ++o) acc[cb][0] += lds_ld<f32x4>(((w + o) & 3) * 25600 + (cb * 3 + (3 - o)) * 1024 + l * 16);
      asm volatile("" : "+v"(acc[cb][0]));
    }
    __syncthreads();
#pragma unroll
    for (int cb = 0; cb < 5; ++cb) {
      f32x4 s = acc[cb][0];
      s += lds_ld<f32x4>(w * 25600 + (15 + cb) * 1024 + l * 16);
#pragma unroll
      for (int o = 1; o < 4; ++o) s += lds_ld<f32x4>(p2_attblk((w + o) & 3, cb * 3 + (3 - o)) + l * 16);
      *(u32x2*)(out + oo + 16 * cb) = u32x2{pk_bf16(s[0], s[1]), pk_bf16(s[2], s[3])};
    }
    if constexpr (!JOB) asm volatile(".Lfk_end_h:");
    __builtin_amdgcn_endpgm();
  }
}

__host__ __device__ inline void decode(int bid, int& rt, int& ct) {
  const int xcd = bid & 7, idx = bid >> 3, cgrp = xcd >> 1, ci = idx >> 2, c2 = cgrp - 2;
  const int a = 18 + 4 * c2 + ci, b = 22 + 2 * c2 + ci, c = ci == 7 ? 30 : 16 + c2;
  const int m = ci < 4 ? a : (ci < 6 ? b : c);
  ct = cgrp < 2 ? cgrp * 8 + ci : m;
  rt = 4 * (xcd & 1) + (idx & 3) + (ct == 30 ? 8 * c2 : 0);
}

__global__ __launch_bounds__(512, 2) void fused_kernel(const u16* __restrict__ x, const u16* __restrict__ wqkv, const u16* __restrict__ wo,
                                                       const u16* __restrict__ wg, const u16* __restrict__ wu, const u16* __restrict__ wd,
                                                       u16* __restrict__ out, char* ws) {
  asm volatile("" ::"s"(x), "s"(wqkv), "s"(wo), "s"(wg), "s"(wu), "s"(wd), "s"(out), "s"(ws));
  const int bid = blockIdx.x, xcd = bid & 7, idx = bid >> 3;
  const int l = threadIdx.x & 63, w = __builtin_amdgcn_readfirstlane(threadIdx.x >> 6);
  const int r2 = xcd * 4 + (idx >> 3), c2 = idx & 7;
  u32x4 xa, xb;
  u32 ep;
  asm volatile("s_load_dword %2, %5, 0x0\n\tglobal_load_dwordx4 %0, %3, %4\n\tglobal_load_dwordx4 %1, %3, %4 offset:640"
               : "=&v"(xa), "=&v"(xb), "=&s"(ep) : "v"(min(l, 39) * 16), "s"((const char*)x + (size_t)(bid * 8 + w) * 1280), "s"(ws + EP_OFF) : "memory");
  int rt, ct;
  decode(bid, rt, ct);
  asm volatile("" : "+s"(rt), "+s"(ct));
  P1Src src;
  phase1_start(src, rt & 7, ct, x, wqkv, wg, wu);
  {
    const char* pc; u32 n0, n1;
    asm volatile("s_getpc_b64 %0\n.Lfk_beg:\n\ts_mov_b32 %1, .Lfk_end_h-.Lfk_beg\n\ts_mov_b32 %2, .Lfk_end_a-.Lfk_beg" : "=s"(pc), "=s"(n0), "=s"(n1));
    dma16((const char*)((size_t)pc & ~(size_t)15) + min((u32)(idx * 8 + w) * 512u, max(n0, n1) - 1024u), (l & 31) * 16, L_SP + (w & 3) * 1024);
  }
  asm volatile("s_waitcnt vmcnt(8) lgkmcnt(0)" : "+v"(xa), "+v"(xb), "+s"(ep)::"memory");
  const u32 tag = ep + 1u;
  {
    float s = 0.f, q = 0.f;
#pragma unroll
    for (int e = 0; e < 4; ++e) { s = dot2(xa[e], 0x3f803f80u, s); q = dot2(xa[e], xa[e], q); s = dot2(xb[e], 0x3f803f80u, s); q = dot2(xb[e], xb[e], q); }
    s = wave_sum(l < 40 ? s : 0.f); q = wave_sum(l < 40 ? q : 0.f);
    const float mean = s * (1.f / 640.f);
    const float sd = __builtin_amdgcn_sqrtf(round_bf(round_bf(fmaxf(q * (1.f / 640.f) - mean * mean, 0.f)) + 1e-5f));
    bstore8(u32x2{pk_bf16(mean, sd), tag}, __builtin_amdgcn_make_buffer_rsrc(ws, 0, WS_END, 0x00020000), l == 0 ? STG_OFF + (bid * 8 + w) * 8 : OOB);
  }
  u32x4 WO[5][2];
  if (ct == 30) {
    spare(rt, c2, tag, wo, wd, ws, WO);
    phase2<true>(r2, c2, rt, false, tag, x, wd, out, ws, WO);
  } else {
    if (ct < 18) phase1<0>(src, rt, ct, ct, c2, tag, wd, ws);
    else if (ct < 26) phase1<2>(src, rt, ct, ct - 18, c2, tag, wd, ws);
    else phase1<1>(src, rt, ct, ct - 26, c2, tag, wd, ws);
    phase2<false>(r2, c2, -1, ct >= 18 && ct < 26, tag, x, wd, out, ws, WO);
  }
}

extern "C" void kernel_launch(void* const* d_in, const int* in_sizes, int n_in, void* d_out, int out_size, void* d_ws, size_t ws_size, hipStream_t stream) {
  (void)hipFuncSetAttribute((const void*)fused_kernel, hipFuncAttributeMaxDynamicSharedMemorySize, LDS_BYTES);
  fused_kernel<<<256, 512, LDS_BYTES, stream>>>((const u16*)d_in[0], (const u16*)d_in[1], (const u16*)d_in[2], (const u16*)d_in[4], (const u16*)d_in[5],
                                                (const u16*)d_in[6], (u16*)d_out, (char*)d_ws);
}
```
